```python
import torch

def _softmax(x, axis=-1):
    return torch.softmax(x, dim=axis)
def _gelu(x):
    return torch.nn.functional.gelu(x, approximate="tanh")
def _silu(x):
    return torch.nn.functional.silu(x)
def _sigmoid(x):
    return torch.sigmoid(x)
def _layernorm(x, axis=-1, eps=1e-5):
    m = x.mean(dim=axis, keepdim=True)
    v = x.var(dim=axis, keepdim=True, unbiased=False)
    return (x - m) / torch.sqrt(v + eps)
def _scatter_add(tgt, idx, src, axis=0):
    return tgt.clone().index_add_(axis, idx, src)
def _segment_sum(src, seg, n):
    out = torch.zeros((n,) + src.shape[1:], dtype=src.dtype)
    return out.index_add_(0, seg, src)
def _pad(x, pw):
    flat = tuple(v for pair in reversed(pw) for v in pair)
    return torch.nn.functional.pad(x, flat)

kernel_name = 'concat_heads__gqa_block_cached__gqa_block_cached__decode'

def setup_inputs():

    _w = (lambda a: (lambda t: t.to(torch.bfloat16) if t.is_floating_point() else t)(torch.as_tensor(a)))
    def _A_setup():
        B, Sq, D = 8, 1, 512
        Hq, Hkv, Sk, Dh = 32, 8, 8192, 128
        assert Hq % Hkv == 0, (Hq, Hkv)
        return {
            "x": _w((lambda *s: torch.randn(*s, dtype=torch.bfloat16) * 0.8)(B, Sq, D)),
            "k_cache": _w((lambda *s: torch.randn(*s, dtype=torch.bfloat16) * 0.8)(B, Hkv, Sk, Dh)),
            "v_cache": _w((lambda *s: torch.randn(*s, dtype=torch.bfloat16) * 0.05)(B, Hkv, Sk, Dh)),
            "wq": _w((lambda *s: torch.randn(*s, dtype=torch.bfloat16) * 0.8)(D, Hq * Dh) * (D ** -0.5)),
            "wo": _w((lambda *s: torch.randn(*s, dtype=torch.bfloat16) * 0.05)(Hq * Dh, D) * ((Hq * Dh) ** -0.5)),
        }
    _A_in = _A_setup()

    def _B_setup():
        B, Sq, D = 8, 1, 512
        Hq, Hkv, Sk, Dh = 32, 8, 8192, 128
        assert Hq % Hkv == 0, (Hq, Hkv)
        return {
            "x": _w((lambda *s: torch.randn(*s, dtype=torch.bfloat16) * 0.8)(B, Sq, D)),
            "k_cache": _w((lambda *s: torch.randn(*s, dtype=torch.bfloat16) * 0.8)(B, Hkv, Sk, Dh)),
            "v_cache": _w((lambda *s: torch.randn(*s, dtype=torch.bfloat16) * 0.05)(B, Hkv, Sk, Dh)),
            "wq": _w((lambda *s: torch.randn(*s, dtype=torch.bfloat16) * 0.8)(D, Hq * Dh) * (D ** -0.5)),
            "wo": _w((lambda *s: torch.randn(*s, dtype=torch.bfloat16) * 0.05)(Hq * Dh, D) * ((Hq * Dh) ** -0.5)),
        }
    _B_in = _B_setup()

    return {
        "A_x": _A_in["x"],
        "A_k_cache": _A_in["k_cache"],
        "A_v_cache": _A_in["v_cache"],
        "A_wq": _A_in["wq"],
        "A_wo": _A_in["wo"],
        "B_x": _B_in["x"],
        "B_k_cache": _B_in["k_cache"],
        "B_v_cache": _B_in["v_cache"],
        "B_wq": _B_in["wq"],
        "B_wo": _B_in["wo"],
    }

def reference(A_x, A_k_cache, A_v_cache, A_wq, A_wo, B_x, B_k_cache, B_v_cache, B_wq, B_wo):
    def _A_ref(x, k_cache, v_cache, wq, wo):
        B, Sq, D = x.shape
        Hq, Dh = 32, 128
        Hkv = k_cache.shape[1]
        rep = Hq // Hkv
        q = (lambda x, *d: x.permute(*d))((x @ wq).reshape(B, Sq, Hq, Dh), 0, 2, 1, 3)
        k = (lambda x, n, axis: torch.repeat_interleave(x, n, dim=axis))(k_cache, rep, axis=1)
        v = (lambda x, n, axis: torch.repeat_interleave(x, n, dim=axis))(v_cache, rep, axis=1)
        scores = torch.einsum("bhqd,bhkd->bhqk", q, k) * (Dh ** -0.5)
        p = _softmax(scores, axis=-1)
        o = torch.einsum("bhqk,bhkd->bhqd", p, v)
        return (lambda x, *d: x.permute(*d))(o, 0, 2, 1, 3).reshape(B, Sq, Hq * Dh) @ wo

    def _B_ref(x, k_cache, v_cache, wq, wo):
        B, Sq, D = x.shape
        Hq, Dh = 32, 128
        Hkv = k_cache.shape[1]
        rep = Hq // Hkv
        q = (lambda x, *d: x.permute(*d))((x @ wq).reshape(B, Sq, Hq, Dh), 0, 2, 1, 3)
        k = (lambda x, n, axis: torch.repeat_interleave(x, n, dim=axis))(k_cache, rep, axis=1)
        v = (lambda x, n, axis: torch.repeat_interleave(x, n, dim=axis))(v_cache, rep, axis=1)
        scores = torch.einsum("bhqd,bhkd->bhqk", q, k) * (Dh ** -0.5)
        p = _softmax(scores, axis=-1)
        o = torch.einsum("bhqk,bhkd->bhqd", p, v)
        return (lambda x, *d: x.permute(*d))(o, 0, 2, 1, 3).reshape(B, Sq, Hq * Dh) @ wo

    a = _A_ref(A_x, A_k_cache, A_v_cache, A_wq, A_wo)
    b = _B_ref(B_x, B_k_cache, B_v_cache, B_wq, B_wo)
    return torch.concatenate([a, b], axis=-1)
```

```cpp
#include <hip/hip_runtime.h>
#include <hip/hip_bf16.h>
#include <stdint.h>

typedef unsigned u32;
typedef u32 v4u __attribute__((ext_vector_type(4)));
typedef u32 v2u __attribute__((ext_vector_type(2)));
typedef float f32x4 __attribute__((ext_vector_type(4)));
typedef float f32x2 __attribute__((ext_vector_type(2)));
typedef __bf16 bf16x8 __attribute__((ext_vector_type(8)));
typedef __bf16 bf16x2 __attribute__((ext_vector_type(2)));
typedef short v4s __attribute__((ext_vector_type(4)));
#define LAS __attribute__((address_space(3)))

constexpr int NB = 8, DM = 512, HQ = 32, HKV = 8, SK = 8192, DH = 128;
constexpr int NWG = 256, NTHR = 256;
constexpr int UT = SK / 16;
constexpr int KV_BYTES = NB * HKV * SK * DH * 2;
constexpr int OOB = 0x9C40000;
constexpr float CSC = 0.08838834764831845f * 1.4426950408889634f;

constexpr size_t WS_Q = 0;
constexpr size_t WS_PART = 131072;
constexpr int PART_STRIDE = 132;
constexpr size_t WS_OP = WS_PART + (size_t)NWG * 4 * PART_STRIDE * 4;

struct Params {
  const void* x[2];
  const void* k[2];
  const void* v[2];
  const void* wq[2];
  const void* wo[2];
  void* out;
  unsigned char* ws;
};

__device__ __forceinline__ f32x4 mfma16(v4u a, v4u b, f32x4 c) {
  return __builtin_amdgcn_mfma_f32_16x16x32_bf16(__builtin_bit_cast(bf16x8, a), __builtin_bit_cast(bf16x8, b), c, 0, 0, 0);
}
__device__ __forceinline__ u32 pack_bf16(float lo, float hi) {
  f32x2 f = {lo, hi};
  return __builtin_bit_cast(u32, __builtin_convertvector(f, bf16x2));
}
__device__ __forceinline__ v2u tr_read(LAS unsigned char* p) {
  return __builtin_bit_cast(v2u, __builtin_amdgcn_ds_read_tr16_b64_v4i16((LAS v4s*)p));
}

__device__ __forceinline__ float proj32(LAS unsigned char* L, const v4u (&wreg)[8], const v4u (&afrag)[4], int w, int lane, int tid) {
  LAS unsigned char* wl = L + w * 8192;
#pragma unroll
  for (int i = 0; i < 8; ++i) *(LAS v4u*)(wl + i * 1024 + lane * 16) = wreg[i];
  const int g = lane >> 4, q = (lane >> 2) & 3, p = lane & 3;
  LAS unsigned char* rb = wl + (8 * g + q) * 64 + p * 8;
  f32x4 acc[2] = {{0.f, 0.f, 0.f, 0.f}, {0.f, 0.f, 0.f, 0.f}};
#pragma unroll
  for (int s = 0; s < 4; ++s)
#pragma unroll
    for (int ct = 0; ct < 2; ++ct) {
      const v2u lo = tr_read(rb + s * 2048 + ct * 32);
      const v2u hi = tr_read(rb + s * 2048 + ct * 32 + 256);
      const v4u bfrag = {lo.x, lo.y, hi.x, hi.y};
      acc[ct] = mfma16(afrag[s], bfrag, acc[ct]);
    }
  LAS f32x4* red = (LAS f32x4*)(L + 32768);
  if (lane < 32) {
    red[(w * 2 + 0) * 32 + lane] = acc[0];
    red[(w * 2 + 1) * 32 + lane] = acc[1];
  }
  __syncthreads();
  const int l32 = tid & 31, r = (tid >> 5) & 3, ct = tid >> 7;
  LAS float* rf = (LAS float*)(L + 32768);
  float s = 0.f;
#pragma unroll
  for (int ww = 0; ww < 4; ++ww) s += rf[((ww * 2 + ct) * 32 + l32) * 4 + r];
  return s;
}

__global__ void __launch_bounds__(NTHR) k_qproj(Params p) {
  __shared__ __attribute__((aligned(16))) unsigned char smem[32768 + 4096];
  LAS unsigned char* L = (LAS unsigned char*)smem;
  const int tid = threadIdx.x, lane = tid & 63, w = __builtin_amdgcn_readfirstlane(tid >> 6);
  const int wg = blockIdx.x, br = wg >> 7, cgi = wg & 127;
  const int col0 = cgi * 32;
  const unsigned char* W = (const unsigned char*)p.wq[br];
  const unsigned char* X = (const unsigned char*)p.x[br];
  v4u wreg[8], af[4];
#pragma unroll
  for (int i = 0; i < 8; ++i)
    wreg[i] = *(const v4u*)(W + (size_t)(128 * w + 16 * i + (lane >> 2)) * (HQ * DH * 2) + col0 * 2 + (lane & 3) * 16);
#pragma unroll
  for (int s = 0; s < 4; ++s) af[s] = *(const v4u*)(X + (lane & 7) * (DM * 2) + (128 * w + 32 * s + 8 * (lane >> 4)) * 2);
  const float r = proj32(L, wreg, af, w, lane, tid);
  const int l32 = tid & 31, rr = (tid >> 5) & 3, ct = tid >> 7;
  const int b = 4 * (l32 >> 4) + rr, n = 16 * ct + (l32 & 15);
  unsigned short* q = (unsigned short*)(p.ws + WS_Q);
  q[(br * 8 + b) * 4096 + col0 + n] = (unsigned short)(pack_bf16(r, r) & 0xffffu);
}

__global__ void __launch_bounds__(NTHR) k_attn(Params p) {
  __shared__ __attribute__((aligned(16))) float smem[4 * 4 * PART_STRIDE];
  const int tid = threadIdx.x, lane = tid & 63, w = __builtin_amdgcn_readfirstlane(tid >> 6);
  const int wg = blockIdx.x, side = wg & 1, U = wg >> 1, br = U >> 6, ub = U & 63, b = ub >> 3, kvh = ub & 7;
  const __amdgpu_buffer_rsrc_t rk = __builtin_amdgcn_make_buffer_rsrc((void*)p.k[br], 0, KV_BYTES, 0x00020000);
  const __amdgpu_buffer_rsrc_t rv = __builtin_amdgcn_make_buffer_rsrc((void*)p.v[br], 0, KV_BYTES, 0x00020000);
  const int voffK = (lane & 15) * 256 + (lane >> 4) * 16;
  const int voffV = (lane >> 4) * 1024 + (lane & 15) * 16;
  const int NS = 64;
  const int start = ub * UT + (side ? UT - 1 - w : w);
  const int step = side ? -4 : 4;
  constexpr int W = 2;
  v4u kb[W][4], vb[W][4];
  auto issueK = [&](int s, int so) {
#pragma unroll
    for (int q = 0; q < 4; ++q) kb[s][q] = __builtin_amdgcn_raw_buffer_load_b128(rk, voffK + q * 64, so, 2);
  };
  auto issueV = [&](int s, int so) {
#pragma unroll
    for (int q = 0; q < 4; ++q) vb[s][q] = __builtin_amdgcn_raw_buffer_load_b128(rv, voffV + q * 256, so, 2);
  };
  v4u qf[4];
  {
    const unsigned char* q = p.ws + WS_Q + ((size_t)(br * 8 + b) * 4096 + kvh * 512 + ((lane >> 2) & 3) * 128 + 8 * (lane >> 4)) * 2;
#pragma unroll
    for (int s = 0; s < 4; ++s) qf[s] = *(const v4u*)(q + s * 64);
  }
#pragma unroll
  for (int s = 0; s < W; ++s) { issueK(s, (start + step * s) * 4096); issueV(s, (start + step * s) * 4096); }
  __builtin_amdgcn_sched_barrier(0);
  const int jq = lane & 3;
  const u32 m0 = jq == 0 ? 0x0000ffffu : (jq == 1 ? 0xffff0000u : 0u);
  const u32 m1 = jq == 2 ? 0x0000ffffu : (jq == 3 ? 0xffff0000u : 0u);
  float mraw = -INFINITY, mc = 0.f, lsum = 0.f;
  f32x4 accLo = {0.f, 0.f, 0.f, 0.f}, accHi = {0.f, 0.f, 0.f, 0.f};
  for (int i = 0; i < NS; i += W) {
#pragma unroll
    for (int s = 0; s < W; ++s) {
      const int nx = i + s + W;
      const int so = nx < NS ? (start + step * nx) * 4096 : OOB;
      f32x4 sc = {0.f, 0.f, 0.f, 0.f};
#pragma unroll
      for (int q = 0; q < 4; ++q) sc = mfma16(kb[s][q], qf[q], sc);
      __builtin_amdgcn_sched_barrier(0);
      issueK(s, so);
      __builtin_amdgcn_sched_barrier(0);
      float mx = fmaxf(fmaxf(sc[0], sc[1]), fmaxf(sc[2], sc[3]));
      if (__builtin_amdgcn_ballot_w64(mx > mraw) != 0ull) {
        mx = fmaxf(mx, __shfl_xor(mx, 16));
        mx = fmaxf(mx, __shfl_xor(mx, 32));
        const float mnew = fmaxf(mraw, mx);
        const float alpha = __builtin_amdgcn_exp2f((mraw - mnew) * CSC);
        mraw = mnew; mc = mnew * CSC;
        lsum *= alpha;
        const float ab = __shfl(alpha, (lane >> 4) * 4);
        accLo *= ab; accHi *= ab;
      }
#pragma unroll
      for (int t = 0; t < 4; ++t) {
        const float pe = __builtin_amdgcn_exp2f(fmaf(sc[t], CSC, -mc));
        lsum += pe;
        const u32 pk = pack_bf16(pe, pe);
        const u32 x0 = pk & m0, x1 = pk & m1;
        const v4u alo = {x0, x1, 0u, 0u}, ahi = {0u, 0u, x0, x1};
        accLo = mfma16(alo, vb[s][t], accLo);
        accHi = mfma16(ahi, vb[s][t], accHi);
      }
      __builtin_amdgcn_sched_barrier(0);
      issueV(s, so);
      __builtin_amdgcn_sched_barrier(0);
    }
  }
  lsum += __shfl_xor(lsum, 16);
  lsum += __shfl_xor(lsum, 32);
  {
    float* rec = smem + (w * 4 + (lane >> 4)) * PART_STRIDE + 8 * (lane & 15);
    *(f32x4*)rec = accLo;
    *(f32x4*)(rec + 4) = accHi;
    if (lane < 16 && (lane & 3) == 0) {
      float* r2 = smem + (w * 4 + (lane >> 2)) * PART_STRIDE + 128;
      r2[0] = mc; r2[1] = lsum;
    }
  }
  __syncthreads();
  {
    const int h = tid >> 6, d0 = 2 * lane;
    float M = -INFINITY;
#pragma unroll
    for (int ww = 0; ww < 4; ++ww) M = fmaxf(M, smem[(ww * 4 + h) * PART_STRIDE + 128]);
    float Ls = 0.f, o0 = 0.f, o1 = 0.f;
#pragma unroll
    for (int ww = 0; ww < 4; ++ww) {
      const float* rec = smem + (ww * 4 + h) * PART_STRIDE;
      const float sc = __builtin_amdgcn_exp2f(rec[128] - M);
      Ls = fmaf(sc, rec[129], Ls);
      o0 = fmaf(sc, rec[d0], o0);
      o1 = fmaf(sc, rec[d0 + 1], o1);
    }
    float* part = (float*)(p.ws + WS_PART) + ((size_t)wg * 4 + h) * PART_STRIDE;
    *(f32x2*)(part + d0) = f32x2{o0, o1};
    if (lane == 0) *(f32x2*)(part + 128) = f32x2{M, Ls};
  }
}

__global__ void __launch_bounds__(NTHR) k_oproj(Params p) {
  __shared__ __attribute__((aligned(16))) unsigned char smem[32768 + 4096];
  LAS unsigned char* L = (LAS unsigned char*)smem;
  const int tid = threadIdx.x, lane = tid & 63, w = __builtin_amdgcn_readfirstlane(tid >> 6);
  const int wg = blockIdx.x, br = wg >> 7, kvh = (wg >> 4) & 7, cg = wg & 15;
  const unsigned char* W = (const unsigned char*)p.wo[br];
  v4u wreg[8], af[4];
#pragma unroll
  for (int i = 0; i < 8; ++i)
    wreg[i] = *(const v4u*)(W + (size_t)(kvh * 512 + 128 * w + 16 * i + (lane >> 2)) * (DM * 2) + cg * 64 + (lane & 3) * 16);
  const int b = lane & 7, side = (lane >> 3) & 1, g = lane >> 4;
  const int src = ((br * 64 + b * 8 + kvh) << 1) | side;
  const float* part = (const float*)(p.ws + WS_PART) + ((size_t)src * 4 + w) * PART_STRIDE;
  const f32x2 ml = *(const f32x2*)(part + 128);
  const float mo = __shfl_xor(ml.x, 8), lo = __shfl_xor(ml.y, 8);
  const float M = fmaxf(ml.x, mo);
  const float sc = __builtin_amdgcn_exp2f(ml.x - M), so = __builtin_amdgcn_exp2f(mo - M);
  const float f = sc / (sc * ml.y + so * lo);
#pragma unroll
  for (int s = 0; s < 4; ++s) {
    f32x4 a0 = *(const f32x4*)(part + 32 * s + 8 * g), a1 = *(const f32x4*)(part + 32 * s + 8 * g + 4);
    a0 *= f; a1 *= f;
#pragma unroll
    for (int j = 0; j < 4; ++j) { a0[j] += __shfl_xor(a0[j], 8); a1[j] += __shfl_xor(a1[j], 8); }
    af[s] = v4u{pack_bf16(a0[0], a0[1]), pack_bf16(a0[2], a0[3]), pack_bf16(a1[0], a1[1]), pack_bf16(a1[2], a1[3])};
  }
  const float r = proj32(L, wreg, af, w, lane, tid);
  const int l32 = tid & 31, rr = (tid >> 5) & 3, ct = tid >> 7;
  const int bo = 4 * (l32 >> 4) + rr, n = 16 * ct + (l32 & 15);
  float* op = (float*)(p.ws + WS_OP);
  op[((br * 8 + kvh) * 8 + bo) * 512 + cg * 32 + n] = r;
}

__global__ void __launch_bounds__(256) k_final(Params p) {
  const int i = blockIdx.x * 256 + threadIdx.x;
  const int br = i >> 12, b = (i >> 9) & 7, n = i & 511;
  const float* op = (const float*)(p.ws + WS_OP);
  float s = 0.f;
#pragma unroll
  for (int k = 0; k < 8; ++k) s += op[((br * 8 + k) * 8 + b) * 512 + n];
  ((unsigned short*)p.out)[b * 1024 + br * 512 + n] = (unsigned short)(pack_bf16(s, s) & 0xffffu);
}

extern "C" void kernel_launch(void* const* d_in, const int* in_sizes, int n_in, void* d_out, int out_size, void* d_ws, size_t ws_size,
                              hipStream_t stream) {
  Params p;
  p.x[0] = d_in[0]; p.k[0] = d_in[1]; p.v[0] = d_in[2]; p.wq[0] = d_in[3]; p.wo[0] = d_in[4];
  p.x[1] = d_in[5]; p.k[1] = d_in[6]; p.v[1] = d_in[7]; p.wq[1] = d_in[8]; p.wo[1] = d_in[9];
  p.out = d_out; p.ws = (unsigned char*)d_ws;
  k_qproj<<<NWG, NTHR, 0, stream>>>(p);
  k_attn<<<NWG, NTHR, 0, stream>>>(p);
  k_oproj<<<NWG, NTHR, 0, stream>>>(p);
  k_final<<<32, 256, 0, stream>>>(p);
}
```

```cpp
#include <hip/hip_runtime.h>
#include <hip/hip_bf16.h>
#include <stdint.h>

typedef unsigned u32;
typedef u32 v4u __attribute__((ext_vector_type(4)));
typedef u32 v2u __attribute__((ext_vector_type(2)));
typedef float f32x4 __attribute__((ext_vector_type(4)));
typedef float f32x2 __attribute__((ext_vector_type(2)));
typedef __bf16 bf16x8 __attribute__((ext_vector_type(8)));
typedef __bf16 bf16x2 __attribute__((ext_vector_type(2)));
typedef short v4s __attribute__((ext_vector_type(4)));
#define LAS __attribute__((address_space(3)))

#define NSLOT 7
#define NWIN 4
#define PRE 2
#define POLL1 3
#define POLL2 5
#define POLL_SLEEP 8

constexpr int NB = 8, DM = 512, HQ = 32, HKV = 8, SK = 8192, DH = 128;
constexpr int NWG = 256, NTHR = 256;
constexpr int UT = SK / 16;
constexpr int NS = UT / 8;
constexpr int KV_BYTES = NB * HKV * SK * DH * 2;
constexpr float CSC = 0.08838834764831845f * 1.4426950408889634f;
constexpr int AUX_NT = 2, AUX_SC1 = 16;
constexpr unsigned SPIN_CAP = 1u << 17;
constexpr int SOOB = 0x9C40000;

constexpr int WS_EPOCH = 0;
constexpr int WS_QG = 4096;
constexpr int PG_HEAD = 1088, PG_WG = 4 * PG_HEAD;
constexpr int WS_PG = WS_QG + 2 * 8 * 8 * 8192;
constexpr int WS_OG = WS_PG + NWG * PG_WG;
constexpr int WS_END = WS_OG + NWG * 4096;
#define STAMP(i) do { } while (0)
#define STAMPV(i, v) do { } while (0)

constexpr int L_W = 0, L_RED = 32768, L_MRG = L_RED + 8192, MRG_REC = 132, L_Q = L_MRG + 16 * MRG_REC * 4, L_USED = L_Q + 1024;
constexpr int L_END = 84 * 1024;
static_assert(L_USED <= L_END, "");

struct Params {
  const void* x[2];
  const void* k[2];
  const void* v[2];
  const void* wq[2];
  const void* wo[2];
  void* out;
  unsigned char* ws;
};

__device__ __forceinline__ f32x4 mfma32(v4u a, v4u b, f32x4 c) {
  return __builtin_amdgcn_mfma_f32_16x16x32_bf16(__builtin_bit_cast(bf16x8, a), __builtin_bit_cast(bf16x8, b), c, 0, 0, 0);
}
__device__ __forceinline__ f32x4 mfma16(v2u a, v2u b, f32x4 c) {
  return __builtin_amdgcn_mfma_f32_16x16x16bf16_1k(__builtin_bit_cast(v4s, a), __builtin_bit_cast(v4s, b), c, 0, 0, 0);
}
__device__ __forceinline__ u32 pack_bf16(float lo, float hi) {
  f32x2 f = {lo, hi};
  return __builtin_bit_cast(u32, __builtin_convertvector(f, bf16x2));
}
__device__ __forceinline__ v2u tr_read(LAS unsigned char* p) {
  return __builtin_bit_cast(v2u, __builtin_amdgcn_ds_read_tr16_b64_v4i16((LAS v4s*)p));
}
__device__ __forceinline__ u32 f2u(float f) { return __builtin_bit_cast(u32, f); }
__device__ __forceinline__ float u2f(u32 u) { return __builtin_bit_cast(float, u); }
__device__ __forceinline__ float ror8(float x) { return u2f((u32)__builtin_amdgcn_update_dpp(0, (int)f2u(x), 0x128, 0xf, 0xf, false)); }
#define MEM_FENCE() asm volatile("" ::: "memory")
#define SB() __builtin_amdgcn_sched_barrier(0)
__device__ __forceinline__ bool all_lanes(bool c) { return __builtin_amdgcn_ballot_w64(c) == ~0ull; }

__device__ __forceinline__ void stage_w(LAS unsigned char* L, const v4u (&wreg)[8], int w, int lane) {
  LAS unsigned char* wl = L + L_W + w * 8192;
#pragma unroll
  for (int i = 0; i < 8; ++i) *(LAS v4u*)(wl + i * 1024 + lane * 16) = wreg[i];
}
__device__ __forceinline__ f32x2 proj64(LAS unsigned char* L, const v4u (&afrag)[2], int w, int lane, int tid) {
  const int g = lane >> 4, q = (lane >> 2) & 3, p = lane & 3;
  LAS unsigned char* rb = L + L_W + w * 8192 + (8 * g + q) * 128 + p * 8;
  f32x4 acc[4];
#pragma unroll
  for (int ct = 0; ct < 4; ++ct) acc[ct] = f32x4{0.f, 0.f, 0.f, 0.f};
#pragma unroll
  for (int s = 0; s < 2; ++s)
#pragma unroll
    for (int ct = 0; ct < 4; ++ct) {
      const v2u lo = tr_read(rb + s * 4096 + ct * 32);
      const v2u hi = tr_read(rb + s * 4096 + ct * 32 + 512);
      const v4u bfrag = {lo.x, lo.y, hi.x, hi.y};
      acc[ct] = mfma32(afrag[s], bfrag, acc[ct]);
    }
  LAS f32x4* red = (LAS f32x4*)(L + L_RED);
  if (lane < 32) {
#pragma unroll
    for (int ct = 0; ct < 4; ++ct) red[(w * 4 + ct) * 32 + lane] = acc[ct];
  }
  __syncthreads();
  const int ct = tid >> 6, r = (tid >> 4) & 3, l32 = 16 * ((tid >> 3) & 1) + 2 * (tid & 7);
  LAS float* rf = (LAS float*)(L + L_RED) + (ct * 32 + l32) * 4 + r;
  float s0 = 0.f, s1 = 0.f;
#pragma unroll
  for (int ww = 0; ww < 4; ++ww) { s0 += rf[ww * 512]; s1 += rf[ww * 512 + 4]; }
  return f32x2{s0, s1};
}

__global__ void __launch_bounds__(NTHR) k_fused(Params p) {
  __shared__ __attribute__((aligned(16))) unsigned char smem[L_END];
  LAS unsigned char* L = (LAS unsigned char*)smem;
  const int tid = threadIdx.x, lane = tid & 63, w = __builtin_amdgcn_readfirstlane(tid >> 6);
  const int wg = blockIdx.x, side = wg & 1, U = wg >> 1, br = U >> 6, ub = U & 63, b = ub >> 3, kvh = ub & 7;
  STAMP(0);
  STAMPV(15, 0);
  STAMPV(14, __builtin_amdgcn_s_getreg((3 << 11) | 20) & 7u);
  const __amdgpu_buffer_rsrc_t rk = __builtin_amdgcn_make_buffer_rsrc((void*)p.k[br], 0, KV_BYTES, 0x00020000);
  const __amdgpu_buffer_rsrc_t rv = __builtin_amdgcn_make_buffer_rsrc((void*)p.v[br], 0, KV_BYTES, 0x00020000);
  const __amdgpu_buffer_rsrc_t rw = __builtin_amdgcn_make_buffer_rsrc((void*)p.ws, 0, WS_END, 0x00020000);
  const __amdgpu_buffer_rsrc_t rq = __builtin_amdgcn_make_buffer_rsrc((void*)p.wq[br], 0, DM * HQ * DH * 2, 0x00020000);
  const __amdgpu_buffer_rsrc_t ro = __builtin_amdgcn_make_buffer_rsrc((void*)p.wo[br], 0, DM * HQ * DH * 2, 0x00020000);
  const __amdgpu_buffer_rsrc_t rx = __builtin_amdgcn_make_buffer_rsrc((void*)p.x[br], 0, NB * DM * 2, 0x00020000);
  const int voffK = (lane & 15) * 256 + (lane >> 4) * 16;
  const int voffV = (lane >> 4) * 1024 + (lane & 15) * 16;
  const int start = (ub * UT + (side ? UT - 1 - w : w)) * 4096;
  const int step = side ? -4 * 4096 : 4 * 4096;
  constexpr int P = NSLOT, W = NWIN;
  v4u kb[P][4], vb[P][4];
  auto issueK = [&](int s, int so) {
#pragma unroll
    for (int q = 0; q < 4; ++q) kb[s][q] = __builtin_amdgcn_raw_buffer_load_b128(rk, voffK + q * 64, so, AUX_NT);
  };
  auto issueV = [&](int s, int so) {
#pragma unroll
    for (int q = 0; q < 4; ++q) vb[s][q] = __builtin_amdgcn_raw_buffer_load_b128(rv, voffV + q * 256, so, AUX_NT);
  };
  auto issueT = [&](int s) {
    issueK(s, start + step * s); SB();
    issueV(s, start + step * s); SB();
  };
  const int pb = 4 * ((tid >> 3) & 1) + ((tid >> 4) & 3), pn = 16 * (tid >> 6) + 2 * (tid & 7);
  const v2u ep = __builtin_amdgcn_raw_buffer_load_b64(rw, 0, WS_EPOCH + br * 256, AUX_SC1);
  v4u wreg[8], af[2];
  {
    const int vo = (lane >> 3) * (HQ * DH * 2) + (lane & 7) * 16;
    const int so = (256 * side + 64 * w) * (HQ * DH * 2) + (kvh * 512 + b * 64) * 2;
#pragma unroll
    for (int i = 0; i < 8; ++i) wreg[i] = __builtin_amdgcn_raw_buffer_load_b128(rq, vo, so + i * 8 * (HQ * DH * 2), AUX_NT);
    const int xo = (lane & 7) * (DM * 2) + 8 * (lane >> 4) * 2;
#pragma unroll
    for (int s = 0; s < 2; ++s) af[s] = __builtin_amdgcn_raw_buffer_load_b128(rx, xo + s * 64, (256 * side + 64 * w) * 2, 0);
  }
  SB();
#pragma unroll
  for (int s = 0; s < PRE; ++s) issueT(s);
  const u32 tagA = __builtin_amdgcn_readfirstlane(ep.x) + 1u;
  const u32 tagB = __builtin_amdgcn_readfirstlane(ep.y) + 0x9E3779B9u;
  stage_w(L, wreg, w, lane);
  {
    const f32x2 r = proj64(L, af, w, lane, tid);
    STAMP(1);
    __builtin_amdgcn_raw_buffer_store_b128(v4u{f2u(r.x), tagA, f2u(r.y), tagB}, rw, pb * 8192 + (pn >> 1) * 16,
                                           WS_QG + (br * 8 + kvh) * 65536 + side * 4096 + b * 512, AUX_SC1);
  }
  SB();
  const int qso = WS_QG + ((br * 8 + kvh) * 8 + b) * 8192 + w * 1024;
  v4u g0, g1, h0, h1;
#pragma unroll
  for (int s = PRE; s < P; ++s) {
    issueT(s);
    if (s == POLL1) {
      MEM_FENCE();
      g0 = __builtin_amdgcn_raw_buffer_load_b128(rw, lane * 16, qso, AUX_SC1);
      g1 = __builtin_amdgcn_raw_buffer_load_b128(rw, lane * 16, qso + 4096, AUX_SC1);
      SB();
    }
    if (s == POLL2) {
      MEM_FENCE();
      h0 = __builtin_amdgcn_raw_buffer_load_b128(rw, lane * 16, qso, AUX_SC1);
      h1 = __builtin_amdgcn_raw_buffer_load_b128(rw, lane * 16, qso + 4096, AUX_SC1);
      SB();
    }
  }
  STAMP(2);
  u32 qv = pack_bf16(u2f(g0.x) + u2f(g1.x), u2f(g0.z) + u2f(g1.z));
  if (!all_lanes(g0.y == tagA && g0.w == tagB && g1.y == tagA && g1.w == tagB)) {
    v4u r0 = h0, r1 = h1;
    unsigned spins = 0;
    for (;;) {
      qv = pack_bf16(u2f(r0.x) + u2f(r1.x), u2f(r0.z) + u2f(r1.z));
      if (all_lanes(r0.y == tagA && r0.w == tagB && r1.y == tagA && r1.w == tagB)) break;
      if (++spins > SPIN_CAP) break;
      __builtin_amdgcn_s_sleep(2);
      MEM_FENCE();
      r0 = __builtin_amdgcn_raw_buffer_load_b128(rw, lane * 16, qso, AUX_SC1);
      r1 = __builtin_amdgcn_raw_buffer_load_b128(rw, lane * 16, qso + 4096, AUX_SC1);
      asm volatile("" : "+v"(r0), "+v"(r1));
    }
    STAMPV(15, spins + 1);
  }
  STAMP(3);
  v4u qf[4];
  {
    LAS unsigned char* ql = L + L_Q;
    *(LAS u32*)(ql + w * 256 + lane * 4) = qv;
    __syncthreads();
    LAS unsigned char* qr = ql + ((lane >> 2) & 3) * 256 + (lane >> 4) * 16;
#pragma unroll
    for (int s = 0; s < 4; ++s) qf[s] = *(LAS v4u*)(qr + s * 64);
  }
  STAMP(4);
  const int jq = lane & 3;
  const u32 m0 = jq == 0 ? 0x0000ffffu : (jq == 1 ? 0xffff0000u : 0u);
  const u32 m1 = jq == 2 ? 0x0000ffffu : (jq == 3 ? 0xffff0000u : 0u);
  float mraw = -INFINITY, mc = 0.f, lsum = 0.f;
  f32x4 accLo = {0.f, 0.f, 0.f, 0.f}, accHi = {0.f, 0.f, 0.f, 0.f};
  auto tile = [&](int s, bool refill, int so) {
    f32x4 sc = {0.f, 0.f, 0.f, 0.f};
#pragma unroll
    for (int q = 0; q < 4; ++q) sc = mfma32(kb[s][q], qf[q], sc);
    SB();
    if (refill) issueK(s, so);
    SB();
    float mx = fmaxf(fmaxf(sc[0], sc[1]), fmaxf(sc[2], sc[3]));
    if (__builtin_amdgcn_ballot_w64(mx > mraw) != 0ull) {
      mx = fmaxf(mx, __shfl_xor(mx, 16));
      mx = fmaxf(mx, __shfl_xor(mx, 32));
      const float mnew = fmaxf(mraw, mx);
      const float alpha = __builtin_amdgcn_exp2f((mraw - mnew) * CSC);
      mraw = mnew; mc = mnew * CSC;
      lsum *= alpha;
      const float ab = __shfl(alpha, (lane >> 4) * 4);
      accLo *= ab; accHi *= ab;
    }
#pragma unroll
    for (int t = 0; t < 4; ++t) {
      const float pe = __builtin_amdgcn_exp2f(fmaf(sc[t], CSC, -mc));
      lsum += pe;
      const u32 pk = pack_bf16(pe, pe);
      const v2u a = {pk & m0, pk & m1};
      accLo = mfma16(a, v2u{vb[s][t].x, vb[s][t].y}, accLo);
      accHi = mfma16(a, v2u{vb[s][t].z, vb[s][t].w}, accHi);
    }
    SB();
    if (refill) issueV(s, so);
    SB();
  };
  {
    static_assert(P - W <= W, "");
    int so = start + step * P;
#pragma unroll
    for (int s = 0; s < W; ++s) {
      tile(s, true, so); so += step;
      if (W + s < P) tile(W + s, false, 0);
    }
    asm volatile("" :: "v"(h0), "v"(h1));
    constexpr int REST = NS - P - W;
    constexpr int R = REST % W;
#pragma unroll
    for (int s = 0; s < R; ++s) { tile(s, true, so); so += step; }
    for (int i = 0; i < REST / W; ++i) {
#pragma unroll
      for (int s = 0; s < W; ++s) { tile((R + s) % W, true, so); so += step; }
    }
    {
      const int vo = (lane >> 3) * (DM * 2) + (lane & 7) * 16;
      const int wso = (kvh * 512 + 256 * side + 64 * w) * (DM * 2) + b * 128;
#pragma unroll
      for (int i = 0; i < 8; ++i) wreg[i] = __builtin_amdgcn_raw_buffer_load_b128(ro, vo, wso + i * 8 * (DM * 2), AUX_NT);
    }
    SB();
#pragma unroll
    for (int s = 0; s < W; ++s) tile((R + s) % W, false, 0);
  }
  STAMP(5);
  lsum += __shfl_xor(lsum, 16);
  lsum += __shfl_xor(lsum, 32);
  LAS float* mrg = (LAS float*)(L + L_MRG);
  {
    LAS float* rec = mrg + (w * 4 + (lane >> 4)) * MRG_REC + 8 * (lane & 15);
    *(LAS f32x4*)rec = accLo;
    *(LAS f32x4*)(rec + 4) = accHi;
    if (lane < 16 && (lane & 3) == 0) {
      LAS float* r2 = mrg + (w * 4 + (lane >> 2)) * MRG_REC + 128;
      r2[0] = mc; r2[1] = lsum;
    }
  }
  __syncthreads();
  {
    const int d0 = 2 * lane;
    float M = -INFINITY;
#pragma unroll
    for (int ww = 0; ww < 4; ++ww) M = fmaxf(M, mrg[(ww * 4 + w) * MRG_REC + 128]);
    float Ls = 0.f, o0 = 0.f, o1 = 0.f;
#pragma unroll
    for (int ww = 0; ww < 4; ++ww) {
      LAS const float* rec = mrg + (ww * 4 + w) * MRG_REC;
      const float sc = __builtin_amdgcn_exp2f(rec[128] - M);
      Ls = fmaf(sc, rec[129], Ls);
      o0 = fmaf(sc, rec[d0], o0);
      o1 = fmaf(sc, rec[d0 + 1], o1);
    }
    const int pso = WS_PG + wg * PG_WG + w * PG_HEAD;
    __builtin_amdgcn_raw_buffer_store_b128(v4u{f2u(o0), tagA, f2u(o1), tagB}, rw, lane * 16, pso, AUX_SC1);
    if (lane == 0) __builtin_amdgcn_raw_buffer_store_b128(v4u{f2u(M), tagA, f2u(Ls), tagB}, rw, 1024, pso, AUX_SC1);
  }
  SB();
  STAMP(6);
  {
    const int sb = lane & 7, ss = (lane >> 3) & 1, g = lane >> 4;
    const int src = ((br * 64 + sb * 8 + kvh) << 1) | ss;
    const int vo = src * PG_WG + g * 64;
    const int gso = WS_PG + (2 * side + (w >> 1)) * PG_HEAD;
    const int pso = gso + (w & 1) * 512;
    v4u ml = {0u, ~tagA, 0u, 0u};
    v4u gr[8];
#pragma unroll
    for (int j = 0; j < 8; ++j) gr[j] = v4u{0u, ~tagA, 0u, 0u};
    {
      bool mlok = false, ok = false, first = true;
      unsigned spins = 0;
      for (;;) {
        MEM_FENCE();
        const bool few = first || __builtin_popcountll(__builtin_amdgcn_ballot_w64(!mlok)) <= 8;
        const bool needp = !ok && (mlok || few);
        if (!mlok) ml = __builtin_amdgcn_raw_buffer_load_b128(rw, src * PG_WG + 1024, gso, AUX_SC1);
        if (needp) {
#pragma unroll
          for (int s = 0; s < 2; ++s)
#pragma unroll
            for (int j = 0; j < 4; ++j) gr[s * 4 + j] = __builtin_amdgcn_raw_buffer_load_b128(rw, vo + s * 256 + j * 16, pso, AUX_SC1);
        }
        if (first) { stage_w(L, wreg, w, lane); STAMP(7); }
        first = false;
        mlok = ml.y == tagA && ml.w == tagB;
        u32 bad = 0u;
#pragma unroll
        for (int j = 0; j < 8; ++j) bad |= (gr[j].y ^ tagA) | (gr[j].w ^ tagB);
        ok = mlok && bad == 0u;
        if (all_lanes(ok)) break;
        if (++spins > SPIN_CAP) break;
        if (!few) __builtin_amdgcn_s_sleep(POLL_SLEEP);
      }
    }
    STAMP(8);
    const float mm = u2f(ml.x), ll = u2f(ml.z);
    const float mo = ror8(mm), lo = ror8(ll);
    const float M = fmaxf(mm, mo);
    const float sc = __builtin_amdgcn_exp2f(mm - M), so2 = __builtin_amdgcn_exp2f(mo - M);
    const float f = sc / (sc * ll + so2 * lo);
#pragma unroll
    for (int s = 0; s < 2; ++s) {
      u32 pk[4];
#pragma unroll
      for (int j = 0; j < 4; ++j) {
        float a0 = u2f(gr[s * 4 + j].x) * f, a1 = u2f(gr[s * 4 + j].z) * f;
        a0 += ror8(a0); a1 += ror8(a1);
        pk[j] = pack_bf16(a0, a1);
      }
      af[s] = v4u{pk[0], pk[1], pk[2], pk[3]};
    }
  }
  {
    const f32x2 r = proj64(L, af, w, lane, tid);
    const int dest = ((br * 64 + b * 8 + pb) << 1) | (pn >> 5);
    __builtin_amdgcn_raw_buffer_store_b128(v4u{f2u(r.x), tagA, f2u(r.y), tagB}, rw, dest * 4096 + ((pn & 31) >> 1) * 16,
                                           WS_OG + (2 * kvh + side) * 256, AUX_SC1);
  }
  STAMP(9);
  if (w == 0) {
    v4u a[4];
    unsigned spins = 0;
    for (;;) {
      MEM_FENCE();
      u32 bad = 0u;
#pragma unroll
      for (int i = 0; i < 4; ++i) a[i] = __builtin_amdgcn_raw_buffer_load_b128(rw, lane * 16 + i * 1024, WS_OG + wg * 4096, AUX_SC1);
#pragma unroll
      for (int i = 0; i < 4; ++i) bad |= (a[i].y ^ tagA) | (a[i].w ^ tagB);
      if (all_lanes(bad == 0u)) break;
      if (++spins > SPIN_CAP) break;
      __builtin_amdgcn_s_sleep(1);
    }
    float s0 = (u2f(a[0].x) + u2f(a[1].x)) + (u2f(a[2].x) + u2f(a[3].x));
    float s1 = (u2f(a[0].z) + u2f(a[1].z)) + (u2f(a[2].z) + u2f(a[3].z));
    s0 += __shfl_xor(s0, 16); s1 += __shfl_xor(s1, 16);
    s0 += __shfl_xor(s0, 32); s1 += __shfl_xor(s1, 32);
    if (lane < 16) ((u32*)p.out)[(kvh * 1024 + br * 512 + (2 * b + side) * 32) / 2 + lane] = pack_bf16(s0, s1);
    if (ub == 0 && side == 0 && lane == 0) __builtin_amdgcn_raw_buffer_store_b64(v2u{tagA, tagB}, rw, 0, WS_EPOCH + br * 256, AUX_SC1);
    STAMP(10);
  }
}

extern "C" void kernel_launch(void* const* d_in, const int* in_sizes, int n_in, void* d_out, int out_size, void* d_ws, size_t ws_size,
                              hipStream_t stream) {
  Params p;
  p.x[0] = d_in[0]; p.k[0] = d_in[1]; p.v[0] = d_in[2]; p.wq[0] = d_in[3]; p.wo[0] = d_in[4];
  p.x[1] = d_in[5]; p.k[1] = d_in[6]; p.v[1] = d_in[7]; p.wq[1] = d_in[8]; p.wo[1] = d_in[9];
  p.out = d_out; p.ws = (unsigned char*)d_ws;
  k_fused<<<NWG, NTHR, 0, stream>>>(p);
}
```

```cpp
#include <hip/hip_runtime.h>
#include <hip/hip_bf16.h>
#include <stdint.h>
#include <utility>

typedef unsigned u32;
typedef u32 v4u __attribute__((ext_vector_type(4)));
typedef u32 v2u __attribute__((ext_vector_type(2)));
typedef float f32x4 __attribute__((ext_vector_type(4)));
typedef float f32x2 __attribute__((ext_vector_type(2)));
typedef __bf16 bf16x8 __attribute__((ext_vector_type(8)));
typedef __bf16 bf16x2 __attribute__((ext_vector_type(2)));
typedef short v4s __attribute__((ext_vector_type(4)));
#define LAS __attribute__((address_space(3)))

#define NFLY 5
#define NHEAD 3
#define RING 16
#define NWIDE 8
#define TAU 230
#define TBASE 560
#define DRY 0
#define POLL_SLEEP 8
constexpr int TPI = RING / 4, NIT = 32 / TPI;
static_assert(32 % TPI == 0, "");

constexpr int NB = 8, DM = 512, HQ = 32, HKV = 8, SK = 8192, DH = 128;
constexpr int NWG = 256, NTHR = 512;
constexpr int KV_BYTES = NB * HKV * SK * DH * 2;
constexpr float CSC = 0.08838834764831845f * 1.4426950408889634f;
constexpr int AUX_NT = 2, AUX_SC1 = 16;
constexpr unsigned SPIN_CAP = 1u << 17, LSPIN_CAP = 1u << 22;
constexpr int OOB = 0x40000000;

constexpr int WS_EPOCH = 0;
constexpr int WS_QG = 4096;
constexpr int PG_HEAD = 1088, PG_WG = 4 * PG_HEAD;
constexpr int WS_PG = WS_QG + 2 * 8 * 8 * 16384;
constexpr int WS_OG = WS_PG + 2 * NWG * PG_WG;
constexpr int WS_END = WS_OG + 2 * NWG * 4096;
#define STAMP(i) do { } while (0)
#define STAMPV(i, v) do { } while (0)

constexpr int MRG_REC = 132;
constexpr int L_W = 0;
constexpr int L_RED = 65536;
constexpr int L_MRG = L_RED + 32768;
constexpr int L_Q = L_MRG + 2 * 16 * MRG_REC * 4;
constexpr int L_SC = L_Q + 2048;
constexpr int L_FLAG = L_SC + 32768;
constexpr int L_FL = L_FLAG + 64;
constexpr int L_END = L_FL + 1024;

struct Params {
  const void* x[2];
  const void* k[2];
  const void* v[2];
  const void* wq[2];
  const void* wo[2];
  void* out;
  unsigned char* ws;
};

__device__ __forceinline__ f32x4 mfma32(v4u a, v4u b, f32x4 c) {
  return __builtin_amdgcn_mfma_f32_16x16x32_bf16(__builtin_bit_cast(bf16x8, a), __builtin_bit_cast(bf16x8, b), c, 0, 0, 0);
}
__device__ __forceinline__ f32x4 mfma16(v2u a, v2u b, f32x4 c) {
  return __builtin_amdgcn_mfma_f32_16x16x16bf16_1k(__builtin_bit_cast(v4s, a), __builtin_bit_cast(v4s, b), c, 0, 0, 0);
}
__device__ __forceinline__ u32 pack_bf16(float lo, float hi) {
  f32x2 f = {lo, hi};
  return __builtin_bit_cast(u32, __builtin_convertvector(f, bf16x2));
}
__device__ __forceinline__ v2u tr_read(LAS unsigned char* p) {
  return __builtin_bit_cast(v2u, __builtin_amdgcn_ds_read_tr16_b64_v4i16((LAS v4s*)p));
}
__device__ __forceinline__ u32 f2u(float f) { return __builtin_bit_cast(u32, f); }
__device__ __forceinline__ float u2f(u32 u) { return __builtin_bit_cast(float, u); }
template <int CTRL> __device__ __forceinline__ float dpp(float x) {
  return u2f((u32)__builtin_amdgcn_mov_dpp((int)f2u(x), CTRL, 0xf, 0xf, true));
}
#define MEM_FENCE() asm volatile("" ::: "memory")
#define SB() __builtin_amdgcn_sched_barrier(0)
#define WAITVM(n) do { asm volatile("s_waitcnt vmcnt(%0)" :: "i"(n) : "memory"); SB(); } while (0)
#define WAITVM2(x, n, m) do { asm volatile("s_cmp_lg_u32 %0, 0\n\ts_cbranch_scc1 1f\n\ts_waitcnt vmcnt(%1)\n1:\n\ts_waitcnt vmcnt(%2)" :: "s"(x), "i"(n), "i"(m) : "memory", "scc"); SB(); } while (0)
#define AI __attribute__((always_inline))
__device__ __forceinline__ bool all_lanes(bool c) { return __builtin_amdgcn_ballot_w64(c) == ~0ull; }
template <int LO, int... I, class F> __device__ __forceinline__ void static_for_impl(std::integer_sequence<int, I...>, F&& f) {
  (f(std::integral_constant<int, LO + I>{}), ...);
}
template <int LO, int HI, class F> __device__ __forceinline__ void static_for(F&& f) {
  if constexpr (HI > LO) static_for_impl<LO>(std::make_integer_sequence<int, HI - LO>{}, f);
}

__device__ __forceinline__ void lds_arrive(LAS unsigned char* L, int idx, int lane) {
  asm volatile("s_waitcnt lgkmcnt(0)" ::: "memory");
  if (lane == 0) __hip_atomic_fetch_add((LAS u32*)(L + L_FLAG) + idx, 1u, __ATOMIC_RELAXED, __HIP_MEMORY_SCOPE_WORKGROUP);
  MEM_FENCE();
}
__device__ __forceinline__ void lds_wait(LAS unsigned char* L, int idx, u32 want, int sleep) {
  for (unsigned spins = 0; spins < LSPIN_CAP; ++spins) {
    MEM_FENCE();
    const u32 v = __hip_atomic_load((LAS u32*)(L + L_FLAG) + idx, __ATOMIC_RELAXED, __HIP_MEMORY_SCOPE_WORKGROUP);
    if (__builtin_amdgcn_readfirstlane(v) >= want) break;
    if (sleep) __builtin_amdgcn_s_sleep(1);
  }
  MEM_FENCE();
}

__global__ void __launch_bounds__(NTHR, 2) k_fused(Params p) {
  __shared__ __attribute__((aligned(16))) unsigned char smem[L_END];
  LAS unsigned char* L = (LAS unsigned char*)smem;
  const int lane = threadIdx.x & 63, w8 = __builtin_amdgcn_readfirstlane(threadIdx.x >> 6), w = w8 & 3;
  const int wg = blockIdx.x, qd = wg & 3, ub = wg >> 2, b = ub >> 3, kvh = ub & 7;
  const int tid = w * 64 + lane;
  const int grp = w8 >> 2;
  const __amdgpu_buffer_rsrc_t rw = __builtin_amdgcn_make_buffer_rsrc((void*)p.ws, 0, WS_END, 0x00020000);
  const int pb = 4 * ((tid >> 3) & 1) + ((tid >> 4) & 3), pn = 16 * (tid >> 6) + 2 * (tid & 7);
  u32 tagA = 0u, tagB = 0u, gbar = 0u;
  auto bad4 = [&](const v4u& g) AI -> u32 { return (g.y ^ tagA) | (g.w ^ tagB); };
  auto ok4 = [&](const v4u (&g)[4]) AI -> bool { return all_lanes((bad4(g[0]) | bad4(g[1]) | bad4(g[2]) | bad4(g[3])) == 0u); };
  auto put = [&](float v0, float v1, int off) AI { __builtin_amdgcn_raw_buffer_store_b128(v4u{f2u(v0), tagA, f2u(v1), tagB}, rw, off, 0, AUX_SC1); };
  auto proj_put = [&](int r, v4u afrag) AI {
    LAS unsigned char* rb = L + L_W + r * 16384 + w * 4096 + (8 * (lane >> 4) + ((lane >> 2) & 3)) * 128 + (lane & 3) * 8;
    LAS f32x4* red = (LAS f32x4*)(L + L_RED + r * 8192);
    f32x4 acc[4];
#pragma unroll
    for (int ct = 0; ct < 4; ++ct) {
      const v2u lo = tr_read(rb + ct * 32);
      const v2u hi = tr_read(rb + ct * 32 + 512);
      acc[ct] = mfma32(afrag, v4u{lo.x, lo.y, hi.x, hi.y}, f32x4{0.f, 0.f, 0.f, 0.f});
    }
    if (lane < 32) {
#pragma unroll
      for (int ct = 0; ct < 4; ++ct) red[(w * 4 + ct) * 32 + lane] = acc[ct];
    }
  };
  auto group_barrier = [&]() AI {
    lds_arrive(L, 4 + grp, lane);
    gbar += 4u;
    lds_wait(L, 4 + grp, gbar, 0);
  };
  auto proj_get = [&](int r) AI -> f32x2 {
    LAS float* rf = (LAS float*)(L + L_RED + r * 8192) + (w * 32 + 16 * ((tid >> 3) & 1) + 2 * (tid & 7)) * 4 + ((tid >> 4) & 3);
    float s0 = 0.f, s1 = 0.f;
#pragma unroll
    for (int ww = 0; ww < 4; ++ww) { s0 += rf[ww * 512]; s1 += rf[ww * 512 + 4]; }
    return f32x2{s0, s1};
  };

  if (grp == 0) {
    const u32 t0 = (u32)__builtin_amdgcn_s_memrealtime();
    STAMP(0);
    STAMPV(14, __builtin_amdgcn_s_getreg((3 << 11) | 20) & 7u);
    const int voffK = ((lane >> 2) & 3) * 256 + (lane >> 4) * 64 + (lane & 3) * 16;
    const int voffV = lane * 16;
    const int sbase = wg * 524288 + w * 4096;
    v4u ring[RING];
    {
      const __amdgpu_buffer_rsrc_t rk = __builtin_amdgcn_make_buffer_rsrc((void*)p.k[0], 0, KV_BYTES, 0x00020000);
      static_for<0, RING>([&](auto I) AI {
        constexpr int i = decltype(I)::value;
        if constexpr (i == NHEAD) __builtin_amdgcn_s_barrier();
        if constexpr (i >= NHEAD) WAITVM(NHEAD - 1);
        ring[i] = __builtin_amdgcn_raw_buffer_load_b128(rk, voffK + (i & 3) * 1024, sbase + (i >> 2) * 16384, AUX_NT);
        SB();
      });
    }
    STAMP(2);
    const int jq = lane & 3;
    const u32 m0 = jq == 0 ? 0x0000ffffu : (jq == 1 ? 0xffff0000u : 0u);
    const u32 m1 = jq == 2 ? 0x0000ffffu : (jq == 3 ? 0xffff0000u : 0u);
    LAS unsigned char* scl = L + L_SC + w * 8192 + (lane >> 2) * 16;
    u32 due = TBASE;
    auto behind = [&]() AI -> int {
      const u32 t = (u32)__builtin_amdgcn_s_memrealtime() - t0;
      const int r = __builtin_amdgcn_readfirstlane(t > due ? 1 : 0);
      due += TAU * RING / 16;
      return r;
    };
    lds_wait(L, 0, 4u, 0);
    STAMP(3);
#pragma unroll 1
    for (int br = 0; br < 2; ++br) {
      const v4u qf = *(LAS v4u*)(L + L_Q + br * 1024 + voffK);
      const void* pk = p.k[br];
      const void* pv = p.v[br];
      float mx = -INFINITY;
#pragma unroll 1
      for (int it = 0; it < NIT; ++it) {
        const bool last = it == NIT - 1;
        const int wide = behind();
        const __amdgpu_buffer_rsrc_t rn = __builtin_amdgcn_make_buffer_rsrc((void*)(last ? pv : pk), 0, KV_BYTES, 0x00020000);
        const int vn = last ? voffV : voffK;
        const int sn = sbase + (last ? 0 : (it + 1) * TPI * 16384);
        f32x4 st;
        static_for<0, RING>([&](auto I) AI {
          constexpr int i = decltype(I)::value;
          const f32x4 d = mfma32(ring[i], qf, f32x4{0.f, 0.f, 0.f, 0.f});
          float s = (dpp<0x00>(d[0]) + dpp<0x55>(d[1])) + (dpp<0xAA>(d[2]) + dpp<0xFF>(d[3]));
          mx = fmaxf(mx, s);
          asm volatile("" : "+v"(s), "+v"(mx));
          st[i & 3] = s;
          if constexpr ((i & 3) == 3) { if (jq == 0) *(LAS f32x4*)(scl + (it * TPI + (i >> 2)) * 256) = st; }
          SB();
          WAITVM2(wide, NFLY - 1, NWIDE - 1);
          ring[i] = __builtin_amdgcn_raw_buffer_load_b128(rn, vn + (i & 3) * 1024, sn + (i >> 2) * 16384, AUX_NT);
          SB();
        });
      }
      mx = fmaxf(mx, __shfl_xor(mx, 16));
      mx = fmaxf(mx, __shfl_xor(mx, 32));
      const float mc = mx * CSC;
      if (br == 0) STAMP(11); else STAMP(12);
      float lsum = 0.f;
      f32x4 accLo = {0.f, 0.f, 0.f, 0.f}, accHi = {0.f, 0.f, 0.f, 0.f};
#pragma unroll 1
      for (int it = 0; it < NIT; ++it) {
        const bool last = it == NIT - 1;
        const int wide = behind();
        const __amdgpu_buffer_rsrc_t rn =
            __builtin_amdgcn_make_buffer_rsrc((void*)(last ? p.k[1] : pv), 0, (last && br == 1) ? 0 : KV_BYTES, 0x00020000);
        const int vn = last ? voffK : voffV;
        const int sn = sbase + (last ? 0 : (it + 1) * TPI * 16384);
        f32x4 sv[TPI];
#pragma unroll
        for (int k = 0; k < TPI; ++k) sv[k] = *(LAS f32x4*)(scl + (it * TPI + k) * 256);
        static_for<0, RING>([&](auto I) AI {
          constexpr int i = decltype(I)::value;
          const float pe = __builtin_amdgcn_exp2f(fmaf(sv[i >> 2][i & 3], CSC, -mc));
          lsum += pe;
          asm volatile("" : "+v"(lsum));
          const u32 pkd = pack_bf16(pe, pe);
          const v2u a = {pkd & m0, pkd & m1};
          accLo = mfma16(a, v2u{ring[i].x, ring[i].y}, accLo);
          accHi = mfma16(a, v2u{ring[i].z, ring[i].w}, accHi);
          SB();
          WAITVM2(wide, NFLY - 1, NWIDE - 1);
          ring[i] = __builtin_amdgcn_raw_buffer_load_b128(rn, vn + (i & 3) * 1024, sn + (i >> 2) * 16384, AUX_NT);
          SB();
        });
      }
      lsum += __shfl_xor(lsum, 16);
      lsum += __shfl_xor(lsum, 32);
      LAS float* mrg = (LAS float*)(L + L_MRG) + br * 16 * MRG_REC;
      LAS float* rec = mrg + (w * 4 + (lane >> 4)) * MRG_REC + 8 * (lane & 15);
      *(LAS f32x4*)rec = accLo;
      *(LAS f32x4*)(rec + 4) = accHi;
      if (lane < 16 && jq == 0) {
        LAS float* r2 = mrg + (w * 4 + (lane >> 2)) * MRG_REC + 128;
        r2[0] = mc; r2[1] = lsum;
      }
      lds_arrive(L, 2, lane);
      if (br == 0) STAMP(4); else STAMP(5);
    }
    tagA = __builtin_amdgcn_readfirstlane(((LAS u32*)(L + L_FLAG))[8]);
    tagB = __builtin_amdgcn_readfirstlane(((LAS u32*)(L + L_FLAG))[9]);
  } else {
    STAMPV(15, 0);
    const v2u ep = __builtin_amdgcn_raw_buffer_load_b64(rw, 0, WS_EPOCH, AUX_SC1);
    const int vo_w = lane & 7, vr_w = lane >> 3;
    v4u wr[4][4], xf[2];
#pragma unroll
    for (int br = 0; br < 2; ++br) {
      const __amdgpu_buffer_rsrc_t rq = __builtin_amdgcn_make_buffer_rsrc((void*)p.wq[br], 0, DM * HQ * DH * 2, 0x00020000);
      const __amdgpu_buffer_rsrc_t rx = __builtin_amdgcn_make_buffer_rsrc((void*)p.x[br], 0, NB * DM * 2, 0x00020000);
      const int so = (128 * qd + 32 * w) * (HQ * DH * 2) + (kvh * 512 + b * 64) * 2;
#pragma unroll
      for (int i = 0; i < 4; ++i) wr[br][i] = __builtin_amdgcn_raw_buffer_load_b128(rq, vr_w * (HQ * DH * 2) + vo_w * 16, so + i * 8 * (HQ * DH * 2), AUX_NT);
      xf[br] = __builtin_amdgcn_raw_buffer_load_b128(rx, (lane & 7) * (DM * 2) + (lane >> 4) * 16, (128 * qd + 32 * w) * 2, 0);
      SB();
    }
    if (w == 0 && lane < 8) ((LAS u32*)(L + L_FLAG))[lane] = 0u;
    asm volatile("s_waitcnt lgkmcnt(0)" ::: "memory");
    __builtin_amdgcn_s_barrier();
    tagA = __builtin_amdgcn_readfirstlane(ep.x) + 1u;
    tagB = __builtin_amdgcn_readfirstlane(ep.y) + 0x9E3779B9u;
    if (lane == 0) { ((LAS u32*)(L + L_FLAG))[8] = tagA; ((LAS u32*)(L + L_FLAG))[9] = tagB; }
    auto stage_w = [&](int r) AI {
      LAS unsigned char* wl = L + L_W + r * 16384 + w * 4096;
#pragma unroll
      for (int i = 0; i < 4; ++i) *(LAS v4u*)(wl + i * 1024 + lane * 16) = wr[r][i];
    };
    stage_w(0);
    proj_put(0, xf[0]);
    stage_w(1);
    proj_put(1, xf[1]);
    group_barrier();
#pragma unroll
    for (int br = 0; br < 2; ++br) {
      const f32x2 r = proj_get(br);
      put(r.x, r.y, WS_QG + (br * 8 + kvh) * 131072 + qd * 4096 + b * 512 + pb * 16384 + (pn >> 1) * 16);
    }
    SB();
    STAMP(1);
#pragma unroll
    for (int br = 0; br < 2; ++br) {
      const __amdgpu_buffer_rsrc_t ro = __builtin_amdgcn_make_buffer_rsrc((void*)p.wo[br], 0, DM * HQ * DH * 2, 0x00020000);
      const int so = (kvh * 512 + 128 * qd + 32 * w) * (DM * 2) + b * 128;
#pragma unroll
      for (int i = 0; i < 4; ++i) wr[2 + br][i] = __builtin_amdgcn_raw_buffer_load_b128(ro, vr_w * (DM * 2) + vo_w * 16, so + i * 8 * (DM * 2), AUX_NT);
      SB();
    }
    stage_w(2);
    SB();
    {
      const int qso = WS_QG + (kvh * 8 + b) * 16384 + w * 1024;
      v4u g[2][4];
      unsigned spins = 0;
      for (;;) {
        MEM_FENCE();
#pragma unroll
        for (int br = 0; br < 2; ++br)
#pragma unroll
          for (int i = 0; i < 4; ++i) g[br][i] = __builtin_amdgcn_raw_buffer_load_b128(rw, lane * 16, qso + br * 8 * 131072 + i * 4096, AUX_SC1);
        if (ok4(g[0]) && ok4(g[1])) break;
        if (++spins > SPIN_CAP) break;
        __builtin_amdgcn_s_sleep(4);
      }
      STAMPV(15, spins);
#pragma unroll
      for (int br = 0; br < 2; ++br)
        *(LAS u32*)(L + L_Q + br * 1024 + w * 256 + lane * 4) = pack_bf16((u2f(g[br][0].x) + u2f(g[br][1].x)) + (u2f(g[br][2].x) + u2f(g[br][3].x)),
                                                                          (u2f(g[br][0].z) + u2f(g[br][1].z)) + (u2f(g[br][2].z) + u2f(g[br][3].z)));
      stage_w(3);
      lds_arrive(L, 0, lane);
    }
  }

  const int br = grp;
  const int h1_mo = (((lane >> 2) & 7) * 32 + (lane & 3)) * PG_WG + 1024;
  const int h1_vo = (lane >> 4) * PG_WG + (lane & 15) * 16;
#pragma unroll 1
  for (int pass = (grp == 1 && DRY) ? 0 : 1; pass < 2; ++pass) {
    const int dead = pass == 0 ? OOB : 0;
    if (pass == 1) lds_wait(L, 2, 8u, 1);
    {
      LAS float* mrg = (LAS float*)(L + L_MRG) + br * 16 * MRG_REC;
      const int d0 = 2 * lane;
      float M = -INFINITY;
#pragma unroll
      for (int ww = 0; ww < 4; ++ww) M = fmaxf(M, mrg[(ww * 4 + w) * MRG_REC + 128]);
      float Ls = 0.f, o0 = 0.f, o1 = 0.f;
#pragma unroll
      for (int ww = 0; ww < 4; ++ww) {
        LAS const float* rec = mrg + (ww * 4 + w) * MRG_REC;
        const float f = __builtin_amdgcn_exp2f(rec[128] - M);
        Ls = fmaf(f, rec[129], Ls);
        o0 = fmaf(f, rec[d0], o0);
        o1 = fmaf(f, rec[d0 + 1], o1);
      }
      const int pso = WS_PG + (br * NWG + wg) * PG_WG + w * PG_HEAD + dead;
      put(o0, o1, pso + lane * 16);
      put(M, Ls, lane == 0 ? pso + 1024 : OOB);
    }
    if (grp == 1) STAMP(6);
    const int gso = WS_PG + (br * NWG + kvh * 4) * PG_WG + qd * PG_HEAD + dead;
    v4u ml = {0u, ~tagA, 0u, 0u}, gr[8];
#pragma unroll
    for (int i = 0; i < 8; ++i) gr[i] = v4u{0u, ~tagA, 0u, 0u};
    {
      u32 okm = 0u, dm = 0u;
      bool few = true;
      for (unsigned spins = 0; spins < SPIN_CAP; ++spins) {
        MEM_FENCE();
        ml = __builtin_amdgcn_raw_buffer_load_b128(rw, h1_mo, gso, AUX_SC1);
        v4u t[8];
        bool want[8];
#pragma unroll
        for (int i = 0; i < 8; ++i) {
          want[i] = !((dm >> i) & 1u) && (few || ((okm >> (4 * i)) & 15u) == 15u);
          t[i] = __builtin_amdgcn_raw_buffer_load_b128(rw, h1_vo, want[i] ? gso + i * 32 * PG_WG + w * 256 : OOB, AUX_SC1);
        }
        SB();
#pragma unroll
        for (int i = 0; i < 8; ++i) gr[i] = want[i] ? t[i] : gr[i];
        okm = (u32)__builtin_amdgcn_ballot_w64(bad4(ml) == 0u);
        dm = 0u;
#pragma unroll
        for (int i = 0; i < 8; ++i) dm |= all_lanes(bad4(gr[i]) == 0u) ? (1u << i) : 0u;
        if ((okm == ~0u && dm == 255u) || pass == 0) break;
        few = __builtin_popcount(~okm) <= 4;
        if (!few) __builtin_amdgcn_s_sleep(POLL_SLEEP);
      }
    }
    if (grp == 1) STAMP(8);
    {
      const float m = u2f(ml.x);
      float M = fmaxf(m, dpp<0xB1>(m));
      M = fmaxf(M, dpp<0x4E>(M));
      const float e = __builtin_amdgcn_exp2f(m - M);
      const float el = e * u2f(ml.z);
      const float Ls = (dpp<0x00>(el) + dpp<0x55>(el)) + (dpp<0xAA>(el) + dpp<0xFF>(el));
      LAS float* cmb = (LAS float*)(L + L_W + br * 16384 + w * 4096);
      LAS float* fl = (LAS float*)(L + L_FL + w8 * 128);
      if (lane < 32) fl[(lane & 3) * 8 + (lane >> 2)] = e / Ls;
      const f32x4 f0 = *(LAS f32x4*)(fl + (lane >> 4) * 8), f1 = *(LAS f32x4*)(fl + (lane >> 4) * 8 + 4);
#pragma unroll
      for (int i = 0; i < 8; ++i) {
        const float f = i < 4 ? f0[i & 3] : f1[i & 3];
        *(LAS f32x2*)(cmb + i * 128 + lane * 2) = f32x2{u2f(gr[i].x) * f, u2f(gr[i].z) * f};
      }
      LAS float* cr = cmb + (lane & 7) * 128 + ((lane >> 3) & 1) * 64 + (lane >> 4) * 8;
      f32x4 a0 = *(LAS f32x4*)cr + *(LAS f32x4*)(cr + 32), a1 = *(LAS f32x4*)(cr + 4) + *(LAS f32x4*)(cr + 36);
#pragma unroll
      for (int j = 0; j < 4; ++j) { a0[j] += dpp<0x128>(a0[j]); a1[j] += dpp<0x128>(a1[j]); }
      proj_put(2 + br, v4u{pack_bf16(a0[0], a0[1]), pack_bf16(a0[2], a0[3]), pack_bf16(a1[0], a1[1]), pack_bf16(a1[2], a1[3])});
      group_barrier();
      const f32x2 r = proj_get(2 + br);
      const int dest = (b * 8 + pb) * 4 + (pn >> 4);
      put(r.x, r.y, WS_OG + br * NWG * 4096 + (4 * kvh + qd) * 128 + dest * 4096 + ((pn & 15) >> 1) * 16 + dead);
      if (pass == 0) group_barrier();
    }
    if (grp == 1) STAMP(9);
    if (w == 0) {
      v4u a[4];
      for (unsigned spins = 0; spins < SPIN_CAP; ++spins) {
        MEM_FENCE();
#pragma unroll
        for (int i = 0; i < 4; ++i) a[i] = __builtin_amdgcn_raw_buffer_load_b128(rw, lane * 16 + i * 1024, WS_OG + (br * NWG + wg) * 4096 + dead, AUX_SC1);
        if (ok4(a) || pass == 0) break;
        __builtin_amdgcn_s_sleep(1);
      }
      float s0 = (u2f(a[0].x) + u2f(a[1].x)) + (u2f(a[2].x) + u2f(a[3].x));
      float s1 = (u2f(a[0].z) + u2f(a[1].z)) + (u2f(a[2].z) + u2f(a[3].z));
      s0 += dpp<0x128>(s0); s1 += dpp<0x128>(s1);
      s0 += __shfl_xor(s0, 16); s1 += __shfl_xor(s1, 16);
      s0 += __shfl_xor(s0, 32); s1 += __shfl_xor(s1, 32);
      if (lane < 8 && pass == 1) ((u32*)p.out)[(kvh * 1024 + br * 512 + b * 64 + qd * 16) / 2 + lane] = pack_bf16(s0, s1);
    }
  }
  if (wg == 0 && w8 == 4 && lane == 0) __builtin_amdgcn_raw_buffer_store_b64(v2u{tagA, tagB}, rw, 0, WS_EPOCH, AUX_SC1);
  if (w8 == 4) STAMP(10);
  if (w8 == 0) STAMP(13);
}

extern "C" void kernel_launch(void* const* d_in, const int* in_sizes, int n_in, void* d_out, int out_size, void* d_ws, size_t ws_size,
                              hipStream_t stream) {
  Params p;
  p.x[0] = d_in[0]; p.k[0] = d_in[1]; p.v[0] = d_in[2]; p.wq[0] = d_in[3]; p.wo[0] = d_in[4];
  p.x[1] = d_in[5]; p.k[1] = d_in[6]; p.v[1] = d_in[7]; p.wq[1] = d_in[8]; p.wo[1] = d_in[9];
  p.out = d_out; p.ws = (unsigned char*)d_ws;
  k_fused<<<NWG, NTHR, 0, stream>>>(p);
}
```

```cpp
#include <hip/hip_runtime.h>
#include <hip/hip_bf16.h>
#include <stdint.h>
#include <utility>

typedef unsigned u32;
typedef u32 v4u __attribute__((ext_vector_type(4)));
typedef u32 v2u __attribute__((ext_vector_type(2)));
typedef float f32x4 __attribute__((ext_vector_type(4)));
typedef float f32x2 __attribute__((ext_vector_type(2)));
typedef __bf16 bf16x8 __attribute__((ext_vector_type(8)));
typedef __bf16 bf16x2 __attribute__((ext_vector_type(2)));
typedef short v4s __attribute__((ext_vector_type(4)));
#define LAS __attribute__((address_space(3)))

#define PB_IDX 99
#define PB_BASE 12000
#define PB_AMP 0
#define NFLY 5
#define NHEAD 3
#define RING 16
#define NWIDE 8
#define TAU 221
#define TBASE 560
#define DRY 0
#define POLL_SLEEP 8
constexpr int TPI = RING / 4, NIT = 32 / TPI;
static_assert(32 % TPI == 0, "");

constexpr int NB = 8, DM = 512, HQ = 32, HKV = 8, SK = 8192, DH = 128;
constexpr int NWG = 256, NTHR = 512;
constexpr int KV_BYTES = NB * HKV * SK * DH * 2;
constexpr float CSC = 0.08838834764831845f * 1.4426950408889634f;
constexpr int AUX_NT = 2, AUX_SC1 = 16;
constexpr unsigned SPIN_CAP = 1u << 17, LSPIN_CAP = 1u << 22;
constexpr int OOB = 0x40000000;

constexpr int WS_EPOCH = 0;
constexpr int WS_QG = 4096;
constexpr int PG_HEAD = 1088, PG_WG = 4 * PG_HEAD;
constexpr int WS_PG = WS_QG + 2 * 8 * 8 * 16384;
constexpr int WS_OG = WS_PG + 2 * NWG * PG_WG;
constexpr int WS_END = WS_OG + 2 * NWG * 4096;
#define STAMP(i) do { } while (0)
#define STAMPV(i, v) do { } while (0)

constexpr int MRG_REC = 132;
constexpr int L_W = 0;
constexpr int L_RED = 65536;
constexpr int L_MRG = L_RED + 32768;
constexpr int L_Q = L_MRG + 2 * 16 * MRG_REC * 4;
constexpr int L_SC = L_Q + 2048;
constexpr int L_FLAG = L_SC + 32768;
constexpr int L_FL = L_FLAG + 64;
constexpr int L_END = L_FL + 1024;

struct Params {
  const void* x[2];
  const void* k[2];
  const void* v[2];
  const void* wq[2];
  const void* wo[2];
  void* out;
  unsigned char* ws;
};

__device__ __forceinline__ f32x4 mfma32(v4u a, v4u b, f32x4 c) {
  return __builtin_amdgcn_mfma_f32_16x16x32_bf16(__builtin_bit_cast(bf16x8, a), __builtin_bit_cast(bf16x8, b), c, 0, 0, 0);
}
__device__ __forceinline__ f32x4 mfma16(v2u a, v2u b, f32x4 c) {
  return __builtin_amdgcn_mfma_f32_16x16x16bf16_1k(__builtin_bit_cast(v4s, a), __builtin_bit_cast(v4s, b), c, 0, 0, 0);
}
__device__ __forceinline__ u32 pack_bf16(float lo, float hi) {
  f32x2 f = {lo, hi};
  return __builtin_bit_cast(u32, __builtin_convertvector(f, bf16x2));
}
__device__ __forceinline__ v2u tr_read(LAS unsigned char* p) {
  return __builtin_bit_cast(v2u, __builtin_amdgcn_ds_read_tr16_b64_v4i16((LAS v4s*)p));
}
__device__ __forceinline__ u32 f2u(float f) { return __builtin_bit_cast(u32, f); }
__device__ __forceinline__ float u2f(u32 u) { return __builtin_bit_cast(float, u); }
template <int CTRL> __device__ __forceinline__ float dpp(float x) {
  return u2f((u32)__builtin_amdgcn_mov_dpp((int)f2u(x), CTRL, 0xf, 0xf, true));
}
#define MEM_FENCE() asm volatile("" ::: "memory")
#define SB() __builtin_amdgcn_sched_barrier(0)
#define WAITVM(n) do { asm volatile("s_waitcnt vmcnt(%0)" :: "i"(n) : "memory"); SB(); } while (0)
#define WAITVM2(x, n, m) do { asm volatile("s_cmp_lg_u32 %0, 0\n\ts_cbranch_scc1 1f\n\ts_waitcnt vmcnt(%1)\n1:\n\ts_waitcnt vmcnt(%2)" :: "s"(x), "i"(n), "i"(m) : "memory", "scc"); SB(); } while (0)
#define AI __attribute__((always_inline))
__device__ __forceinline__ bool all_lanes(bool c) { return __builtin_amdgcn_ballot_w64(c) == ~0ull; }
template <int LO, int... I, class F> __device__ __forceinline__ void static_for_impl(std::integer_sequence<int, I...>, F&& f) {
  (f(std::integral_constant<int, LO + I>{}), ...);
}
template <int LO, int HI, class F> __device__ __forceinline__ void static_for(F&& f) {
  if constexpr (HI > LO) static_for_impl<LO>(std::make_integer_sequence<int, HI - LO>{}, f);
}

__device__ __forceinline__ void lds_arrive(LAS unsigned char* L, int idx, int lane) {
  asm volatile("s_waitcnt lgkmcnt(0)" ::: "memory");
  if (lane == 0) __hip_atomic_fetch_add((LAS u32*)(L + L_FLAG) + idx, 1u, __ATOMIC_RELAXED, __HIP_MEMORY_SCOPE_WORKGROUP);
  MEM_FENCE();
}
__device__ __forceinline__ void lds_wait(LAS unsigned char* L, int idx, u32 want, int sleep) {
  for (unsigned spins = 0; spins < LSPIN_CAP; ++spins) {
    MEM_FENCE();
    const u32 v = __hip_atomic_load((LAS u32*)(L + L_FLAG) + idx, __ATOMIC_RELAXED, __HIP_MEMORY_SCOPE_WORKGROUP);
    if (__builtin_amdgcn_readfirstlane(v) >= want) break;
    if (sleep) __builtin_amdgcn_s_sleep(1);
  }
  MEM_FENCE();
}

__global__ void __launch_bounds__(NTHR, 2) k_fused(Params p) {
  __shared__ __attribute__((aligned(16))) unsigned char smem[L_END];
  LAS unsigned char* L = (LAS unsigned char*)smem;
  const int lane = threadIdx.x & 63, w8 = __builtin_amdgcn_readfirstlane(threadIdx.x >> 6), w = w8 & 3;
  const int wg = blockIdx.x, qd = wg & 3, ub = wg >> 2, b = ub >> 3, kvh = ub & 7;
  const int tid = w * 64 + lane;
  const int grp = w8 >> 2;
  const __amdgpu_buffer_rsrc_t rw = __builtin_amdgcn_make_buffer_rsrc((void*)p.ws, 0, WS_END, 0x00020000);
  const int pb = 4 * ((tid >> 3) & 1) + ((tid >> 4) & 3), pn = 16 * (tid >> 6) + 2 * (tid & 7);
  u32 tagA = 0u, tagB = 0u, gbar = 0u;
  auto bad4 = [&](const v4u& g) AI -> u32 { return (g.y ^ tagA) | (g.w ^ tagB); };
  auto ok4 = [&](const v4u (&g)[4]) AI -> bool { return all_lanes((bad4(g[0]) | bad4(g[1]) | bad4(g[2]) | bad4(g[3])) == 0u); };
  auto put = [&](float v0, float v1, int off) AI { __builtin_amdgcn_raw_buffer_store_b128(v4u{f2u(v0), tagA, f2u(v1), tagB}, rw, off, 0, AUX_SC1); };
  auto proj_put = [&](int r, v4u afrag) AI {
    LAS unsigned char* rb = L + L_W + r * 16384 + w * 4096 + (8 * (lane >> 4) + ((lane >> 2) & 3)) * 128 + (lane & 3) * 8;
    LAS f32x4* red = (LAS f32x4*)(L + L_RED + r * 8192);
    f32x4 acc[4];
#pragma unroll
    for (int ct = 0; ct < 4; ++ct) {
      const v2u lo = tr_read(rb + ct * 32);
      const v2u hi = tr_read(rb + ct * 32 + 512);
      acc[ct] = mfma32(afrag, v4u{lo.x, lo.y, hi.x, hi.y}, f32x4{0.f, 0.f, 0.f, 0.f});
    }
    if (lane < 32) {
#pragma unroll
      for (int ct = 0; ct < 4; ++ct) red[(w * 4 + ct) * 32 + lane] = acc[ct];
    }
  };
  auto group_barrier = [&]() AI {
    lds_arrive(L, 4 + grp, lane);
    gbar += 4u;
    lds_wait(L, 4 + grp, gbar, 0);
  };
  auto proj_get = [&](int r) AI -> f32x2 {
    LAS float* rf = (LAS float*)(L + L_RED + r * 8192) + (w * 32 + 16 * ((tid >> 3) & 1) + 2 * (tid & 7)) * 4 + ((tid >> 4) & 3);
    float s0 = 0.f, s1 = 0.f;
#pragma unroll
    for (int ww = 0; ww < 4; ++ww) { s0 += rf[ww * 512]; s1 += rf[ww * 512 + 4]; }
    return f32x2{s0, s1};
  };

  u32 pt0 = (u32)__builtin_amdgcn_s_memrealtime(), pst = 0;
#define PROBE(i) do { if (PB_IDX == (i)) pst = (u32)__builtin_amdgcn_s_memrealtime() - pt0; } while (0)
  if (grp == 0) {
    const u32 t0 = (u32)__builtin_amdgcn_s_memrealtime();
    STAMP(0);
    STAMPV(14, __builtin_amdgcn_s_getreg((3 << 11) | 20) & 7u);
    const int voffK = ((lane >> 2) & 3) * 256 + (lane >> 4) * 64 + (lane & 3) * 16;
    const int voffV = lane * 16;
    const int sbase = wg * 524288 + w * 4096;
    v4u ring[RING];
    {
      const __amdgpu_buffer_rsrc_t rk = __builtin_amdgcn_make_buffer_rsrc((void*)p.k[0], 0, KV_BYTES, 0x00020000);
      static_for<0, RING>([&](auto I) AI {
        constexpr int i = decltype(I)::value;
        if constexpr (i == NHEAD) __builtin_amdgcn_s_barrier();
        if constexpr (i >= NHEAD) WAITVM(NHEAD - 1);
        ring[i] = __builtin_amdgcn_raw_buffer_load_b128(rk, voffK + (i & 3) * 1024, sbase + (i >> 2) * 16384, AUX_NT);
        SB();
      });
    }
    STAMP(2);
    PROBE(2);
    const int jq = lane & 3;
    const u32 m0 = jq == 0 ? 0x0000ffffu : (jq == 1 ? 0xffff0000u : 0u);
    const u32 m1 = jq == 2 ? 0x0000ffffu : (jq == 3 ? 0xffff0000u : 0u);
    LAS unsigned char* scl = L + L_SC + w * 8192 + (lane >> 2) * 16;
    u32 due = TBASE;
    auto behind = [&]() AI -> int {
      const u32 t = (u32)__builtin_amdgcn_s_memrealtime() - t0;
      const int r = __builtin_amdgcn_readfirstlane(t > due ? 1 : 0);
      due += TAU * RING / 16;
      return r;
    };
    lds_wait(L, 0, 4u, 0);
    STAMP(3);
    PROBE(3);
#pragma unroll 1
    for (int br = 0; br < 2; ++br) {
      const v4u qf = *(LAS v4u*)(L + L_Q + br * 1024 + voffK);
      const void* pk = p.k[br];
      const void* pv = p.v[br];
      float mx = -INFINITY;
#pragma unroll 1
      for (int it = 0; it < NIT; ++it) {
        const bool last = it == NIT - 1;
        const int wide = behind();
        const __amdgpu_buffer_rsrc_t rn = __builtin_amdgcn_make_buffer_rsrc((void*)(last ? pv : pk), 0, KV_BYTES, 0x00020000);
        const int vn = last ? voffV : voffK;
        const int sn = sbase + (last ? 0 : (it + 1) * TPI * 16384);
        f32x4 st;
        static_for<0, RING>([&](auto I) AI {
          constexpr int i = decltype(I)::value;
          const f32x4 d = mfma32(ring[i], qf, f32x4{0.f, 0.f, 0.f, 0.f});
          float s = (dpp<0x00>(d[0]) + dpp<0x55>(d[1])) + (dpp<0xAA>(d[2]) + dpp<0xFF>(d[3]));
          mx = fmaxf(mx, s);
          asm volatile("" : "+v"(s), "+v"(mx));
          st[i & 3] = s;
          if constexpr ((i & 3) == 3) { if (jq == 0) *(LAS f32x4*)(scl + (it * TPI + (i >> 2)) * 256) = st; }
          SB();
          WAITVM2(wide, NFLY - 1, NWIDE - 1);
          ring[i] = __builtin_amdgcn_raw_buffer_load_b128(rn, vn + (i & 3) * 1024, sn + (i >> 2) * 16384, AUX_NT);
          SB();
        });
      }
      mx = fmaxf(mx, __shfl_xor(mx, 16));
      mx = fmaxf(mx, __shfl_xor(mx, 32));
      const float mc = mx * CSC;
      if (br == 0) PROBE(11); else PROBE(12);
      float lsum = 0.f;
      f32x4 accLo = {0.f, 0.f, 0.f, 0.f}, accHi = {0.f, 0.f, 0.f, 0.f};
#pragma unroll 1
      for (int it = 0; it < NIT; ++it) {
        const bool last = it == NIT - 1;
        const int wide = behind();
        const __amdgpu_buffer_rsrc_t rn =
            __builtin_amdgcn_make_buffer_rsrc((void*)(last ? p.k[1] : pv), 0, (last && br == 1) ? 0 : KV_BYTES, 0x00020000);
        const int vn = last ? voffK : voffV;
        const int sn = sbase + (last ? 0 : (it + 1) * TPI * 16384);
        f32x4 sv[TPI];
#pragma unroll
        for (int k = 0; k < TPI; ++k) sv[k] = *(LAS f32x4*)(scl + (it * TPI + k) * 256);
        static_for<0, RING>([&](auto I) AI {
          constexpr int i = decltype(I)::value;
          const float pe = __builtin_amdgcn_exp2f(fmaf(sv[i >> 2][i & 3], CSC, -mc));
          lsum += pe;
          asm volatile("" : "+v"(lsum));
          const u32 pkd = pack_bf16(pe, pe);
          const v2u a = {pkd & m0, pkd & m1};
          accLo = mfma16(a, v2u{ring[i].x, ring[i].y}, accLo);
          accHi = mfma16(a, v2u{ring[i].z, ring[i].w}, accHi);
          SB();
          WAITVM2(wide, NFLY - 1, NWIDE - 1);
          ring[i] = __builtin_amdgcn_raw_buffer_load_b128(rn, vn + (i & 3) * 1024, sn + (i >> 2) * 16384, AUX_NT);
          SB();
        });
      }
      lsum += __shfl_xor(lsum, 16);
      lsum += __shfl_xor(lsum, 32);
      LAS float* mrg = (LAS float*)(L + L_MRG) + br * 16 * MRG_REC;
      LAS float* rec = mrg + (w * 4 + (lane >> 4)) * MRG_REC + 8 * (lane & 15);
      *(LAS f32x4*)rec = accLo;
      *(LAS f32x4*)(rec + 4) = accHi;
      if (lane < 16 && jq == 0) {
        LAS float* r2 = mrg + (w * 4 + (lane >> 2)) * MRG_REC + 128;
        r2[0] = mc; r2[1] = lsum;
      }
      lds_arrive(L, 2, lane);
      if (br == 0) PROBE(4); else PROBE(5);
    }
    tagA = __builtin_amdgcn_readfirstlane(((LAS u32*)(L + L_FLAG))[8]);
    tagB = __builtin_amdgcn_readfirstlane(((LAS u32*)(L + L_FLAG))[9]);
  } else {
    STAMPV(15, 0);
    const v2u ep = __builtin_amdgcn_raw_buffer_load_b64(rw, 0, WS_EPOCH, AUX_SC1);
    const int vo_w = lane & 7, vr_w = lane >> 3;
    v4u wr[4][4], xf[2];
#pragma unroll
    for (int br = 0; br < 2; ++br) {
      const __amdgpu_buffer_rsrc_t rq = __builtin_amdgcn_make_buffer_rsrc((void*)p.wq[br], 0, DM * HQ * DH * 2, 0x00020000);
      const __amdgpu_buffer_rsrc_t rx = __builtin_amdgcn_make_buffer_rsrc((void*)p.x[br], 0, NB * DM * 2, 0x00020000);
      const int so = (128 * qd + 32 * w) * (HQ * DH * 2) + (kvh * 512 + b * 64) * 2;
#pragma unroll
      for (int i = 0; i < 4; ++i) wr[br][i] = __builtin_amdgcn_raw_buffer_load_b128(rq, vr_w * (HQ * DH * 2) + vo_w * 16, so + i * 8 * (HQ * DH * 2), AUX_NT);
      xf[br] = __builtin_amdgcn_raw_buffer_load_b128(rx, (lane & 7) * (DM * 2) + (lane >> 4) * 16, (128 * qd + 32 * w) * 2, 0);
      SB();
    }
    if (w == 0 && lane < 8) ((LAS u32*)(L + L_FLAG))[lane] = 0u;
    asm volatile("s_waitcnt lgkmcnt(0)" ::: "memory");
    __builtin_amdgcn_s_barrier();
    tagA = __builtin_amdgcn_readfirstlane(ep.x) + 1u;
    tagB = __builtin_amdgcn_readfirstlane(ep.y) + 0x9E3779B9u;
    if (lane == 0) { ((LAS u32*)(L + L_FLAG))[8] = tagA; ((LAS u32*)(L + L_FLAG))[9] = tagB; }
    auto stage_w = [&](int r) AI {
      LAS unsigned char* wl = L + L_W + r * 16384 + w * 4096;
#pragma unroll
      for (int i = 0; i < 4; ++i) *(LAS v4u*)(wl + i * 1024 + lane * 16) = wr[r][i];
    };
    stage_w(0);
    proj_put(0, xf[0]);
    stage_w(1);
    proj_put(1, xf[1]);
    group_barrier();
#pragma unroll
    for (int br = 0; br < 2; ++br) {
      const f32x2 r = proj_get(br);
      put(r.x, r.y, WS_QG + (br * 8 + kvh) * 131072 + qd * 4096 + b * 512 + pb * 16384 + (pn >> 1) * 16);
    }
    SB();
    PROBE(1);
#pragma unroll
    for (int br = 0; br < 2; ++br) {
      const __amdgpu_buffer_rsrc_t ro = __builtin_amdgcn_make_buffer_rsrc((void*)p.wo[br], 0, DM * HQ * DH * 2, 0x00020000);
      const int so = (kvh * 512 + 128 * qd + 32 * w) * (DM * 2) + b * 128;
#pragma unroll
      for (int i = 0; i < 4; ++i) wr[2 + br][i] = __builtin_amdgcn_raw_buffer_load_b128(ro, vr_w * (DM * 2) + vo_w * 16, so + i * 8 * (DM * 2), AUX_NT);
      SB();
    }
    stage_w(2);
    SB();
    {
      const int qso = WS_QG + (kvh * 8 + b) * 16384 + w * 1024;
      v4u g[2][4];
      unsigned spins = 0;
      for (;;) {
        MEM_FENCE();
#pragma unroll
        for (int br = 0; br < 2; ++br)
#pragma unroll
          for (int i = 0; i < 4; ++i) g[br][i] = __builtin_amdgcn_raw_buffer_load_b128(rw, lane * 16, qso + br * 8 * 131072 + i * 4096, AUX_SC1);
        if (ok4(g[0]) && ok4(g[1])) break;
        if (++spins > SPIN_CAP) break;
        __builtin_amdgcn_s_sleep(4);
      }
      STAMPV(15, spins);
#pragma unroll
      for (int br = 0; br < 2; ++br)
        *(LAS u32*)(L + L_Q + br * 1024 + w * 256 + lane * 4) = pack_bf16((u2f(g[br][0].x) + u2f(g[br][1].x)) + (u2f(g[br][2].x) + u2f(g[br][3].x)),
                                                                          (u2f(g[br][0].z) + u2f(g[br][1].z)) + (u2f(g[br][2].z) + u2f(g[br][3].z)));
      stage_w(3);
      lds_arrive(L, 0, lane);
    }
  }

  const int br = grp;
  const int h1_mo = (((lane >> 2) & 7) * 32 + (lane & 3)) * PG_WG + 1024;
  const int h1_vo = (lane >> 4) * PG_WG + (lane & 15) * 16;
#pragma unroll 1
  for (int pass = (grp == 1 && DRY) ? 0 : 1; pass < 2; ++pass) {
    const int dead = pass == 0 ? OOB : 0;
    if (pass == 1) lds_wait(L, 2, 8u, 1);
    {
      LAS float* mrg = (LAS float*)(L + L_MRG) + br * 16 * MRG_REC;
      const int d0 = 2 * lane;
      float M = -INFINITY;
#pragma unroll
      for (int ww = 0; ww < 4; ++ww) M = fmaxf(M, mrg[(ww * 4 + w) * MRG_REC + 128]);
      float Ls = 0.f, o0 = 0.f, o1 = 0.f;
#pragma unroll
      for (int ww = 0; ww < 4; ++ww) {
        LAS const float* rec = mrg + (ww * 4 + w) * MRG_REC;
        const float f = __builtin_amdgcn_exp2f(rec[128] - M);
        Ls = fmaf(f, rec[129], Ls);
        o0 = fmaf(f, rec[d0], o0);
        o1 = fmaf(f, rec[d0 + 1], o1);
      }
      const int pso = WS_PG + (br * NWG + wg) * PG_WG + w * PG_HEAD + dead;
      put(o0, o1, pso + lane * 16);
      put(M, Ls, lane == 0 ? pso + 1024 : OOB);
    }
    if (grp == 1) STAMP(6);
    const int gso = WS_PG + (br * NWG + kvh * 4) * PG_WG + qd * PG_HEAD + dead;
    v4u ml = {0u, ~tagA, 0u, 0u}, gr[8];
#pragma unroll
    for (int i = 0; i < 8; ++i) gr[i] = v4u{0u, ~tagA, 0u, 0u};
    {
      u32 okm = 0u, dm = 0u;
      bool few = true;
      for (unsigned spins = 0; spins < SPIN_CAP; ++spins) {
        MEM_FENCE();
        ml = __builtin_amdgcn_raw_buffer_load_b128(rw, h1_mo, gso, AUX_SC1);
        v4u t[8];
        bool want[8];
#pragma unroll
        for (int i = 0; i < 8; ++i) {
          want[i] = !((dm >> i) & 1u) && (few || ((okm >> (4 * i)) & 15u) == 15u);
          t[i] = __builtin_amdgcn_raw_buffer_load_b128(rw, h1_vo, want[i] ? gso + i * 32 * PG_WG + w * 256 : OOB, AUX_SC1);
        }
        SB();
#pragma unroll
        for (int i = 0; i < 8; ++i) gr[i] = want[i] ? t[i] : gr[i];
        okm = (u32)__builtin_amdgcn_ballot_w64(bad4(ml) == 0u);
        dm = 0u;
#pragma unroll
        for (int i = 0; i < 8; ++i) dm |= all_lanes(bad4(gr[i]) == 0u) ? (1u << i) : 0u;
        if ((okm == ~0u && dm == 255u) || pass == 0) break;
        few = __builtin_popcount(~okm) <= 4;
        if (!few) __builtin_amdgcn_s_sleep(POLL_SLEEP);
      }
    }
    if (grp == 1) STAMP(8);
    {
      const float m = u2f(ml.x);
      float M = fmaxf(m, dpp<0xB1>(m));
      M = fmaxf(M, dpp<0x4E>(M));
      const float e = __builtin_amdgcn_exp2f(m - M);
      const float el = e * u2f(ml.z);
      const float Ls = (dpp<0x00>(el) + dpp<0x55>(el)) + (dpp<0xAA>(el) + dpp<0xFF>(el));
      LAS float* cmb = (LAS float*)(L + L_W + br * 16384 + w * 4096);
      LAS float* fl = (LAS float*)(L + L_FL + w8 * 128);
      if (lane < 32) fl[(lane & 3) * 8 + (lane >> 2)] = e / Ls;
      const f32x4 f0 = *(LAS f32x4*)(fl + (lane >> 4) * 8), f1 = *(LAS f32x4*)(fl + (lane >> 4) * 8 + 4);
#pragma unroll
      for (int i = 0; i < 8; ++i) {
        const float f = i < 4 ? f0[i & 3] : f1[i & 3];
        *(LAS f32x2*)(cmb + i * 128 + lane * 2) = f32x2{u2f(gr[i].x) * f, u2f(gr[i].z) * f};
      }
      LAS float* cr = cmb + (lane & 7) * 128 + ((lane >> 3) & 1) * 64 + (lane >> 4) * 8;
      f32x4 a0 = *(LAS f32x4*)cr + *(LAS f32x4*)(cr + 32), a1 = *(LAS f32x4*)(cr + 4) + *(LAS f32x4*)(cr + 36);
#pragma unroll
      for (int j = 0; j < 4; ++j) { a0[j] += dpp<0x128>(a0[j]); a1[j] += dpp<0x128>(a1[j]); }
      proj_put(2 + br, v4u{pack_bf16(a0[0], a0[1]), pack_bf16(a0[2], a0[3]), pack_bf16(a1[0], a1[1]), pack_bf16(a1[2], a1[3])});
      group_barrier();
      const f32x2 r = proj_get(2 + br);
      const int dest = (b * 8 + pb) * 4 + (pn >> 4);
      put(r.x, r.y, WS_OG + br * NWG * 4096 + (4 * kvh + qd) * 128 + dest * 4096 + ((pn & 15) >> 1) * 16 + dead);
      if (pass == 0) group_barrier();
    }
    if (grp == 1) STAMP(9);
    if (w == 0) {
      v4u a[4];
      for (unsigned spins = 0; spins < SPIN_CAP; ++spins) {
        MEM_FENCE();
#pragma unroll
        for (int i = 0; i < 4; ++i) a[i] = __builtin_amdgcn_raw_buffer_load_b128(rw, lane * 16 + i * 1024, WS_OG + (br * NWG + wg) * 4096 + dead, AUX_SC1);
        if (ok4(a) || pass == 0) break;
        __builtin_amdgcn_s_sleep(1);
      }
      float s0 = (u2f(a[0].x) + u2f(a[1].x)) + (u2f(a[2].x) + u2f(a[3].x));
      float s1 = (u2f(a[0].z) + u2f(a[1].z)) + (u2f(a[2].z) + u2f(a[3].z));
      s0 += dpp<0x128>(s0); s1 += dpp<0x128>(s1);
      s0 += __shfl_xor(s0, 16); s1 += __shfl_xor(s1, 16);
      s0 += __shfl_xor(s0, 32); s1 += __shfl_xor(s1, 32);
      if (lane < 8 && pass == 1) ((u32*)p.out)[(kvh * 1024 + br * 512 + b * 64 + qd * 16) / 2 + lane] = pack_bf16(s0, s1);
    }
  }
  if (wg == 0 && w8 == 4 && lane == 0) __builtin_amdgcn_raw_buffer_store_b64(v2u{tagA, tagB}, rw, 0, WS_EPOCH, AUX_SC1);
  if (w8 == 4) STAMP(10);
  PROBE(13);
  { const u32 tgt = PB_BASE + PB_AMP * pst; for (unsigned s = 0; s < (1u << 20); ++s) { if ((u32)__builtin_amdgcn_s_memrealtime() - pt0 >= tgt) break; __builtin_amdgcn_s_sleep(1); } }
}

extern "C" void kernel_launch(void* const* d_in, const int* in_sizes, int n_in, void* d_out, int out_size, void* d_ws, size_t ws_size,
                              hipStream_t stream) {
  Params p;
  p.x[0] = d_in[0]; p.k[0] = d_in[1]; p.v[0] = d_in[2]; p.wq[0] = d_in[3]; p.wo[0] = d_in[4];
  p.x[1] = d_in[5]; p.k[1] = d_in[6]; p.v[1] = d_in[7]; p.wq[1] = d_in[8]; p.wo[1] = d_in[9];
  p.out = d_out; p.ws = (unsigned char*)d_ws;
  k_fused<<<NWG, NTHR, 0, stream>>>(p);
}
```

```cpp
#include <hip/hip_runtime.h>
#include <hip/hip_bf16.h>
#include <stdint.h>
#include <utility>

typedef unsigned u32;
typedef u32 v4u __attribute__((ext_vector_type(4)));
typedef u32 v2u __attribute__((ext_vector_type(2)));
typedef float f32x4 __attribute__((ext_vector_type(4)));
typedef float f32x2 __attribute__((ext_vector_type(2)));
typedef __bf16 bf16x8 __attribute__((ext_vector_type(8)));
typedef __bf16 bf16x2 __attribute__((ext_vector_type(2)));
typedef short v4s __attribute__((ext_vector_type(4)));
#define LAS __attribute__((address_space(3)))

#define NFLY 5
#define NHEAD 3
#define RING 16
#define NWIDE 6
#define TAU 217
#define TBASE 510
#define DRY 0
#define FIRST_ALL 0
#define FEW 4
#define Q_WAIT 32
#define EARLY_A 1
#define H2_WAIT 16
#define H2_SLEEP 2
#define POLL_SLEEP 4
#define NITX NIT
constexpr int TPI = RING / 4, NIT = 32 / TPI;
static_assert(32 % TPI == 0, "");

constexpr int NB = 8, DM = 512, HQ = 32, HKV = 8, SK = 8192, DH = 128;
constexpr int NWG = 256, NTHR = 512;
constexpr int KV_BYTES = NB * HKV * SK * DH * 2;
constexpr float CSC = 0.08838834764831845f * 1.4426950408889634f;
constexpr int AUX_NT = 2, AUX_SC1 = 16;
constexpr unsigned SPIN_CAP = 1u << 17, LSPIN_CAP = 1u << 22;
constexpr int OOB = 0x40000000;

constexpr int WS_EPOCH = 0;
constexpr int WS_QG = 4096;
constexpr int PG_HEAD = 512, PG_WG = 4 * PG_HEAD;
constexpr int WS_PG = WS_QG + 2 * 8 * 8 * 8192;
constexpr int WS_ML = WS_PG + 2 * NWG * PG_WG;
constexpr int WS_OG = WS_ML + 2 * 8 * 4 * 512;
constexpr int WS_END = WS_OG + 2 * NWG * 4096;
#define STAMP(i) do { } while (0)
#define STAMPV(i, v) do { } while (0)

constexpr int MRG_REC = 132;
constexpr int L_W = 0;
constexpr int L_RED = 65536;
constexpr int L_MRG = L_RED + 32768;
constexpr int L_Q = L_MRG + 2 * 16 * MRG_REC * 4;
constexpr int L_SC = L_Q + 2048;
constexpr int L_FLAG = L_SC + 32768;
constexpr int L_O = L_FLAG + 64;
constexpr int L_END = L_O + 2048;

struct Params {
  const void* x[2];
  const void* k[2];
  const void* v[2];
  const void* wq[2];
  const void* wo[2];
  void* out;
  unsigned char* ws;
};

__device__ __forceinline__ f32x4 mfma32(v4u a, v4u b, f32x4 c) {
  return __builtin_amdgcn_mfma_f32_16x16x32_bf16(__builtin_bit_cast(bf16x8, a), __builtin_bit_cast(bf16x8, b), c, 0, 0, 0);
}
__device__ __forceinline__ f32x4 mfma16(v2u a, v2u b, f32x4 c) {
  return __builtin_amdgcn_mfma_f32_16x16x16bf16_1k(__builtin_bit_cast(v4s, a), __builtin_bit_cast(v4s, b), c, 0, 0, 0);
}
__device__ __forceinline__ u32 pack_bf16(float lo, float hi) {
  f32x2 f = {lo, hi};
  return __builtin_bit_cast(u32, __builtin_convertvector(f, bf16x2));
}
__device__ __forceinline__ v2u tr_read(LAS unsigned char* p) {
  return __builtin_bit_cast(v2u, __builtin_amdgcn_ds_read_tr16_b64_v4i16((LAS v4s*)p));
}
__device__ __forceinline__ u32 f2u(float f) { return __builtin_bit_cast(u32, f); }
__device__ __forceinline__ float u2f(u32 u) { return __builtin_bit_cast(float, u); }
template <int CTRL> __device__ __forceinline__ float dpp(float x) {
  return u2f((u32)__builtin_amdgcn_mov_dpp((int)f2u(x), CTRL, 0xf, 0xf, true));
}
__device__ __forceinline__ f32x4 unpack4(v4u g) {
  return f32x4{u2f(g.x << 12), u2f(((g.x >> 20) << 12) | (g.y << 24)), u2f(g.z << 12), u2f(((g.z >> 20) << 12) | (g.w << 24))};
}
__device__ __forceinline__ float red32(float a, float b) {
  const auto s = __builtin_amdgcn_permlane32_swap(f2u(a), f2u(b), false, false);
  return u2f(s[0]) + u2f(s[1]);
}
__device__ __forceinline__ float red16(float a, float b) {
  const auto s = __builtin_amdgcn_permlane16_swap(f2u(a), f2u(b), false, false);
  return u2f(s[0]) + u2f(s[1]);
}
#define MEM_FENCE() asm volatile("" ::: "memory")
#define SB() __builtin_amdgcn_sched_barrier(0)
#define WAITVM(n) do { asm volatile("s_waitcnt vmcnt(%0)" :: "i"(n) : "memory"); SB(); } while (0)
#define WAITVM2(x, y, n, m) do { asm volatile("s_cmp_gt_u32 %0, %1\n\ts_cbranch_scc1 1f\n\ts_waitcnt vmcnt(%2)\n1:\n\ts_waitcnt vmcnt(%3)" :: "s"(x), "s"(y), "i"(n), "i"(m) : "memory", "scc"); SB(); } while (0)
#define AI __attribute__((always_inline))
__device__ __forceinline__ bool all_lanes(bool c) { return __builtin_amdgcn_ballot_w64(c) == ~0ull; }
template <int LO, int... I, class F> __device__ __forceinline__ void static_for_impl(std::integer_sequence<int, I...>, F&& f) {
  (f(std::integral_constant<int, LO + I>{}), ...);
}
template <int LO, int HI, class F> __device__ __forceinline__ void static_for(F&& f) {
  if constexpr (HI > LO) static_for_impl<LO>(std::make_integer_sequence<int, HI - LO>{}, f);
}

__device__ __forceinline__ void lds_arrive(LAS unsigned char* L, int idx, int lane) {
  asm volatile("s_waitcnt lgkmcnt(0)" ::: "memory");
  if (lane == 0) __hip_atomic_fetch_add((LAS u32*)(L + L_FLAG) + idx, 1u, __ATOMIC_RELAXED, __HIP_MEMORY_SCOPE_WORKGROUP);
  MEM_FENCE();
}
__device__ __forceinline__ void lds_wait(LAS unsigned char* L, int idx, u32 want, int sleep) {
  for (unsigned spins = 0; spins < LSPIN_CAP; ++spins) {
    MEM_FENCE();
    const u32 v = __hip_atomic_load((LAS u32*)(L + L_FLAG) + idx, __ATOMIC_RELAXED, __HIP_MEMORY_SCOPE_WORKGROUP);
    if (__builtin_amdgcn_readfirstlane(v) >= want) break;
    if (sleep) __builtin_amdgcn_s_sleep(1);
  }
  MEM_FENCE();
}

__global__ void __launch_bounds__(NTHR, 2) k_fused(Params p) {
  __shared__ __attribute__((aligned(16))) unsigned char smem[L_END];
  LAS unsigned char* L = (LAS unsigned char*)smem;
  const int lane = threadIdx.x & 63, w8 = __builtin_amdgcn_readfirstlane(threadIdx.x >> 6), w = w8 & 3;
  const int wg = blockIdx.x, qd = wg & 3, ub = wg >> 2, b = ub >> 3, kvh = ub & 7;
  const int ks = (b & 1) * 4 + qd;
  const int tid = w * 64 + lane;
  const int grp = w8 >> 2;
  const __amdgpu_buffer_rsrc_t rw = __builtin_amdgcn_make_buffer_rsrc((void*)p.ws, 0, WS_END, 0x00020000);
  const int pb = 4 * ((tid >> 3) & 1) + ((tid >> 4) & 3), pn = 16 * (tid >> 6) + 2 * (tid & 7);
  u32 tagA = 0u, tagB = 0u, gbar = 0u;
  auto bad4 = [&](const v4u& g) AI -> u32 { return (g.y ^ tagA) | (g.w ^ tagB); };
  auto badp = [&](const v4u& g) AI -> u32 { return ((g.y ^ (tagA << 8)) | (g.w ^ (tagB << 8))) >> 8; };
  auto put = [&](float v0, float v1, int off) AI { __builtin_amdgcn_raw_buffer_store_b128(v4u{f2u(v0), tagA, f2u(v1), tagB}, rw, off, 0, AUX_SC1); };
  auto packh = [&](float v0, float v1, int off) AI -> v2u {
    const u32 a = f2u(v0) + 0x800u, c = f2u(v1) + 0x800u, ta = tagA << 8, tb = tagB << 8;
    return v2u{(a >> 12) | ((c << 8) & 0xFFF00000u), (c >> 24) | ((off & 8) ? tb : ta)};
  };
  auto puth = [&](float v0, float v1, int off) AI { __builtin_amdgcn_raw_buffer_store_b64(packh(v0, v1, off), rw, off, 0, AUX_SC1); };
  auto proj_put = [&](int r, v4u afrag) AI {
    LAS unsigned char* rb = L + L_W + r * 16384 + w * 4096 + (8 * (lane >> 4) + ((lane >> 2) & 3)) * 128 + (lane & 3) * 8;
    LAS f32x4* red = (LAS f32x4*)(L + L_RED + r * 8192);
    f32x4 acc[4];
#pragma unroll
    for (int ct = 0; ct < 4; ++ct) {
      const v2u lo = tr_read(rb + ct * 32);
      const v2u hi = tr_read(rb + ct * 32 + 512);
      acc[ct] = mfma32(afrag, v4u{lo.x, lo.y, hi.x, hi.y}, f32x4{0.f, 0.f, 0.f, 0.f});
    }
    if (lane < 32) {
#pragma unroll
      for (int ct = 0; ct < 4; ++ct) red[(w * 4 + ct) * 32 + lane] = acc[ct];
    }
  };
  auto group_barrier = [&]() AI {
    lds_arrive(L, 4 + grp, lane);
    gbar += 4u;
    lds_wait(L, 4 + grp, gbar, 0);
  };
  auto proj_get = [&](int r) AI -> f32x2 {
    LAS float* rf = (LAS float*)(L + L_RED + r * 8192) + (w * 32 + 16 * ((tid >> 3) & 1) + 2 * (tid & 7)) * 4 + ((tid >> 4) & 3);
    float s0 = 0.f, s1 = 0.f;
#pragma unroll
    for (int ww = 0; ww < 4; ++ww) { s0 += rf[ww * 512]; s1 += rf[ww * 512 + 4]; }
    return f32x2{s0, s1};
  };

  if (grp == 0) {
    const u32 t0 = (u32)__builtin_amdgcn_s_memrealtime();
    STAMP(0);
    STAMPV(14, __builtin_amdgcn_s_getreg((3 << 11) | 20) & 7u);
    const int voffK = ((lane >> 2) & 3) * 256 + (lane >> 4) * 64 + (lane & 3) * 16;
    const int voffV = lane * 16;
    const int sbase = wg * 524288 + w * 4096;
    v4u ring[RING];
    {
      const __amdgpu_buffer_rsrc_t rk = __builtin_amdgcn_make_buffer_rsrc((void*)p.k[0], 0, KV_BYTES, 0x00020000);
      static_for<0, RING>([&](auto I) AI {
        constexpr int i = decltype(I)::value;
        if constexpr (i == NHEAD) __builtin_amdgcn_s_barrier();
        if constexpr (i >= NHEAD) WAITVM(NHEAD - 1);
        ring[i] = __builtin_amdgcn_raw_buffer_load_b128(rk, voffK + (i & 3) * 1024, sbase + (i >> 2) * 16384, AUX_NT);
        SB();
      });
    }
    STAMP(2);
    const int jq = lane & 3;
    const u32 m0 = jq == 0 ? 0x0000ffffu : (jq == 1 ? 0xffff0000u : 0u);
    const u32 m1 = jq == 2 ? 0x0000ffffu : (jq == 3 ? 0xffff0000u : 0u);
    LAS unsigned char* scl = L + L_SC + w * 8192 + (lane >> 2) * 16;
    u32 due = TBASE - TAU * RING / 16;
    lds_wait(L, 0, 4u, 0);
    STAMP(3);
#pragma unroll 1
    for (int br = 0; br < 2; ++br) {
      const v4u qf = *(LAS v4u*)(L + L_Q + br * 1024 + voffK);
      const void* pk = p.k[br];
      const void* pv = p.v[br];
      float mx = -INFINITY;
#pragma unroll 1
      for (int it = 0; it < NITX; ++it) {
        const bool last = it == NIT - 1;
        const u32 now = (u32)__builtin_amdgcn_s_memrealtime() - t0;
        due += TAU * RING / 16;
        const __amdgpu_buffer_rsrc_t rn = __builtin_amdgcn_make_buffer_rsrc((void*)(last ? pv : pk), 0, KV_BYTES, 0x00020000);
        const int vn = last ? voffV : voffK;
        const int sn = sbase + (last ? 0 : (it + 1) * TPI * 16384);
        f32x4 st;
        static_for<0, RING>([&](auto I) AI {
          constexpr int i = decltype(I)::value;
          const f32x4 d = mfma32(ring[i], qf, f32x4{0.f, 0.f, 0.f, 0.f});
          float s = (dpp<0x00>(d[0]) + dpp<0x55>(d[1])) + (dpp<0xAA>(d[2]) + dpp<0xFF>(d[3]));
          mx = fmaxf(mx, s);
          asm volatile("" : "+v"(s), "+v"(mx));
          st[i & 3] = s;
          if constexpr ((i & 3) == 3) { if (jq == 0) *(LAS f32x4*)(scl + (it * TPI + (i >> 2)) * 256) = st; }
          SB();
          WAITVM2(now, due, NFLY - 1, NWIDE - 1);
          ring[i] = __builtin_amdgcn_raw_buffer_load_b128(rn, vn + (i & 3) * 1024, sn + (i >> 2) * 16384, AUX_NT);
          SB();
        });
      }
      mx = fmaxf(mx, __shfl_xor(mx, 16));
      mx = fmaxf(mx, __shfl_xor(mx, 32));
      const float mc = mx * CSC;
      if (br == 0) STAMP(11); else STAMP(12);
      float lsum = 0.f;
      f32x4 accLo = {0.f, 0.f, 0.f, 0.f}, accHi = {0.f, 0.f, 0.f, 0.f};
#pragma unroll 1
      for (int it = 0; it < NITX; ++it) {
        const bool last = it == NIT - 1;
        const u32 now = (u32)__builtin_amdgcn_s_memrealtime() - t0;
        due += TAU * RING / 16;
        const __amdgpu_buffer_rsrc_t rn =
            __builtin_amdgcn_make_buffer_rsrc((void*)(last ? p.k[1] : pv), 0, (last && br == 1) ? 0 : KV_BYTES, 0x00020000);
        const int vn = last ? voffK : voffV;
        const int sn = sbase + (last ? 0 : (it + 1) * TPI * 16384);
        if (last && br == 1) lds_arrive(L, 1, lane);
        f32x4 sv[TPI];
#pragma unroll
        for (int k = 0; k < TPI; ++k) sv[k] = *(LAS f32x4*)(scl + (it * TPI + k) * 256);
        static_for<0, RING>([&](auto I) AI {
          constexpr int i = decltype(I)::value;
          const float pe = __builtin_amdgcn_exp2f(fmaf(sv[i >> 2][i & 3], CSC, -mc));
          lsum += pe;
          asm volatile("" : "+v"(lsum));
          const u32 pkd = pack_bf16(pe, pe);
          const v2u a = {pkd & m0, pkd & m1};
          accLo = mfma16(a, v2u{ring[i].x, ring[i].y}, accLo);
          accHi = mfma16(a, v2u{ring[i].z, ring[i].w}, accHi);
          SB();
          WAITVM2(now, due, NFLY - 1, NWIDE - 1);
          ring[i] = __builtin_amdgcn_raw_buffer_load_b128(rn, vn + (i & 3) * 1024, sn + (i >> 2) * 16384, AUX_NT);
          SB();
        });
      }
      lsum += __shfl_xor(lsum, 16);
      lsum += __shfl_xor(lsum, 32);
      LAS float* mrg = (LAS float*)(L + L_MRG) + br * 16 * MRG_REC;
      LAS float* rec = mrg + (w * 4 + (lane >> 4)) * MRG_REC + 8 * (lane & 15);
      *(LAS f32x4*)rec = accLo;
      *(LAS f32x4*)(rec + 4) = accHi;
      if (lane < 16 && jq == 0) {
        LAS float* r2 = mrg + (w * 4 + (lane >> 2)) * MRG_REC + 128;
        r2[0] = mc; r2[1] = lsum;
      }
      lds_arrive(L, 2 + br, lane);
      if (br == 0) STAMP(4); else STAMP(5);
    }
    tagA = __builtin_amdgcn_readfirstlane(((LAS u32*)(L + L_FLAG))[8]);
    tagB = __builtin_amdgcn_readfirstlane(((LAS u32*)(L + L_FLAG))[9]);
  } else {
    STAMPV(15, 0);
    const v2u ep = __builtin_amdgcn_raw_buffer_load_b64(rw, 0, WS_EPOCH, AUX_SC1);
    const int vo_w = lane & 7, vr_w = lane >> 3;
    v4u wr[2][4], xf[2];
#pragma unroll
    for (int br = 0; br < 2; ++br) {
      const __amdgpu_buffer_rsrc_t rq = __builtin_amdgcn_make_buffer_rsrc((void*)p.wq[br], 0, DM * HQ * DH * 2, 0x00020000);
      const __amdgpu_buffer_rsrc_t rx = __builtin_amdgcn_make_buffer_rsrc((void*)p.x[br], 0, NB * DM * 2, 0x00020000);
      const int so = (128 * qd + 32 * w) * (HQ * DH * 2) + (kvh * 512 + b * 64) * 2;
#pragma unroll
      for (int i = 0; i < 4; ++i) wr[br][i] = __builtin_amdgcn_raw_buffer_load_b128(rq, vr_w * (HQ * DH * 2) + vo_w * 16, so + i * 8 * (HQ * DH * 2), AUX_NT);
      xf[br] = __builtin_amdgcn_raw_buffer_load_b128(rx, (lane & 7) * (DM * 2) + (lane >> 4) * 16, (128 * qd + 32 * w) * 2, 0);
      SB();
    }
    if (w == 0 && lane < 8) ((LAS u32*)(L + L_FLAG))[lane] = 0u;
    asm volatile("s_waitcnt lgkmcnt(0)" ::: "memory");
    __builtin_amdgcn_s_barrier();
    tagA = __builtin_amdgcn_readfirstlane(ep.x) + 1u;
    tagB = __builtin_amdgcn_readfirstlane(ep.y) + 0x9E3779B9u;
    if (lane == 0) { ((LAS u32*)(L + L_FLAG))[8] = tagA; ((LAS u32*)(L + L_FLAG))[9] = tagB; }
    auto stage_w = [&](int r) AI {
      LAS unsigned char* wl = L + L_W + r * 16384 + w * 4096;
#pragma unroll
      for (int i = 0; i < 4; ++i) *(LAS v4u*)(wl + i * 1024 + lane * 16) = wr[r][i];
    };
    stage_w(0);
    proj_put(0, xf[0]);
    stage_w(1);
    proj_put(1, xf[1]);
    group_barrier();
#pragma unroll
    for (int br = 0; br < 2; ++br) {
      const f32x2 r = proj_get(br);
      puth(r.x, r.y, WS_QG + (br * 8 + kvh) * 65536 + qd * 2048 + b * 256 + pb * 8192 + (pn >> 1) * 8);
    }
    SB();
    STAMP(1);
    __builtin_amdgcn_s_sleep(Q_WAIT);
    SB();
    {
      const int qvo = WS_QG + (kvh * 8 + b) * 8192 + w * 512 + (lane >> 5) * 2048 + (lane & 31) * 16;
      v4u g[2][2];
      unsigned spins = 0;
      for (;;) {
        MEM_FENCE();
#pragma unroll
        for (int br = 0; br < 2; ++br)
#pragma unroll
          for (int i = 0; i < 2; ++i) g[br][i] = __builtin_amdgcn_raw_buffer_load_b128(rw, qvo, br * 8 * 65536 + i * 4096, AUX_SC1);
        if (all_lanes((badp(g[0][0]) | badp(g[0][1]) | badp(g[1][0]) | badp(g[1][1])) == 0u)) break;
        if (++spins > SPIN_CAP) break;
        __builtin_amdgcn_s_sleep(4);
      }
      STAMPV(15, spins);
#pragma unroll
      for (int br = 0; br < 2; ++br) {
        const f32x4 s = unpack4(g[br][0]) + unpack4(g[br][1]);
        const float u0 = red32(s[0], s[2]), u1 = red32(s[1], s[3]);
        *(LAS u32*)(L + L_Q + br * 1024 + w * 256 + (lane & 31) * 8 + (lane >> 5) * 4) = pack_bf16(u0, u1);
      }
      lds_arrive(L, 0, lane);
    }
  }

  const int br = 1 - grp;
  lds_wait(L, 2 + br, 4u, grp);
  {
    LAS float* mrg = (LAS float*)(L + L_MRG) + br * 16 * MRG_REC;
    const int d0 = 2 * lane;
    float M = -INFINITY;
#pragma unroll
    for (int ww = 0; ww < 4; ++ww) M = fmaxf(M, mrg[(ww * 4 + w) * MRG_REC + 128]);
    float Ls = 0.f, o0 = 0.f, o1 = 0.f;
#pragma unroll
    for (int ww = 0; ww < 4; ++ww) {
      LAS const float* rec = mrg + (ww * 4 + w) * MRG_REC;
      const float f = __builtin_amdgcn_exp2f(rec[128] - M);
      Ls = fmaf(f, rec[129], Ls);
      o0 = fmaf(f, rec[d0], o0);
      o1 = fmaf(f, rec[d0 + 1], o1);
    }
    const int po = WS_PG + (br * NWG + wg) * PG_WG + w * PG_HEAD + lane * 8;
    v2u ph = packh(o0, o1, po);
    v4u wo4[2][4];
    if (grp == 1) {
      asm volatile("" : "+v"(ph), "+v"(M), "+v"(Ls));
      lds_wait(L, 1, 4u, 1);
#pragma unroll
      for (int bq = 0; bq < 2; ++bq) {
        const __amdgpu_buffer_rsrc_t ro = __builtin_amdgcn_make_buffer_rsrc((void*)p.wo[bq], 0, DM * HQ * DH * 2, 0x00020000);
        const int so = (kvh * 512 + 64 * ks + 16 * w) * (DM * 2) + (b >> 1) * 256;
#pragma unroll
        for (int i = 0; i < 4; ++i) wo4[bq][i] = __builtin_amdgcn_raw_buffer_load_b128(ro, (lane >> 4) * (DM * 2) + (lane & 15) * 16, so + i * 4 * (DM * 2), AUX_NT);
      }
      SB();
      if (!EARLY_A) lds_wait(L, 3, 4u, 1);
    }
    __builtin_amdgcn_raw_buffer_store_b64(ph, rw, po, 0, AUX_SC1);
    put(M, Ls, lane == 0 ? WS_ML + ((br * 8 + kvh) * 4 + w) * 512 + (b * 4 + qd) * 16 : OOB);
    if (grp == 1) {
#pragma unroll
      for (int bq = 0; bq < 2; ++bq)
#pragma unroll
        for (int i = 0; i < 4; ++i) *(LAS v4u*)(L + L_W + (2 + bq) * 16384 + w * 4096 + i * 1024 + lane * 16) = wo4[bq][i];
      lds_arrive(L, 6, lane);
    }
    lds_wait(L, 6, 4u, 0);
  }
  if (grp == 0) STAMP(6);
  v4u bf[2][2];
  {
    LAS unsigned char* rb = L + L_W + (2 + br) * 16384 + (8 * (lane >> 4) + ((lane >> 2) & 3)) * 256 + w * 64 + (lane & 3) * 8;
#pragma unroll
    for (int s = 0; s < 2; ++s)
#pragma unroll
      for (int c = 0; c < 2; ++c) {
        const v2u lo = tr_read(rb + s * 8192 + c * 32), hi = tr_read(rb + s * 8192 + c * 32 + 1024);
        bf[s][c] = v4u{lo.x, lo.y, hi.x, hi.y};
      }
  }
  v4u ml = {0u, ~tagA, 0u, 0u}, gr[2];
  {
    const int mo = WS_ML + ((br * 8 + kvh) * 4 + (ks >> 1)) * 512 + (w * 8 + (lane & 7)) * 16;
    const int vo = WS_PG + (br * NWG + (2 * w * 8 + kvh) * 4 + (lane >> 4)) * PG_WG + (ks >> 1) * PG_HEAD + (ks & 1) * 256 + (lane & 15) * 16;
#pragma unroll
    for (int j = 0; j < 2; ++j) gr[j] = v4u{0u, ~(tagA << 8), 0u, 0u};
    bool few = FIRST_ALL;
    u32 okm = 0u;
    for (unsigned spins = 0; spins < SPIN_CAP; ++spins) {
      MEM_FENCE();
      const v4u tm = __builtin_amdgcn_raw_buffer_load_b128(rw, bad4(ml) != 0u ? mo : OOB, 0, AUX_SC1);
      v4u t[2];
      bool want[2];
#pragma unroll
      for (int j = 0; j < 2; ++j) {
        want[j] = badp(gr[j]) != 0u && (few || ((okm >> (4 * j + (lane >> 4))) & 1u));
        t[j] = __builtin_amdgcn_raw_buffer_load_b128(rw, want[j] ? vo + j * 32 * PG_WG : OOB, 0, AUX_SC1);
      }
      SB();
      ml = bad4(ml) != 0u ? tm : ml;
      u32 bad = bad4(ml);
      okm = (u32)__builtin_amdgcn_ballot_w64(bad == 0u) & 255u;
#pragma unroll
      for (int j = 0; j < 2; ++j) { gr[j] = want[j] ? t[j] : gr[j]; bad |= badp(gr[j]); }
      if (all_lanes(bad == 0u)) break;
      few = __builtin_popcount(okm) >= 8 - FEW;
      if (!few) __builtin_amdgcn_s_sleep(POLL_SLEEP);
    }
  }
  if (grp == 0) STAMP(8);
  {
    const float m = u2f(ml.x);
    float M = fmaxf(m, dpp<0xB1>(m));
    M = fmaxf(M, dpp<0x4E>(M));
    const float e = __builtin_amdgcn_exp2f(m - M);
    const float el = e * u2f(ml.z);
    const float Ls = (dpp<0x00>(el) + dpp<0x55>(el)) + (dpp<0xAA>(el) + dpp<0xFF>(el));
    const float fw = e * __builtin_amdgcn_rcpf(Ls);
    LAS unsigned char* ol = L + L_O + br * 1024;
#pragma unroll
    for (int j = 0; j < 2; ++j) {
      const f32x4 a = unpack4(gr[j]) * __shfl(fw, 4 * j + (lane >> 4));
      const float o = red16(red32(a[0], a[1]), red32(a[2], a[3]));
      const int r = lane >> 4;
      *(LAS unsigned short*)(ol + (2 * w + j) * 128 + ((lane & 15) * 4 + ((r & 1) * 2 + (r >> 1))) * 2) = (unsigned short)(pack_bf16(o, o) & 0xffffu);
    }
    group_barrier();
    const v4u af0 = *(LAS v4u*)(ol + (lane & 7) * 128 + (lane >> 4) * 16), af1 = *(LAS v4u*)(ol + (lane & 7) * 128 + 64 + (lane >> 4) * 16);
#pragma unroll
    for (int c = 0; c < 2; ++c) {
      f32x4 d = mfma32(af0, bf[0][c], f32x4{0.f, 0.f, 0.f, 0.f});
      d = mfma32(af1, bf[1][c], d);
      const float e0 = dpp<0xB1>(d[0]), e1 = dpp<0xB1>(d[1]), e2 = dpp<0xB1>(d[2]), e3 = dpp<0xB1>(d[3]);
      const bool odd = lane & 1;
      const int ct = 8 * (b >> 1) + 2 * w + c;
      const int r0 = 4 * (lane >> 4) + (odd ? 2 : 0);
      const int base = WS_OG + br * NWG * 4096 + (((ct >> 2) * 8 + r0) * 4 + (ct & 3)) * 4096 + (kvh * 8 + ks) * 64 + ((lane & 15) >> 1) * 8;
      puth(odd ? e2 : d[0], odd ? d[2] : e0, lane < 32 ? base : OOB);
      puth(odd ? e3 : d[1], odd ? d[3] : e1, lane < 32 ? base + 4 * 4096 : OOB);
    }
  }
  if (grp == 0) STAMP(9);
  if (w == 0) {
    v4u a[4];
#pragma unroll
    for (int i = 0; i < 4; ++i) a[i] = v4u{0u, ~(tagA << 8), 0u, 0u};
    const int vo = WS_OG + (br * NWG + wg) * 4096 + lane * 16;
    if (H2_WAIT) __builtin_amdgcn_s_sleep(H2_WAIT);
    for (unsigned spins = 0; spins < SPIN_CAP; ++spins) {
      MEM_FENCE();
      v4u t[4];
#pragma unroll
      for (int i = 0; i < 4; ++i) t[i] = __builtin_amdgcn_raw_buffer_load_b128(rw, badp(a[i]) != 0u ? vo + i * 1024 : OOB, 0, AUX_SC1);
      SB();
      u32 bad = 0u;
#pragma unroll
      for (int i = 0; i < 4; ++i) { a[i] = badp(a[i]) != 0u ? t[i] : a[i]; bad |= badp(a[i]); }
      if (all_lanes(bad == 0u)) break;
      __builtin_amdgcn_s_sleep(H2_SLEEP);
    }
    f32x4 s = (unpack4(a[0]) + unpack4(a[1])) + (unpack4(a[2]) + unpack4(a[3]));
#pragma unroll
    for (int j = 0; j < 4; ++j) { s[j] += dpp<0x124>(s[j]); s[j] += dpp<0x128>(s[j]); }
    const float o = red16(red32(s[0], s[1]), red32(s[2], s[3]));
    const int r = lane >> 4;
    if ((lane & 15) < 4)
      ((unsigned short*)p.out)[kvh * 1024 + br * 512 + b * 64 + qd * 16 + (lane & 3) * 4 + (r & 1) * 2 + (r >> 1)] = (unsigned short)(pack_bf16(o, o) & 0xffffu);
  }
  if (wg == 0 && w8 == 4 && lane == 0) __builtin_amdgcn_raw_buffer_store_b64(v2u{tagA, tagB}, rw, 0, WS_EPOCH, AUX_SC1);
  if (w8 == 0) STAMP(10);
  if (w8 == 4) STAMP(13);
}

extern "C" void kernel_launch(void* const* d_in, const int* in_sizes, int n_in, void* d_out, int out_size, void* d_ws, size_t ws_size,
                              hipStream_t stream) {
  Params p;
  p.x[0] = d_in[0]; p.k[0] = d_in[1]; p.v[0] = d_in[2]; p.wq[0] = d_in[3]; p.wo[0] = d_in[4];
  p.x[1] = d_in[5]; p.k[1] = d_in[6]; p.v[1] = d_in[7]; p.wq[1] = d_in[8]; p.wo[1] = d_in[9];
  p.out = d_out; p.ws = (unsigned char*)d_ws;
  k_fused<<<NWG, NTHR, 0, stream>>>(p);
}
```

```cpp
#include <hip/hip_runtime.h>
#include <hip/hip_bf16.h>
#include <stdint.h>
#include <utility>

typedef unsigned u32;
typedef u32 v4u __attribute__((ext_vector_type(4)));
typedef u32 v2u __attribute__((ext_vector_type(2)));
typedef float f32x4 __attribute__((ext_vector_type(4)));
typedef float f32x2 __attribute__((ext_vector_type(2)));
typedef __bf16 bf16x8 __attribute__((ext_vector_type(8)));
typedef __bf16 bf16x2 __attribute__((ext_vector_type(2)));
typedef short v4s __attribute__((ext_vector_type(4)));
#define LAS __attribute__((address_space(3)))

#define NFLY 5
#define NWIDE 6
#define NHEAD 3
#define TAU 217
#define TBASE 510
#define Q_WAIT 32
#define FEW 4
#define POLL_SLEEP 4
#define HSLEEP true
#define H1_WAIT 0
#define H2_WAIT 16
#define H2_SLEEP 2

constexpr int NB = 8, DM = 512, HQ = 32, HKV = 8, SK = 8192, DH = 128;
constexpr int NWG = 256, NTHR = 512;
constexpr int RING = 16, TPI = RING / 4, NIT = 32 / TPI;
constexpr int KV_BYTES = NB * HKV * SK * DH * 2;
constexpr int W_BYTES = DM * HQ * DH * 2;
constexpr float CSC = 0.08838834764831845f * 1.4426950408889634f;
constexpr int AUX_NT = 2, AUX_SC1 = 16;
constexpr unsigned SPIN_CAP = 1u << 17, LSPIN_CAP = 1u << 22;
constexpr int OOB = 0x40000000;

constexpr int WS_EPOCH = 0;
constexpr int WS_QG = 4096;
constexpr int PG_HEAD = 512, PG_WG = 4 * PG_HEAD;
constexpr int WS_PG = WS_QG + 2 * 8 * 8 * 8192;
constexpr int WS_ML = WS_PG + 2 * NWG * PG_WG;
constexpr int WS_OG = WS_ML + 2 * 8 * 4 * 512;
constexpr int WS_END = WS_OG + 2 * NWG * 4096;

constexpr int MRG_REC = 132;
constexpr int L_W = 0;
constexpr int L_RED = 65536;
constexpr int L_MRG = L_RED + 16384;
constexpr int L_Q = L_MRG + 2 * 16 * MRG_REC * 4;
constexpr int L_SC = L_Q + 2048;
constexpr int L_O = L_SC + 32768;
constexpr int L_FLAG = L_O + 2048;
constexpr int L_END = L_FLAG + 128 + 16;
enum { F_Q = 0, F_LAST = 1, F_REC = 2  , F_PROJ = 4, F_WO = 5, F_O = 6   };

struct Params {
  const void* x[2];
  const void* k[2];
  const void* v[2];
  const void* wq[2];
  const void* wo[2];
  void* out;
  unsigned char* ws;
};

__device__ __forceinline__ f32x4 mfma32(v4u a, v4u b, f32x4 c) {
  return __builtin_amdgcn_mfma_f32_16x16x32_bf16(__builtin_bit_cast(bf16x8, a), __builtin_bit_cast(bf16x8, b), c, 0, 0, 0);
}
__device__ __forceinline__ f32x4 mfma16(v2u a, v2u b, f32x4 c) {
  return __builtin_amdgcn_mfma_f32_16x16x16bf16_1k(__builtin_bit_cast(v4s, a), __builtin_bit_cast(v4s, b), c, 0, 0, 0);
}
__device__ __forceinline__ u32 pack_bf16(float lo, float hi) {
  f32x2 f = {lo, hi};
  return __builtin_bit_cast(u32, __builtin_convertvector(f, bf16x2));
}
__device__ __forceinline__ v2u tr_read(LAS unsigned char* p) {
  return __builtin_bit_cast(v2u, __builtin_amdgcn_ds_read_tr16_b64_v4i16((LAS v4s*)p));
}
__device__ __forceinline__ u32 f2u(float f) { return __builtin_bit_cast(u32, f); }
__device__ __forceinline__ float u2f(u32 u) { return __builtin_bit_cast(float, u); }
template <int CTRL> __device__ __forceinline__ float dpp(float x) {
  return u2f((u32)__builtin_amdgcn_mov_dpp((int)f2u(x), CTRL, 0xf, 0xf, true));
}
__device__ __forceinline__ v2u packh(float v0, float v1, u32 tag8) {
  const u32 a = f2u(v0) + 0x800u, c = f2u(v1) + 0x800u;
  return v2u{(a >> 12) | ((c << 8) & 0xFFF00000u), (c >> 24) | tag8};
}
__device__ __forceinline__ f32x4 unpack4(v4u g) {
  return f32x4{u2f(g.x << 12), u2f(((g.x >> 20) << 12) | (g.y << 24)), u2f(g.z << 12), u2f(((g.z >> 20) << 12) | (g.w << 24))};
}
__device__ __forceinline__ float red32(float a, float b) {
  const auto s = __builtin_amdgcn_permlane32_swap(f2u(a), f2u(b), false, false);
  return u2f(s[0]) + u2f(s[1]);
}
__device__ __forceinline__ float red16(float a, float b) {
  const auto s = __builtin_amdgcn_permlane16_swap(f2u(a), f2u(b), false, false);
  return u2f(s[0]) + u2f(s[1]);
}
__device__ __forceinline__ float rows_max(float x) {
  const auto s = __builtin_amdgcn_permlane16_swap(f2u(x), f2u(x), false, false);
  x = fmaxf(u2f(s[0]), u2f(s[1]));
  const auto t = __builtin_amdgcn_permlane32_swap(f2u(x), f2u(x), false, false);
  return fmaxf(u2f(t[0]), u2f(t[1]));
}
__device__ __forceinline__ float rows_sum(float x) { x = red16(x, x); return red32(x, x); }
#define MEM_FENCE() asm volatile("" ::: "memory")
#define SB() __builtin_amdgcn_sched_barrier(0)
#define WAITVM(n) do { asm volatile("s_waitcnt vmcnt(%0)" :: "i"(n) : "memory"); SB(); } while (0)
#define WAITVM2(x, y, n, m) do { asm volatile("s_cmp_gt_u32 %0, %1\n\ts_cbranch_scc1 1f\n\ts_waitcnt vmcnt(%2)\n1:\n\ts_waitcnt vmcnt(%3)" :: "s"(x), "s"(y), "i"(n), "i"(m) : "memory", "scc"); SB(); } while (0)
#define AI __attribute__((always_inline))
__device__ __forceinline__ bool all_lanes(bool c) { return __builtin_amdgcn_ballot_w64(c) == ~0ull; }
template <int... I, class F> __device__ __forceinline__ void static_for_impl(std::integer_sequence<int, I...>, F&& f) {
  (f(std::integral_constant<int, I>{}), ...);
}
template <int N, class F> __device__ __forceinline__ void static_for(F&& f) { static_for_impl(std::make_integer_sequence<int, N>{}, f); }

__device__ __forceinline__ void lds_post(LAS unsigned char* L, int slot, int w) {
  MEM_FENCE();
  *(volatile LAS u32*)(L + L_FLAG + slot * 16 + w * 4) = 1u;
  MEM_FENCE();
}
__device__ __forceinline__ void lds_wait(LAS unsigned char* L, int slot, bool sleep) {
  for (unsigned spins = 0; spins < LSPIN_CAP; ++spins) {
    MEM_FENCE();
    const v4u v = *(volatile LAS v4u*)(L + L_FLAG + slot * 16);
    if (__builtin_amdgcn_readfirstlane(v.x & v.y & v.z & v.w) != 0u) break;
    if (sleep) __builtin_amdgcn_s_sleep(1);
  }
  MEM_FENCE();
}

__global__ void __launch_bounds__(NTHR, 2) k_fused(Params p) {
  __shared__ __attribute__((aligned(16))) unsigned char smem[L_END];
  LAS unsigned char* L = (LAS unsigned char*)smem;
  const int lane = threadIdx.x & 63, w8 = __builtin_amdgcn_readfirstlane(threadIdx.x >> 6), w = w8 & 3;
  const int grp = w8 >> 2;
  const int wg = blockIdx.x, qd = wg & 3, ub = wg >> 2, b = ub >> 3, kvh = ub & 7;
  const int ks = (b & 1) * 4 + qd;
  const __amdgpu_buffer_rsrc_t rw = __builtin_amdgcn_make_buffer_rsrc((void*)p.ws, 0, WS_END, 0x00020000);
  u32 tagA, tagB;

  if (grp == 0) {
    const u32 t0 = (u32)__builtin_amdgcn_s_memrealtime();
    const int voffK = ((lane >> 2) & 3) * 256 + (lane >> 4) * 64 + (lane & 3) * 16;
    const int voffV = lane * 16;
    const int sbase = wg * 524288 + w * 4096;
    v4u ring[RING];
    {
      const __amdgpu_buffer_rsrc_t rk = __builtin_amdgcn_make_buffer_rsrc((void*)p.k[0], 0, KV_BYTES, 0x00020000);
      static_for<RING>([&](auto I) AI {
        constexpr int i = decltype(I)::value;
        if constexpr (i == NHEAD) __builtin_amdgcn_s_barrier();
        if constexpr (i >= NHEAD) WAITVM(NHEAD - 1);
        ring[i] = __builtin_amdgcn_raw_buffer_load_b128(rk, voffK + (i & 3) * 1024, sbase + (i >> 2) * 16384, AUX_NT);
        SB();
      });
    }
    const int jq = lane & 3;
    const u32 m0 = jq == 0 ? 0x0000ffffu : (jq == 1 ? 0xffff0000u : 0u);
    const u32 m1 = jq == 2 ? 0x0000ffffu : (jq == 3 ? 0xffff0000u : 0u);
    LAS unsigned char* scl = L + L_SC + w * 8192 + (lane >> 2) * 16;
    u32 due = TBASE - TAU;
    lds_wait(L, F_Q, false);
    tagA = __builtin_amdgcn_readfirstlane(*(LAS u32*)(L + L_FLAG + 128));
    tagB = __builtin_amdgcn_readfirstlane(*(LAS u32*)(L + L_FLAG + 132));
#pragma unroll 1
    for (int br = 0; br < 2; ++br) {
      const v4u qf = *(LAS v4u*)(L + L_Q + br * 1024 + voffK);
      const void* pk = p.k[br];
      const void* pv = p.v[br];
      float mx = -INFINITY;
#pragma unroll 1
      for (int it = 0; it < NIT; ++it) {
        const bool last = it == NIT - 1;
        const u32 now = (u32)__builtin_amdgcn_s_memrealtime() - t0;
        due += TAU;
        const __amdgpu_buffer_rsrc_t rn = __builtin_amdgcn_make_buffer_rsrc((void*)(last ? pv : pk), 0, KV_BYTES, 0x00020000);
        const int vn = last ? voffV : voffK;
        const int sn = sbase + (last ? 0 : (it + 1) * TPI * 16384);
        f32x4 st;
        static_for<RING>([&](auto I) AI {
          constexpr int i = decltype(I)::value;
          const f32x4 d = mfma32(ring[i], qf, f32x4{0.f, 0.f, 0.f, 0.f});
          float s = (dpp<0x00>(d[0]) + dpp<0x55>(d[1])) + (dpp<0xAA>(d[2]) + dpp<0xFF>(d[3]));
          mx = fmaxf(mx, s);
          asm volatile("" : "+v"(s), "+v"(mx));
          st[i & 3] = s;
          if constexpr ((i & 3) == 3) { if (jq == 0) *(LAS f32x4*)(scl + (it * TPI + (i >> 2)) * 256) = st; }
          SB();
          WAITVM2(now, due, NFLY - 1, NWIDE - 1);
          ring[i] = __builtin_amdgcn_raw_buffer_load_b128(rn, vn + (i & 3) * 1024, sn + (i >> 2) * 16384, AUX_NT);
          SB();
        });
      }
      const float mc = rows_max(mx) * CSC;
      float lsum = 0.f;
      f32x4 accLo = {0.f, 0.f, 0.f, 0.f}, accHi = {0.f, 0.f, 0.f, 0.f};
#pragma unroll 1
      for (int it = 0; it < NIT; ++it) {
        const bool last = it == NIT - 1;
        const u32 now = (u32)__builtin_amdgcn_s_memrealtime() - t0;
        due += TAU;
        const __amdgpu_buffer_rsrc_t rn =
            __builtin_amdgcn_make_buffer_rsrc((void*)(last ? p.k[1] : pv), 0, (last && br == 1) ? 0 : KV_BYTES, 0x00020000);
        const int vn = last ? voffK : voffV;
        const int sn = sbase + (last ? 0 : (it + 1) * TPI * 16384);
        if (last && br == 1) lds_post(L, F_LAST, w);
        f32x4 sv[TPI];
#pragma unroll
        for (int k = 0; k < TPI; ++k) sv[k] = *(LAS f32x4*)(scl + (it * TPI + k) * 256);
        static_for<RING>([&](auto I) AI {
          constexpr int i = decltype(I)::value;
          const float pe = __builtin_amdgcn_exp2f(fmaf(sv[i >> 2][i & 3], CSC, -mc));
          lsum += pe;
          asm volatile("" : "+v"(lsum));
          const u32 pkd = pack_bf16(pe, pe);
          const v2u a = {pkd & m0, pkd & m1};
          accLo = mfma16(a, v2u{ring[i].x, ring[i].y}, accLo);
          accHi = mfma16(a, v2u{ring[i].z, ring[i].w}, accHi);
          SB();
          WAITVM2(now, due, NFLY - 1, NWIDE - 1);
          ring[i] = __builtin_amdgcn_raw_buffer_load_b128(rn, vn + (i & 3) * 1024, sn + (i >> 2) * 16384, AUX_NT);
          SB();
        });
      }
      lsum = rows_sum(lsum);
      LAS float* mrg = (LAS float*)(L + L_MRG) + br * 16 * MRG_REC;
      LAS float* rec = mrg + (w * 4 + (lane >> 4)) * MRG_REC + 8 * (lane & 15);
      *(LAS f32x4*)rec = accLo;
      *(LAS f32x4*)(rec + 4) = accHi;
      if (lane < 16 && jq == 0) *(LAS f32x2*)(mrg + (w * 4 + (lane >> 2)) * MRG_REC + 128) = f32x2{mc, lsum};
      lds_post(L, F_REC + br, w);
    }
  } else {
    const int tid = w * 64 + lane;
    const v2u ep = __builtin_amdgcn_raw_buffer_load_b64(rw, 0, WS_EPOCH, AUX_SC1);
    v4u wr[2][4], xf[2];
#pragma unroll
    for (int br = 0; br < 2; ++br) {
      const __amdgpu_buffer_rsrc_t rq = __builtin_amdgcn_make_buffer_rsrc((void*)p.wq[br], 0, W_BYTES, 0x00020000);
      const __amdgpu_buffer_rsrc_t rx = __builtin_amdgcn_make_buffer_rsrc((void*)p.x[br], 0, NB * DM * 2, 0x00020000);
      const int so = (128 * qd + 32 * w) * (HQ * DH * 2) + (kvh * 512 + b * 64) * 2;
#pragma unroll
      for (int i = 0; i < 4; ++i)
        wr[br][i] = __builtin_amdgcn_raw_buffer_load_b128(rq, (lane >> 3) * (HQ * DH * 2) + (lane & 7) * 16, so + i * 8 * (HQ * DH * 2), AUX_NT);
      xf[br] = __builtin_amdgcn_raw_buffer_load_b128(rx, (lane & 7) * (DM * 2) + (lane >> 4) * 16, (128 * qd + 32 * w) * 2, 0);
      SB();
    }
    if (w == 0 && lane < 32) ((LAS u32*)(L + L_FLAG))[lane] = 0u;
    asm volatile("s_waitcnt lgkmcnt(0)" ::: "memory");
    __builtin_amdgcn_s_barrier();
    tagA = __builtin_amdgcn_readfirstlane(ep.x) + 1u;
    tagB = __builtin_amdgcn_readfirstlane(ep.y) + 0x9E3779B9u;
    if (lane == 0) *(LAS v2u*)(L + L_FLAG + 128) = v2u{tagA, tagB};
#pragma unroll
    for (int br = 0; br < 2; ++br) {
      LAS unsigned char* wl = L + L_W + br * 16384 + w * 4096;
#pragma unroll
      for (int i = 0; i < 4; ++i) *(LAS v4u*)(wl + i * 1024 + lane * 16) = wr[br][i];
      LAS unsigned char* rb = wl + (8 * (lane >> 4) + ((lane >> 2) & 3)) * 128 + (lane & 3) * 8;
      LAS f32x4* red = (LAS f32x4*)(L + L_RED + br * 8192);
#pragma unroll
      for (int ct = 0; ct < 4; ++ct) {
        const v2u lo = tr_read(rb + ct * 32), hi = tr_read(rb + ct * 32 + 512);
        const f32x4 d = mfma32(xf[br], v4u{lo.x, lo.y, hi.x, hi.y}, f32x4{0.f, 0.f, 0.f, 0.f});
        if (lane < 32) red[(w * 4 + ct) * 32 + lane] = d;
      }
    }
    lds_post(L, F_PROJ, w);
    lds_wait(L, F_PROJ, false);
    {
      const int pb = 4 * ((tid >> 3) & 1) + ((tid >> 4) & 3), pn = 16 * w + 2 * (tid & 7);
      const u32 tag8 = ((tid & 1) ? tagB : tagA) << 8;
#pragma unroll
      for (int br = 0; br < 2; ++br) {
        LAS float* rf = (LAS float*)(L + L_RED + br * 8192) + (w * 32 + 16 * ((tid >> 3) & 1) + 2 * (tid & 7)) * 4 + ((tid >> 4) & 3);
        float s0 = 0.f, s1 = 0.f;
#pragma unroll
        for (int ww = 0; ww < 4; ++ww) { s0 += rf[ww * 512]; s1 += rf[ww * 512 + 4]; }
        __builtin_amdgcn_raw_buffer_store_b64(packh(s0, s1, tag8), rw, WS_QG + (br * 8 + kvh) * 65536 + pb * 8192 + qd * 2048 + b * 256 + (pn >> 1) * 8, 0, AUX_SC1);
      }
    }
    SB();
    __builtin_amdgcn_s_sleep(Q_WAIT);
    SB();
    {
      const int qvo = WS_QG + (kvh * 8 + b) * 8192 + w * 512 + (lane >> 5) * 2048 + (lane & 31) * 16;
      const u32 ta = tagA << 8, tb = tagB << 8;
      v4u g[2][2];
      for (unsigned spins = 0; spins < SPIN_CAP; ++spins) {
        MEM_FENCE();
        u32 bad = 0u;
#pragma unroll
        for (int br = 0; br < 2; ++br)
#pragma unroll
          for (int i = 0; i < 2; ++i) g[br][i] = __builtin_amdgcn_raw_buffer_load_b128(rw, qvo, br * 8 * 65536 + i * 4096, AUX_SC1);
#pragma unroll
        for (int br = 0; br < 2; ++br)
#pragma unroll
          for (int i = 0; i < 2; ++i) bad |= (g[br][i].y ^ ta) | (g[br][i].w ^ tb);
        if (all_lanes((bad >> 8) == 0u)) break;
        __builtin_amdgcn_s_sleep(4);
      }
#pragma unroll
      for (int br = 0; br < 2; ++br) {
        const f32x4 s = unpack4(g[br][0]) + unpack4(g[br][1]);
        const float u0 = red32(s[0], s[2]), u1 = red32(s[1], s[3]);
        *(LAS u32*)(L + L_Q + br * 1024 + w * 256 + (lane & 31) * 8 + (lane >> 5) * 4) = pack_bf16(u0, u1);
      }
      lds_post(L, F_Q, w);
    }
  }

  const int br = 1 - grp;
  const u32 ta = tagA << 8, tb = tagB << 8;
  auto bad4 = [=](const v4u& g) AI -> u32 { return (g.y ^ tagA) | (g.w ^ tagB); };
  auto badp = [=](const v4u& g) AI -> u32 { return ((g.y ^ ta) | (g.w ^ tb)) >> 8; };
  int a_po = WS_PG + (br * NWG + wg) * PG_WG + w * PG_HEAD + lane * 8;
  int a_pm = lane == 0 ? WS_ML + ((br * 8 + kvh) * 4 + w) * 512 + (b * 4 + qd) * 16 : OOB;
  int a_ml = WS_ML + ((br * 8 + kvh) * 4 + (ks >> 1)) * 512 + (w * 8 + (lane >> 4)) * 16;
  int a_o = WS_PG + (br * NWG + (2 * w * 8 + kvh) * 4 + (lane >> 4)) * PG_WG + (ks >> 1) * PG_HEAD + (ks & 1) * 256 + (lane & 15) * 16;
  const bool odd = lane & 1;
  int a_og = lane < 32 ? WS_OG + br * NWG * 4096 + (((2 * (b >> 1) + (w >> 1)) * 8 + 4 * (lane >> 4) + (odd ? 2 : 0)) * 4 + 2 * (w & 1)) * 4096 +
                             (kvh * 8 + ks) * 64 + ((lane & 15) >> 1) * 8
                       : OOB;
  u32 t_po = (lane & 1) ? tb : ta, t_og = (lane & 2) ? tb : ta;
  asm volatile("" : "+v"(a_po), "+v"(a_pm), "+v"(a_ml), "+v"(a_o), "+v"(a_og), "+v"(t_po), "+v"(t_og));

  lds_wait(L, F_REC + br, grp != 0);
  {
    LAS float* mrg = (LAS float*)(L + L_MRG) + br * 16 * MRG_REC;
    f32x2 ml4[4], o4[4];
#pragma unroll
    for (int ww = 0; ww < 4; ++ww) {
      ml4[ww] = *(LAS f32x2*)(mrg + (ww * 4 + w) * MRG_REC + 128);
      o4[ww] = *(LAS f32x2*)(mrg + (ww * 4 + w) * MRG_REC + 2 * lane);
    }
    float M = fmaxf(fmaxf(ml4[0].x, ml4[1].x), fmaxf(ml4[2].x, ml4[3].x));
    float Ls = 0.f, o0 = 0.f, o1 = 0.f;
#pragma unroll
    for (int ww = 0; ww < 4; ++ww) {
      const float f = __builtin_amdgcn_exp2f(ml4[ww].x - M);
      Ls = fmaf(f, ml4[ww].y, Ls);
      o0 = fmaf(f, o4[ww].x, o0);
      o1 = fmaf(f, o4[ww].y, o1);
    }
    v2u ph = packh(o0, o1, t_po);
    v4u wo4[2][4];
    if (grp == 1) {
      asm volatile("" : "+v"(ph), "+v"(M), "+v"(Ls));
      lds_wait(L, F_LAST, true);
#pragma unroll
      for (int bq = 0; bq < 2; ++bq) {
        const __amdgpu_buffer_rsrc_t ro = __builtin_amdgcn_make_buffer_rsrc((void*)p.wo[bq], 0, W_BYTES, 0x00020000);
        const int so = (kvh * 512 + 64 * ks + 16 * w) * (DM * 2) + (b >> 1) * 256;
#pragma unroll
        for (int i = 0; i < 4; ++i)
          wo4[bq][i] = __builtin_amdgcn_raw_buffer_load_b128(ro, (lane >> 4) * (DM * 2) + (lane & 15) * 16, so + i * 4 * (DM * 2), AUX_NT);
      }
      SB();
      lds_wait(L, F_REC + 1, HSLEEP);
    }
    __builtin_amdgcn_raw_buffer_store_b64(ph, rw, a_po, 0, AUX_SC1);
    __builtin_amdgcn_raw_buffer_store_b128(v4u{f2u(M), tagA, f2u(Ls), tagB}, rw, a_pm, 0, AUX_SC1);
    if (grp == 1) {
#pragma unroll
      for (int bq = 0; bq < 2; ++bq)
#pragma unroll
        for (int i = 0; i < 4; ++i) *(LAS v4u*)(L + L_W + (2 + bq) * 16384 + w * 4096 + i * 1024 + lane * 16) = wo4[bq][i];
      lds_post(L, F_WO, w);
    }
    lds_wait(L, F_WO, false);
  }
  v4u bf[2][2];
  {
    LAS unsigned char* rb = L + L_W + (2 + br) * 16384 + (8 * (lane >> 4) + ((lane >> 2) & 3)) * 256 + w * 64 + (lane & 3) * 8;
#pragma unroll
    for (int s = 0; s < 2; ++s)
#pragma unroll
      for (int c = 0; c < 2; ++c) {
        const v2u lo = tr_read(rb + s * 8192 + c * 32), hi = tr_read(rb + s * 8192 + c * 32 + 1024);
        bf[s][c] = v4u{lo.x, lo.y, hi.x, hi.y};
      }
  }
  v4u ml[2], gr[2];
  {
#pragma unroll
    for (int j = 0; j < 2; ++j) { ml[j] = v4u{0u, ~tagA, 0u, 0u}; gr[j] = v4u{0u, ~ta, 0u, 0u}; }
    bool few = false;
    if (H1_WAIT) __builtin_amdgcn_s_sleep(H1_WAIT);
    for (unsigned spins = 0; spins < SPIN_CAP; ++spins) {
      MEM_FENCE();
      v4u tm[2], t[2];
      bool wm[2], want[2];
#pragma unroll
      for (int j = 0; j < 2; ++j) {
        wm[j] = bad4(ml[j]) != 0u;
        want[j] = badp(gr[j]) != 0u && (few || !wm[j]);
        tm[j] = __builtin_amdgcn_raw_buffer_load_b128(rw, wm[j] ? a_ml + j * 64 : OOB, 0, AUX_SC1);
        t[j] = __builtin_amdgcn_raw_buffer_load_b128(rw, want[j] ? a_o + j * 32 * PG_WG : OOB, 0, AUX_SC1);
      }
      SB();
      u32 bad = 0u;
      int miss = 0;
#pragma unroll
      for (int j = 0; j < 2; ++j) {
        ml[j] = wm[j] ? tm[j] : ml[j];
        gr[j] = want[j] ? t[j] : gr[j];
        const u32 bm = bad4(ml[j]);
        miss += __builtin_popcountll(__builtin_amdgcn_ballot_w64(bm != 0u));
        bad |= bm | badp(gr[j]);
      }
      if (all_lanes(bad == 0u)) break;
      few = miss <= 16 * FEW;
      if (!few) __builtin_amdgcn_s_sleep(POLL_SLEEP);
    }
  }
  {
    LAS unsigned char* ol = L + L_O + br * 1024;
    const int r = lane >> 4;
#pragma unroll
    for (int j = 0; j < 2; ++j) {
      const float m = u2f(ml[j].x);
      const float e = __builtin_amdgcn_exp2f(m - rows_max(m));
      const float fw = e * __builtin_amdgcn_rcpf(rows_sum(e * u2f(ml[j].z)));
      const f32x4 a = unpack4(gr[j]) * fw;
      const float o = red16(red32(a[0], a[1]), red32(a[2], a[3]));
      *(LAS unsigned short*)(ol + (2 * w + j) * 128 + ((lane & 15) * 4 + ((r & 1) * 2 + (r >> 1))) * 2) = (unsigned short)(pack_bf16(o, o) & 0xffffu);
    }
    lds_post(L, F_O + br, w);
    lds_wait(L, F_O + br, false);
    const v4u af0 = *(LAS v4u*)(ol + (lane & 7) * 128 + (lane >> 4) * 16), af1 = *(LAS v4u*)(ol + (lane & 7) * 128 + 64 + (lane >> 4) * 16);
#pragma unroll
    for (int c = 0; c < 2; ++c) {
      f32x4 d = mfma32(af0, bf[0][c], f32x4{0.f, 0.f, 0.f, 0.f});
      d = mfma32(af1, bf[1][c], d);
      const float e0 = dpp<0xB1>(d[0]), e1 = dpp<0xB1>(d[1]), e2 = dpp<0xB1>(d[2]), e3 = dpp<0xB1>(d[3]);
      __builtin_amdgcn_raw_buffer_store_b64(packh(odd ? e2 : d[0], odd ? d[2] : e0, t_og), rw, a_og + c * 4096, 0, AUX_SC1);
      __builtin_amdgcn_raw_buffer_store_b64(packh(odd ? e3 : d[1], odd ? d[3] : e1, t_og), rw, a_og + c * 4096 + 4 * 4096, 0, AUX_SC1);
    }
  }
  if (w == 0) {
    v4u a[4];
#pragma unroll
    for (int i = 0; i < 4; ++i) a[i] = v4u{0u, ~ta, 0u, 0u};
    const int vo = WS_OG + (br * NWG + wg) * 4096 + lane * 16;
    __builtin_amdgcn_s_sleep(H2_WAIT);
    for (unsigned spins = 0; spins < SPIN_CAP; ++spins) {
      MEM_FENCE();
      v4u t[4];
#pragma unroll
      for (int i = 0; i < 4; ++i) t[i] = __builtin_amdgcn_raw_buffer_load_b128(rw, badp(a[i]) != 0u ? vo + i * 1024 : OOB, 0, AUX_SC1);
      SB();
      u32 bad = 0u;
#pragma unroll
      for (int i = 0; i < 4; ++i) { a[i] = badp(a[i]) != 0u ? t[i] : a[i]; bad |= badp(a[i]); }
      if (all_lanes(bad == 0u)) break;
      __builtin_amdgcn_s_sleep(H2_SLEEP);
    }
    f32x4 s = (unpack4(a[0]) + unpack4(a[1])) + (unpack4(a[2]) + unpack4(a[3]));
#pragma unroll
    for (int j = 0; j < 4; ++j) { s[j] += dpp<0x124>(s[j]); s[j] += dpp<0x128>(s[j]); }
    const float o = red16(red32(s[0], s[1]), red32(s[2], s[3]));
    const int r = lane >> 4;
    if ((lane & 15) < 4)
      ((unsigned short*)p.out)[kvh * 1024 + br * 512 + b * 64 + qd * 16 + (lane & 3) * 4 + (r & 1) * 2 + (r >> 1)] = (unsigned short)(pack_bf16(o, o) & 0xffffu);
    if (wg == 0 && grp == 1 && lane == 0) __builtin_amdgcn_raw_buffer_store_b64(v2u{tagA, tagB}, rw, 0, WS_EPOCH, AUX_SC1);
  }
}

extern "C" void kernel_launch(void* const* d_in, const int* in_sizes, int n_in, void* d_out, int out_size, void* d_ws, size_t ws_size,
                              hipStream_t stream) {
  Params p;
  p.x[0] = d_in[0]; p.k[0] = d_in[1]; p.v[0] = d_in[2]; p.wq[0] = d_in[3]; p.wo[0] = d_in[4];
  p.x[1] = d_in[5]; p.k[1] = d_in[6]; p.v[1] = d_in[7]; p.wq[1] = d_in[8]; p.wo[1] = d_in[9];
  p.out = d_out;
  p.ws = (unsigned char*)d_ws;
  k_fused<<<NWG, NTHR, 0, stream>>>(p);
}
```

```cpp
#include <hip/hip_runtime.h>
#include <hip/hip_bf16.h>
#include <stdint.h>
#include <utility>

typedef unsigned u32;
typedef u32 v4u __attribute__((ext_vector_type(4)));
typedef u32 v2u __attribute__((ext_vector_type(2)));
typedef float f32x4 __attribute__((ext_vector_type(4)));
typedef float f32x2 __attribute__((ext_vector_type(2)));
typedef __bf16 bf16x8 __attribute__((ext_vector_type(8)));
typedef __bf16 bf16x2 __attribute__((ext_vector_type(2)));
typedef short v4s __attribute__((ext_vector_type(4)));
#define LAS __attribute__((address_space(3)))

#define NFLY 5
#define NWIDE 6
#define NHEAD 3
#define TAU 217
#define TBASE 510
#define Q_WAIT 32
#define FEW 4
#define POLL_SLEEP 4
#define HSLEEP true
#define H1_WAIT 6
#define H2_WAIT 16
#define H2_SLEEP 2

constexpr int NB = 8, DM = 512, HQ = 32, HKV = 8, SK = 8192, DH = 128;
constexpr int NWG = 256, NTHR = 512;
constexpr int RING = 16, TPI = RING / 4, NIT = 32 / TPI;
constexpr int KV_BYTES = NB * HKV * SK * DH * 2;
constexpr int W_BYTES = DM * HQ * DH * 2;
constexpr float CSC = 0.08838834764831845f * 1.4426950408889634f;
constexpr int AUX_NT = 2, AUX_SC1 = 16;
constexpr unsigned SPIN_CAP = 1u << 17, LSPIN_CAP = 1u << 22;
constexpr int OOB = 0x40000000;

constexpr int WS_EPOCH = 0;
constexpr int WS_QG = 4096;
constexpr int PG_HEAD = 512, PG_WG = 4 * PG_HEAD;
constexpr int WS_PG = WS_QG + 2 * 8 * 8 * 8192;
constexpr int WS_ML = WS_PG + 2 * NWG * PG_WG;
constexpr int WS_OG = WS_ML + 2 * 8 * 4 * 512;
constexpr int WS_END = WS_OG + 2 * NWG * 4096;

constexpr int MRG_REC = 132;
constexpr int L_W = 0;
constexpr int L_RED = 65536;
constexpr int L_MRG = L_RED + 16384;
constexpr int L_Q = L_MRG + 2 * 16 * MRG_REC * 4;
constexpr int L_SC = L_Q + 2048;
constexpr int L_O = L_SC + 32768;
constexpr int L_FLAG = L_O + 2048;
constexpr int L_END = L_FLAG + 128 + 16;
enum { F_Q = 0, F_LAST = 1, F_REC = 2  , F_PROJ = 4, F_WO = 5, F_O = 6   };

struct Params {
  const void* x[2];
  const void* k[2];
  const void* v[2];
  const void* wq[2];
  const void* wo[2];
  void* out;
  unsigned char* ws;
};

__device__ __forceinline__ f32x4 mfma32(v4u a, v4u b, f32x4 c) {
  return __builtin_amdgcn_mfma_f32_16x16x32_bf16(__builtin_bit_cast(bf16x8, a), __builtin_bit_cast(bf16x8, b), c, 0, 0, 0);
}
__device__ __forceinline__ f32x4 mfma16(v2u a, v2u b, f32x4 c) {
  return __builtin_amdgcn_mfma_f32_16x16x16bf16_1k(__builtin_bit_cast(v4s, a), __builtin_bit_cast(v4s, b), c, 0, 0, 0);
}
__device__ __forceinline__ u32 pack_bf16(float lo, float hi) {
  f32x2 f = {lo, hi};
  return __builtin_bit_cast(u32, __builtin_convertvector(f, bf16x2));
}
__device__ __forceinline__ v2u tr_read(LAS unsigned char* p) {
  return __builtin_bit_cast(v2u, __builtin_amdgcn_ds_read_tr16_b64_v4i16((LAS v4s*)p));
}
__device__ __forceinline__ u32 f2u(float f) { return __builtin_bit_cast(u32, f); }
__device__ __forceinline__ float u2f(u32 u) { return __builtin_bit_cast(float, u); }
template <int CTRL> __device__ __forceinline__ float dpp(float x) {
  return u2f((u32)__builtin_amdgcn_mov_dpp((int)f2u(x), CTRL, 0xf, 0xf, true));
}
__device__ __forceinline__ v2u packh(float v0, float v1, u32 tag8) {
  const u32 a = f2u(v0) + 0x800u, c = f2u(v1) + 0x800u;
  return v2u{(a >> 12) | ((c << 8) & 0xFFF00000u), (c >> 24) | tag8};
}
__device__ __forceinline__ f32x4 unpack4(v4u g) {
  return f32x4{u2f(g.x << 12), u2f(((g.x >> 20) << 12) | (g.y << 24)), u2f(g.z << 12), u2f(((g.z >> 20) << 12) | (g.w << 24))};
}
__device__ __forceinline__ float red32(float a, float b) {
  const auto s = __builtin_amdgcn_permlane32_swap(f2u(a), f2u(b), false, false);
  return u2f(s[0]) + u2f(s[1]);
}
__device__ __forceinline__ float red16(float a, float b) {
  const auto s = __builtin_amdgcn_permlane16_swap(f2u(a), f2u(b), false, false);
  return u2f(s[0]) + u2f(s[1]);
}
__device__ __forceinline__ float rows_max(float x) {
  const auto s = __builtin_amdgcn_permlane16_swap(f2u(x), f2u(x), false, false);
  x = fmaxf(u2f(s[0]), u2f(s[1]));
  const auto t = __builtin_amdgcn_permlane32_swap(f2u(x), f2u(x), false, false);
  return fmaxf(u2f(t[0]), u2f(t[1]));
}
__device__ __forceinline__ float rows_sum(float x) { x = red16(x, x); return red32(x, x); }
#define MEM_FENCE() asm volatile("" ::: "memory")
#define SB() __builtin_amdgcn_sched_barrier(0)
#define WAITVM(n) do { asm volatile("s_waitcnt vmcnt(%0)" :: "i"(n) : "memory"); SB(); } while (0)
#define WAITVM2(x, y, n, m) do { asm volatile("s_cmp_gt_u32 %0, %1\n\ts_cbranch_scc1 1f\n\ts_waitcnt vmcnt(%2)\n1:\n\ts_waitcnt vmcnt(%3)" :: "s"(x), "s"(y), "i"(n), "i"(m) : "memory", "scc"); SB(); } while (0)
#define AI __attribute__((always_inline))
__device__ __forceinline__ bool all_lanes(bool c) { return __builtin_amdgcn_ballot_w64(c) == ~0ull; }
template <int... I, class F> __device__ __forceinline__ void static_for_impl(std::integer_sequence<int, I...>, F&& f) {
  (f(std::integral_constant<int, I>{}), ...);
}
template <int N, class F> __device__ __forceinline__ void static_for(F&& f) { static_for_impl(std::make_integer_sequence<int, N>{}, f); }

__device__ __forceinline__ void lds_post(LAS unsigned char* L, int slot, int w) {
  MEM_FENCE();
  *(volatile LAS u32*)(L + L_FLAG + slot * 16 + w * 4) = 1u;
  MEM_FENCE();
}
__device__ __forceinline__ void lds_wait(LAS unsigned char* L, int slot, bool sleep) {
  for (unsigned spins = 0; spins < LSPIN_CAP; ++spins) {
    MEM_FENCE();
    const v4u v = *(volatile LAS v4u*)(L + L_FLAG + slot * 16);
    if (__builtin_amdgcn_readfirstlane(v.x & v.y & v.z & v.w) != 0u) break;
    if (sleep) __builtin_amdgcn_s_sleep(1);
  }
  MEM_FENCE();
}

__global__ void __launch_bounds__(NTHR, 2) k_fused(Params p) {
  __shared__ __attribute__((aligned(16))) unsigned char smem[L_END];
  LAS unsigned char* L = (LAS unsigned char*)smem;
  const int lane = threadIdx.x & 63, w8 = __builtin_amdgcn_readfirstlane(threadIdx.x >> 6), w = w8 & 3;
  const int grp = w8 >> 2;
  const int wg = blockIdx.x, qd = wg & 3, ub = wg >> 2, b = ub >> 3, kvh = ub & 7;
  const int ks = (b & 1) * 4 + qd;
  const __amdgpu_buffer_rsrc_t rw = __builtin_amdgcn_make_buffer_rsrc((void*)p.ws, 0, WS_END, 0x00020000);
  u32 tagA, tagB;

  if (grp == 0) {
    const u32 t0 = (u32)__builtin_amdgcn_s_memrealtime();
    const int voffK = ((lane >> 2) & 3) * 256 + (lane >> 4) * 64 + (lane & 3) * 16;
    const int voffV = lane * 16;
    const int sbase = wg * 524288 + w * 4096;
    v4u ring[RING];
    {
      const __amdgpu_buffer_rsrc_t rk = __builtin_amdgcn_make_buffer_rsrc((void*)p.k[0], 0, KV_BYTES, 0x00020000);
      static_for<RING>([&](auto I) AI {
        constexpr int i = decltype(I)::value;
        if constexpr (i == NHEAD) __builtin_amdgcn_s_barrier();
        if constexpr (i >= NHEAD) WAITVM(NHEAD - 1);
        ring[i] = __builtin_amdgcn_raw_buffer_load_b128(rk, voffK + (i & 3) * 1024, sbase + (i >> 2) * 16384, AUX_NT);
        SB();
      });
    }
    const int jq = lane & 3;
    const u32 m0 = jq == 0 ? 0x0000ffffu : (jq == 1 ? 0xffff0000u : 0u);
    const u32 m1 = jq == 2 ? 0x0000ffffu : (jq == 3 ? 0xffff0000u : 0u);
    LAS unsigned char* scl = L + L_SC + w * 8192 + (lane >> 2) * 16;
    u32 due = TBASE - TAU;
    lds_wait(L, F_Q, false);
    tagA = __builtin_amdgcn_readfirstlane(*(LAS u32*)(L + L_FLAG + 128));
    tagB = __builtin_amdgcn_readfirstlane(*(LAS u32*)(L + L_FLAG + 132));
#pragma unroll 1
    for (int br = 0; br < 2; ++br) {
      const v4u qf = *(LAS v4u*)(L + L_Q + br * 1024 + voffK);
      const void* pk = p.k[br];
      const void* pv = p.v[br];
      float mx = -INFINITY;
#pragma unroll 1
      for (int it = 0; it < NIT; ++it) {
        const bool last = it == NIT - 1;
        const u32 now = (u32)__builtin_amdgcn_s_memrealtime() - t0;
        due += TAU;
        const __amdgpu_buffer_rsrc_t rn = __builtin_amdgcn_make_buffer_rsrc((void*)(last ? pv : pk), 0, KV_BYTES, 0x00020000);
        const int vn = last ? voffV : voffK;
        const int sn = sbase + (last ? 0 : (it + 1) * TPI * 16384);
        f32x4 st;
        static_for<RING>([&](auto I) AI {
          constexpr int i = decltype(I)::value;
          const f32x4 d = mfma32(ring[i], qf, f32x4{0.f, 0.f, 0.f, 0.f});
          float s = (dpp<0x00>(d[0]) + dpp<0x55>(d[1])) + (dpp<0xAA>(d[2]) + dpp<0xFF>(d[3]));
          mx = fmaxf(mx, s);
          asm volatile("" : "+v"(s), "+v"(mx));
          st[i & 3] = s;
          if constexpr ((i & 3) == 3) { if (jq == 0) *(LAS f32x4*)(scl + (it * TPI + (i >> 2)) * 256) = st; }
          SB();
          WAITVM2(now, due, NFLY - 1, NWIDE - 1);
          ring[i] = __builtin_amdgcn_raw_buffer_load_b128(rn, vn + (i & 3) * 1024, sn + (i >> 2) * 16384, AUX_NT);
          SB();
        });
      }
      const float mc = rows_max(mx) * CSC;
      float lsum = 0.f;
      f32x4 accLo = {0.f, 0.f, 0.f, 0.f}, accHi = {0.f, 0.f, 0.f, 0.f};
#pragma unroll 1
      for (int it = 0; it < NIT; ++it) {
        const bool last = it == NIT - 1;
        const u32 now = (u32)__builtin_amdgcn_s_memrealtime() - t0;
        due += TAU;
        const __amdgpu_buffer_rsrc_t rn =
            __builtin_amdgcn_make_buffer_rsrc((void*)(last ? p.k[1] : pv), 0, (last && br == 1) ? 0 : KV_BYTES, 0x00020000);
        const int vn = last ? voffK : voffV;
        const int sn = sbase + (last ? 0 : (it + 1) * TPI * 16384);
        if (last && br == 1) lds_post(L, F_LAST, w);
        f32x4 sv[TPI];
#pragma unroll
        for (int k = 0; k < TPI; ++k) sv[k] = *(LAS f32x4*)(scl + (it * TPI + k) * 256);
        static_for<RING>([&](auto I) AI {
          constexpr int i = decltype(I)::value;
          const float pe = __builtin_amdgcn_exp2f(fmaf(sv[i >> 2][i & 3], CSC, -mc));
          lsum += pe;
          asm volatile("" : "+v"(lsum));
          const u32 pkd = pack_bf16(pe, pe);
          const v2u a = {pkd & m0, pkd & m1};
          accLo = mfma16(a, v2u{ring[i].x, ring[i].y}, accLo);
          accHi = mfma16(a, v2u{ring[i].z, ring[i].w}, accHi);
          SB();
          WAITVM2(now, due, NFLY - 1, NWIDE - 1);
          ring[i] = __builtin_amdgcn_raw_buffer_load_b128(rn, vn + (i & 3) * 1024, sn + (i >> 2) * 16384, AUX_NT);
          SB();
        });
      }
      lsum = rows_sum(lsum);
      LAS float* mrg = (LAS float*)(L + L_MRG) + br * 16 * MRG_REC;
      LAS float* rec = mrg + (w * 4 + (lane >> 4)) * MRG_REC + 8 * (lane & 15);
      *(LAS f32x4*)rec = accLo;
      *(LAS f32x4*)(rec + 4) = accHi;
      if (lane < 16 && jq == 0) *(LAS f32x2*)(mrg + (w * 4 + (lane >> 2)) * MRG_REC + 128) = f32x2{mc, lsum};
      lds_post(L, F_REC + br, w);
    }
  } else {
    const int tid = w * 64 + lane;
    const v2u ep = __builtin_amdgcn_raw_buffer_load_b64(rw, 0, WS_EPOCH, AUX_SC1);
    v4u wr[2][4], xf[2];
#pragma unroll
    for (int br = 0; br < 2; ++br) {
      const __amdgpu_buffer_rsrc_t rq = __builtin_amdgcn_make_buffer_rsrc((void*)p.wq[br], 0, W_BYTES, 0x00020000);
      const __amdgpu_buffer_rsrc_t rx = __builtin_amdgcn_make_buffer_rsrc((void*)p.x[br], 0, NB * DM * 2, 0x00020000);
      const int so = (128 * qd + 32 * w) * (HQ * DH * 2) + (kvh * 512 + b * 64) * 2;
#pragma unroll
      for (int i = 0; i < 4; ++i)
        wr[br][i] = __builtin_amdgcn_raw_buffer_load_b128(rq, (lane >> 3) * (HQ * DH * 2) + (lane & 7) * 16, so + i * 8 * (HQ * DH * 2), AUX_NT);
      xf[br] = __builtin_amdgcn_raw_buffer_load_b128(rx, (lane & 7) * (DM * 2) + (lane >> 4) * 16, (128 * qd + 32 * w) * 2, 0);
      SB();
    }
    if (w == 0 && lane < 32) ((LAS u32*)(L + L_FLAG))[lane] = 0u;
    asm volatile("s_waitcnt lgkmcnt(0)" ::: "memory");
    __builtin_amdgcn_s_barrier();
    tagA = __builtin_amdgcn_readfirstlane(ep.x) + 1u;
    tagB = __builtin_amdgcn_readfirstlane(ep.y) + 0x9E3779B9u;
    if (lane == 0) *(LAS v2u*)(L + L_FLAG + 128) = v2u{tagA, tagB};
#pragma unroll
    for (int br = 0; br < 2; ++br) {
      LAS unsigned char* wl = L + L_W + br * 16384 + w * 4096;
#pragma unroll
      for (int i = 0; i < 4; ++i) *(LAS v4u*)(wl + i * 1024 + lane * 16) = wr[br][i];
      LAS unsigned char* rb = wl + (8 * (lane >> 4) + ((lane >> 2) & 3)) * 128 + (lane & 3) * 8;
      LAS f32x4* red = (LAS f32x4*)(L + L_RED + br * 8192);
#pragma unroll
      for (int ct = 0; ct < 4; ++ct) {
        const v2u lo = tr_read(rb + ct * 32), hi = tr_read(rb + ct * 32 + 512);
        const f32x4 d = mfma32(xf[br], v4u{lo.x, lo.y, hi.x, hi.y}, f32x4{0.f, 0.f, 0.f, 0.f});
        if (lane < 32) red[(w * 4 + ct) * 32 + lane] = d;
      }
    }
    lds_post(L, F_PROJ, w);
    lds_wait(L, F_PROJ, false);
    {
      const int pb = 4 * ((tid >> 3) & 1) + ((tid >> 4) & 3), pn = 16 * w + 2 * (tid & 7);
      const u32 tag8 = ((tid & 1) ? tagB : tagA) << 8;
#pragma unroll
      for (int br = 0; br < 2; ++br) {
        LAS float* rf = (LAS float*)(L + L_RED + br * 8192) + (w * 32 + 16 * ((tid >> 3) & 1) + 2 * (tid & 7)) * 4 + ((tid >> 4) & 3);
        float s0 = 0.f, s1 = 0.f;
#pragma unroll
        for (int ww = 0; ww < 4; ++ww) { s0 += rf[ww * 512]; s1 += rf[ww * 512 + 4]; }
        __builtin_amdgcn_raw_buffer_store_b64(packh(s0, s1, tag8), rw, WS_QG + (br * 8 + kvh) * 65536 + pb * 8192 + qd * 2048 + b * 256 + (pn >> 1) * 8, 0, AUX_SC1);
      }
    }
    SB();
    __builtin_amdgcn_s_sleep(Q_WAIT);
    SB();
    {
      const int qvo = WS_QG + (kvh * 8 + b) * 8192 + w * 512 + (lane >> 5) * 2048 + (lane & 31) * 16;
      const u32 ta = tagA << 8, tb = tagB << 8;
      v4u g[2][2];
      for (unsigned spins = 0; spins < SPIN_CAP; ++spins) {
        MEM_FENCE();
        u32 bad = 0u;
#pragma unroll
        for (int br = 0; br < 2; ++br)
#pragma unroll
          for (int i = 0; i < 2; ++i) g[br][i] = __builtin_amdgcn_raw_buffer_load_b128(rw, qvo, br * 8 * 65536 + i * 4096, AUX_SC1);
#pragma unroll
        for (int br = 0; br < 2; ++br)
#pragma unroll
          for (int i = 0; i < 2; ++i) bad |= (g[br][i].y ^ ta) | (g[br][i].w ^ tb);
        if (all_lanes((bad >> 8) == 0u)) break;
        __builtin_amdgcn_s_sleep(4);
      }
#pragma unroll
      for (int br = 0; br < 2; ++br) {
        const f32x4 s = unpack4(g[br][0]) + unpack4(g[br][1]);
        const float u0 = red32(s[0], s[2]), u1 = red32(s[1], s[3]);
        *(LAS u32*)(L + L_Q + br * 1024 + w * 256 + (lane & 31) * 8 + (lane >> 5) * 4) = pack_bf16(u0, u1);
      }
      lds_post(L, F_Q, w);
    }
  }

  const int br = 1 - grp;
  const u32 ta = tagA << 8, tb = tagB << 8;
  auto bad4 = [=](const v4u& g) AI -> u32 { return (g.y ^ tagA) | (g.w ^ tagB); };
  auto badp = [=](const v4u& g) AI -> u32 { return ((g.y ^ ta) | (g.w ^ tb)) >> 8; };
  int a_po = WS_PG + (br * NWG + wg) * PG_WG + w * PG_HEAD + lane * 8;
  int a_pm = lane == 0 ? WS_ML + ((br * 8 + kvh) * 4 + w) * 512 + (b * 4 + qd) * 16 : OOB;
  int a_ml = WS_ML + ((br * 8 + kvh) * 4 + (ks >> 1)) * 512 + (w * 8 + (lane >> 4)) * 16;
  int a_o = WS_PG + (br * NWG + (2 * w * 8 + kvh) * 4 + (lane >> 4)) * PG_WG + (ks >> 1) * PG_HEAD + (ks & 1) * 256 + (lane & 15) * 16;
  const bool odd = lane & 1;
  int a_og = lane < 32 ? WS_OG + br * NWG * 4096 + (((2 * (b >> 1) + (w >> 1)) * 8 + 4 * (lane >> 4) + (odd ? 2 : 0)) * 4 + 2 * (w & 1)) * 4096 +
                             (kvh * 8 + ks) * 64 + ((lane & 15) >> 1) * 8
                       : OOB;
  u32 t_po = (lane & 1) ? tb : ta, t_og = (lane & 2) ? tb : ta;
  asm volatile("" : "+v"(a_po), "+v"(a_pm), "+v"(a_ml), "+v"(a_o), "+v"(a_og), "+v"(t_po), "+v"(t_og));

  lds_wait(L, F_REC + br, grp != 0);
  {
    LAS float* mrg = (LAS float*)(L + L_MRG) + br * 16 * MRG_REC;
    f32x2 ml4[4], o4[4];
#pragma unroll
    for (int ww = 0; ww < 4; ++ww) {
      ml4[ww] = *(LAS f32x2*)(mrg + (ww * 4 + w) * MRG_REC + 128);
      o4[ww] = *(LAS f32x2*)(mrg + (ww * 4 + w) * MRG_REC + 2 * lane);
    }
    float M = fmaxf(fmaxf(ml4[0].x, ml4[1].x), fmaxf(ml4[2].x, ml4[3].x));
    float Ls = 0.f, o0 = 0.f, o1 = 0.f;
#pragma unroll
    for (int ww = 0; ww < 4; ++ww) {
      const float f = __builtin_amdgcn_exp2f(ml4[ww].x - M);
      Ls = fmaf(f, ml4[ww].y, Ls);
      o0 = fmaf(f, o4[ww].x, o0);
      o1 = fmaf(f, o4[ww].y, o1);
    }
    v2u ph = packh(o0, o1, t_po);
    v4u wo4[2][4];
    if (grp == 1) {
      asm volatile("" : "+v"(ph), "+v"(M), "+v"(Ls));
      lds_wait(L, F_LAST, true);
#pragma unroll
      for (int bq = 0; bq < 2; ++bq) {
        const __amdgpu_buffer_rsrc_t ro = __builtin_amdgcn_make_buffer_rsrc((void*)p.wo[bq], 0, W_BYTES, 0x00020000);
        const int so = (kvh * 512 + 64 * ks + 16 * w) * (DM * 2) + (b >> 1) * 256;
#pragma unroll
        for (int i = 0; i < 4; ++i)
          wo4[bq][i] = __builtin_amdgcn_raw_buffer_load_b128(ro, (lane >> 4) * (DM * 2) + (lane & 15) * 16, so + i * 4 * (DM * 2), AUX_NT);
      }
      SB();
      lds_wait(L, F_REC + 1, HSLEEP);
    }
    __builtin_amdgcn_raw_buffer_store_b64(ph, rw, a_po, 0, AUX_SC1);
    __builtin_amdgcn_raw_buffer_store_b128(v4u{f2u(M), tagA, f2u(Ls), tagB}, rw, a_pm, 0, AUX_SC1);
    if (grp == 1) {
#pragma unroll
      for (int bq = 0; bq < 2; ++bq)
#pragma unroll
        for (int i = 0; i < 4; ++i) *(LAS v4u*)(L + L_W + (2 + bq) * 16384 + w * 4096 + i * 1024 + lane * 16) = wo4[bq][i];
      lds_post(L, F_WO, w);
    }
    lds_wait(L, F_WO, false);
  }
  v4u bf[2][2];
  {
    LAS unsigned char* rb = L + L_W + (2 + br) * 16384 + (8 * (lane >> 4) + ((lane >> 2) & 3)) * 256 + w * 64 + (lane & 3) * 8;
#pragma unroll
    for (int s = 0; s < 2; ++s)
#pragma unroll
      for (int c = 0; c < 2; ++c) {
        const v2u lo = tr_read(rb + s * 8192 + c * 32), hi = tr_read(rb + s * 8192 + c * 32 + 1024);
        bf[s][c] = v4u{lo.x, lo.y, hi.x, hi.y};
      }
  }
  v4u ml[2], gr[2];
  {
#pragma unroll
    for (int j = 0; j < 2; ++j) { ml[j] = v4u{0u, ~tagA, 0u, 0u}; gr[j] = v4u{0u, ~ta, 0u, 0u}; }
    bool few = false;
    if (H1_WAIT) __builtin_amdgcn_s_sleep(H1_WAIT);
    for (unsigned spins = 0; spins < SPIN_CAP; ++spins) {
      MEM_FENCE();
      v4u tm[2], t[2];
      bool wm[2], want[2];
#pragma unroll
      for (int j = 0; j < 2; ++j) {
        wm[j] = bad4(ml[j]) != 0u;
        want[j] = badp(gr[j]) != 0u && (few || !wm[j]);
        tm[j] = __builtin_amdgcn_raw_buffer_load_b128(rw, wm[j] ? a_ml + j * 64 : OOB, 0, AUX_SC1);
        t[j] = __builtin_amdgcn_raw_buffer_load_b128(rw, want[j] ? a_o + j * 32 * PG_WG : OOB, 0, AUX_SC1);
      }
      SB();
      u32 bad = 0u;
      int miss = 0;
#pragma unroll
      for (int j = 0; j < 2; ++j) {
        ml[j] = wm[j] ? tm[j] : ml[j];
        gr[j] = want[j] ? t[j] : gr[j];
        const u32 bm = bad4(ml[j]);
        miss += __builtin_popcountll(__builtin_amdgcn_ballot_w64(bm != 0u));
        bad |= bm | badp(gr[j]);
      }
      if (all_lanes(bad == 0u)) break;
      few = miss <= 16 * FEW;
      if (!few) __builtin_amdgcn_s_sleep(POLL_SLEEP);
    }
  }
  {
    LAS unsigned char* ol = L + L_O + br * 1024;
    const int r = lane >> 4;
#pragma unroll
    for (int j = 0; j < 2; ++j) {
      const float m = u2f(ml[j].x);
      const float e = __builtin_amdgcn_exp2f(m - rows_max(m));
      const float fw = e * __builtin_amdgcn_rcpf(rows_sum(e * u2f(ml[j].z)));
      const f32x4 a = unpack4(gr[j]) * fw;
      const float o = red16(red32(a[0], a[1]), red32(a[2], a[3]));
      *(LAS unsigned short*)(ol + (2 * w + j) * 128 + ((lane & 15) * 4 + ((r & 1) * 2 + (r >> 1))) * 2) = (unsigned short)(pack_bf16(o, o) & 0xffffu);
    }
    lds_post(L, F_O + br, w);
    lds_wait(L, F_O + br, false);
    const v4u af0 = *(LAS v4u*)(ol + (lane & 7) * 128 + (lane >> 4) * 16), af1 = *(LAS v4u*)(ol + (lane & 7) * 128 + 64 + (lane >> 4) * 16);
#pragma unroll
    for (int c = 0; c < 2; ++c) {
      f32x4 d = mfma32(af0, bf[0][c], f32x4{0.f, 0.f, 0.f, 0.f});
      d = mfma32(af1, bf[1][c], d);
      const float e0 = dpp<0xB1>(d[0]), e1 = dpp<0xB1>(d[1]), e2 = dpp<0xB1>(d[2]), e3 = dpp<0xB1>(d[3]);
      __builtin_amdgcn_raw_buffer_store_b64(packh(odd ? e2 : d[0], odd ? d[2] : e0, t_og), rw, a_og + c * 4096, 0, AUX_SC1);
      __builtin_amdgcn_raw_buffer_store_b64(packh(odd ? e3 : d[1], odd ? d[3] : e1, t_og), rw, a_og + c * 4096 + 4 * 4096, 0, AUX_SC1);
    }
  }
  if (w == 0) {
    v4u a[4];
#pragma unroll
    for (int i = 0; i < 4; ++i) a[i] = v4u{0u, ~ta, 0u, 0u};
    const int vo = WS_OG + (br * NWG + wg) * 4096 + lane * 16;
    __builtin_amdgcn_s_sleep(H2_WAIT);
    for (unsigned spins = 0; spins < SPIN_CAP; ++spins) {
      MEM_FENCE();
      v4u t[4];
#pragma unroll
      for (int i = 0; i < 4; ++i) t[i] = __builtin_amdgcn_raw_buffer_load_b128(rw, badp(a[i]) != 0u ? vo + i * 1024 : OOB, 0, AUX_SC1);
      SB();
      u32 bad = 0u;
#pragma unroll
      for (int i = 0; i < 4; ++i) { a[i] = badp(a[i]) != 0u ? t[i] : a[i]; bad |= badp(a[i]); }
      if (all_lanes(bad == 0u)) break;
      __builtin_amdgcn_s_sleep(H2_SLEEP);
    }
    f32x4 s = (unpack4(a[0]) + unpack4(a[1])) + (unpack4(a[2]) + unpack4(a[3]));
#pragma unroll
    for (int j = 0; j < 4; ++j) { s[j] += dpp<0x124>(s[j]); s[j] += dpp<0x128>(s[j]); }
    const float o = red16(red32(s[0], s[1]), red32(s[2], s[3]));
    const int r = lane >> 4;
    const __amdgpu_buffer_rsrc_t rout = __builtin_amdgcn_make_buffer_rsrc(p.out, 0, NB * 2 * DM * 2, 0x00020000);
    __builtin_amdgcn_raw_buffer_store_b16((unsigned short)(pack_bf16(o, o) & 0xffffu), rout,
                                          (lane & 15) < 4 ? (kvh * 1024 + br * 512 + b * 64 + qd * 16 + (lane & 3) * 4 + (r & 1) * 2 + (r >> 1)) * 2 : OOB, 0, AUX_SC1);
    if (wg == 0 && grp == 1 && lane == 0) __builtin_amdgcn_raw_buffer_store_b64(v2u{tagA, tagB}, rw, 0, WS_EPOCH, AUX_SC1);
  }
}

extern "C" void kernel_launch(void* const* d_in, const int* in_sizes, int n_in, void* d_out, int out_size, void* d_ws, size_t ws_size,
                              hipStream_t stream) {
  Params p;
  p.x[0] = d_in[0]; p.k[0] = d_in[1]; p.v[0] = d_in[2]; p.wq[0] = d_in[3]; p.wo[0] = d_in[4];
  p.x[1] = d_in[5]; p.k[1] = d_in[6]; p.v[1] = d_in[7]; p.wq[1] = d_in[8]; p.wo[1] = d_in[9];
  p.out = d_out;
  p.ws = (unsigned char*)d_ws;
  k_fused<<<NWG, NTHR, 0, stream>>>(p);
}
```

```cpp
#include <hip/hip_runtime.h>
#include <hip/hip_bf16.h>
#include <stdint.h>
#include <utility>

typedef unsigned u32;
typedef u32 v4u __attribute__((ext_vector_type(4)));
typedef u32 v2u __attribute__((ext_vector_type(2)));
typedef float f32x4 __attribute__((ext_vector_type(4)));
typedef float f32x2 __attribute__((ext_vector_type(2)));
typedef __bf16 bf16x8 __attribute__((ext_vector_type(8)));
typedef __bf16 bf16x2 __attribute__((ext_vector_type(2)));
typedef short v4s __attribute__((ext_vector_type(4)));
#define LAS __attribute__((address_space(3)))

#define NFLY 5
#define NWIDE 6
#define NHEAD 3
#define TAU 217
#define TBASE 510
#define Q_WAIT 32
#define FEW 4
#define POLL_SLEEP 4
#define TOUCH 1
#define HSLEEP true
#define H1_WAIT 6
#define H2_WAIT 16
#define H2_SLEEP 2

constexpr int NB = 8, DM = 512, HQ = 32, HKV = 8, SK = 8192, DH = 128;
constexpr int NWG = 256, NTHR = 512;
constexpr int RING = 16, TPI = RING / 4, NIT = 32 / TPI;
constexpr int KV_BYTES = NB * HKV * SK * DH * 2;
constexpr int W_BYTES = DM * HQ * DH * 2;
constexpr float CSC = 0.08838834764831845f * 1.4426950408889634f;
constexpr int AUX_NT = 2, AUX_SC1 = 16;
constexpr unsigned SPIN_CAP = 1u << 17, LSPIN_CAP = 1u << 22;
constexpr int OOB = 0x40000000;

constexpr int WS_EPOCH = 0;
constexpr int WS_QG = 4096;
constexpr int PG_HEAD = 512, PG_WG = 4 * PG_HEAD;
constexpr int WS_PG = WS_QG + 2 * 8 * 8 * 8192;
constexpr int WS_ML = WS_PG + 2 * NWG * PG_WG;
constexpr int WS_OG = WS_ML + 2 * 8 * 4 * 512;
constexpr int WS_END = WS_OG + 2 * NWG * 4096;

constexpr int MRG_REC = 132;
constexpr int L_W = 0;
constexpr int L_RED = 65536;
constexpr int L_MRG = L_RED + 16384;
constexpr int L_Q = L_MRG + 2 * 16 * MRG_REC * 4;
constexpr int L_SC = L_Q + 2048;
constexpr int L_O = L_SC + 32768;
constexpr int L_FLAG = L_O + 2048;
constexpr int L_END = L_FLAG + 128 + 16;
enum { F_Q = 0, F_LAST = 1, F_REC = 2  , F_PROJ = 4, F_WO = 5, F_O = 6   };

struct Params {
  const void* x[2];
  const void* k[2];
  const void* v[2];
  const void* wq[2];
  const void* wo[2];
  void* out;
  unsigned char* ws;
};

__device__ __forceinline__ f32x4 mfma32(v4u a, v4u b, f32x4 c) {
  return __builtin_amdgcn_mfma_f32_16x16x32_bf16(__builtin_bit_cast(bf16x8, a), __builtin_bit_cast(bf16x8, b), c, 0, 0, 0);
}
__device__ __forceinline__ f32x4 mfma16(v2u a, v2u b, f32x4 c) {
  return __builtin_amdgcn_mfma_f32_16x16x16bf16_1k(__builtin_bit_cast(v4s, a), __builtin_bit_cast(v4s, b), c, 0, 0, 0);
}
__device__ __forceinline__ u32 pack_bf16(float lo, float hi) {
  f32x2 f = {lo, hi};
  return __builtin_bit_cast(u32, __builtin_convertvector(f, bf16x2));
}
__device__ __forceinline__ v2u tr_read(LAS unsigned char* p) {
  return __builtin_bit_cast(v2u, __builtin_amdgcn_ds_read_tr16_b64_v4i16((LAS v4s*)p));
}
__device__ __forceinline__ u32 f2u(float f) { return __builtin_bit_cast(u32, f); }
__device__ __forceinline__ float u2f(u32 u) { return __builtin_bit_cast(float, u); }
template <int CTRL> __device__ __forceinline__ float dpp(float x) {
  return u2f((u32)__builtin_amdgcn_mov_dpp((int)f2u(x), CTRL, 0xf, 0xf, true));
}
__device__ __forceinline__ v2u packh(float v0, float v1, u32 tag8) {
  const u32 a = f2u(v0) + 0x800u, c = f2u(v1) + 0x800u;
  return v2u{(a >> 12) | ((c << 8) & 0xFFF00000u), (c >> 24) | tag8};
}
__device__ __forceinline__ f32x4 unpack4(v4u g) {
  return f32x4{u2f(g.x << 12), u2f(((g.x >> 20) << 12) | (g.y << 24)), u2f(g.z << 12), u2f(((g.z >> 20) << 12) | (g.w << 24))};
}
__device__ __forceinline__ float red32(float a, float b) {
  const auto s = __builtin_amdgcn_permlane32_swap(f2u(a), f2u(b), false, false);
  return u2f(s[0]) + u2f(s[1]);
}
__device__ __forceinline__ float red16(float a, float b) {
  const auto s = __builtin_amdgcn_permlane16_swap(f2u(a), f2u(b), false, false);
  return u2f(s[0]) + u2f(s[1]);
}
__device__ __forceinline__ float rows_max(float x) {
  const auto s = __builtin_amdgcn_permlane16_swap(f2u(x), f2u(x), false, false);
  x = fmaxf(u2f(s[0]), u2f(s[1]));
  const auto t = __builtin_amdgcn_permlane32_swap(f2u(x), f2u(x), false, false);
  return fmaxf(u2f(t[0]), u2f(t[1]));
}
__device__ __forceinline__ float rows_sum(float x) { x = red16(x, x); return red32(x, x); }
#define MEM_FENCE() asm volatile("" ::: "memory")
#define SB() __builtin_amdgcn_sched_barrier(0)
#define WAITVM(n) do { asm volatile("s_waitcnt vmcnt(%0)" :: "i"(n) : "memory"); SB(); } while (0)
#define WAITVM2(x, y, n, m) do { asm volatile("s_cmp_gt_u32 %0, %1\n\ts_cbranch_scc1 1f\n\ts_waitcnt vmcnt(%2)\n1:\n\ts_waitcnt vmcnt(%3)" :: "s"(x), "s"(y), "i"(n), "i"(m) : "memory", "scc"); SB(); } while (0)
#define AI __attribute__((always_inline))
__device__ __forceinline__ bool all_lanes(bool c) { return __builtin_amdgcn_ballot_w64(c) == ~0ull; }
template <int... I, class F> __device__ __forceinline__ void static_for_impl(std::integer_sequence<int, I...>, F&& f) {
  (f(std::integral_constant<int, I>{}), ...);
}
template <int N, class F> __device__ __forceinline__ void static_for(F&& f) { static_for_impl(std::make_integer_sequence<int, N>{}, f); }

__device__ __forceinline__ void lds_post(LAS unsigned char* L, int slot, int w) {
  MEM_FENCE();
  *(volatile LAS u32*)(L + L_FLAG + slot * 16 + w * 4) = 1u;
  MEM_FENCE();
}
__device__ __forceinline__ void lds_wait(LAS unsigned char* L, int slot, bool sleep) {
  for (unsigned spins = 0; spins < LSPIN_CAP; ++spins) {
    MEM_FENCE();
    const v4u v = *(volatile LAS v4u*)(L + L_FLAG + slot * 16);
    if (__builtin_amdgcn_readfirstlane(v.x & v.y & v.z & v.w) != 0u) break;
    if (sleep) __builtin_amdgcn_s_sleep(1);
  }
  MEM_FENCE();
}

__global__ void __launch_bounds__(NTHR, 2) k_fused(Params p) {
  __shared__ __attribute__((aligned(16))) unsigned char smem[L_END];
  LAS unsigned char* L = (LAS unsigned char*)smem;
  const int lane = threadIdx.x & 63, w8 = __builtin_amdgcn_readfirstlane(threadIdx.x >> 6), w = w8 & 3;
  const int grp = w8 >> 2;
  const int wg = blockIdx.x, qd = wg & 3, ub = wg >> 2, b = ub >> 3, kvh = ub & 7;
  const int ks = (b & 1) * 4 + qd;
  const __amdgpu_buffer_rsrc_t rw = __builtin_amdgcn_make_buffer_rsrc((void*)p.ws, 0, WS_END, 0x00020000);
  u32 tagA, tagB;
  auto touch = [&]() AI {
    const __amdgpu_buffer_rsrc_t rout = __builtin_amdgcn_make_buffer_rsrc(p.out, 0, NB * 2 * DM * 2, 0x00020000);
    const __amdgpu_buffer_rsrc_t ro0 = __builtin_amdgcn_make_buffer_rsrc((void*)p.wo[0], 0, W_BYTES, 0x00020000);
    const __amdgpu_buffer_rsrc_t ro1 = __builtin_amdgcn_make_buffer_rsrc((void*)p.wo[1], 0, W_BYTES, 0x00020000);
    const int one = lane == 0 ? 0 : OOB, so = (kvh * 512 + 64 * ks) * (DM * 2);
    u32 t0 = __builtin_amdgcn_raw_buffer_load_b32(rw, lane < 8 ? WS_PG + lane * 524288 : OOB, 0, AUX_NT);
    u32 t1 = __builtin_amdgcn_raw_buffer_load_b32(rout, one, 0, AUX_NT);
    u32 t2 = __builtin_amdgcn_raw_buffer_load_b32(ro0, one, so, AUX_NT);
    u32 t3 = __builtin_amdgcn_raw_buffer_load_b32(ro1, one, so, AUX_NT);
    asm volatile("" :: "v"(t0), "v"(t1), "v"(t2), "v"(t3));
  };

  if (grp == 0) {
    const u32 t0 = (u32)__builtin_amdgcn_s_memrealtime();
    const int voffK = ((lane >> 2) & 3) * 256 + (lane >> 4) * 64 + (lane & 3) * 16;
    const int voffV = lane * 16;
    const int sbase = wg * 524288 + w * 4096;
    v4u ring[RING];
    {
      const __amdgpu_buffer_rsrc_t rk = __builtin_amdgcn_make_buffer_rsrc((void*)p.k[0], 0, KV_BYTES, 0x00020000);
      static_for<RING>([&](auto I) AI {
        constexpr int i = decltype(I)::value;
        if constexpr (i == NHEAD) __builtin_amdgcn_s_barrier();
        if constexpr (i >= NHEAD) WAITVM(NHEAD - 1);
        ring[i] = __builtin_amdgcn_raw_buffer_load_b128(rk, voffK + (i & 3) * 1024, sbase + (i >> 2) * 16384, AUX_NT);
        SB();
      });
    }
    const int jq = lane & 3;
    const u32 m0 = jq == 0 ? 0x0000ffffu : (jq == 1 ? 0xffff0000u : 0u);
    const u32 m1 = jq == 2 ? 0x0000ffffu : (jq == 3 ? 0xffff0000u : 0u);
    LAS unsigned char* scl = L + L_SC + w * 8192 + (lane >> 2) * 16;
    u32 due = TBASE - TAU;
    lds_wait(L, F_Q, false);
    tagA = __builtin_amdgcn_readfirstlane(*(LAS u32*)(L + L_FLAG + 128));
    tagB = __builtin_amdgcn_readfirstlane(*(LAS u32*)(L + L_FLAG + 132));
#pragma unroll 1
    for (int br = 0; br < 2; ++br) {
      const v4u qf = *(LAS v4u*)(L + L_Q + br * 1024 + voffK);
      const void* pk = p.k[br];
      const void* pv = p.v[br];
      float mx = -INFINITY;
#pragma unroll 1
      for (int it = 0; it < NIT; ++it) {
        const bool last = it == NIT - 1;
        const u32 now = (u32)__builtin_amdgcn_s_memrealtime() - t0;
        due += TAU;
        const __amdgpu_buffer_rsrc_t rn = __builtin_amdgcn_make_buffer_rsrc((void*)(last ? pv : pk), 0, KV_BYTES, 0x00020000);
        const int vn = last ? voffV : voffK;
        const int sn = sbase + (last ? 0 : (it + 1) * TPI * 16384);
        f32x4 st;
        static_for<RING>([&](auto I) AI {
          constexpr int i = decltype(I)::value;
          const f32x4 d = mfma32(ring[i], qf, f32x4{0.f, 0.f, 0.f, 0.f});
          float s = (dpp<0x00>(d[0]) + dpp<0x55>(d[1])) + (dpp<0xAA>(d[2]) + dpp<0xFF>(d[3]));
          mx = fmaxf(mx, s);
          asm volatile("" : "+v"(s), "+v"(mx));
          st[i & 3] = s;
          if constexpr ((i & 3) == 3) { if (jq == 0) *(LAS f32x4*)(scl + (it * TPI + (i >> 2)) * 256) = st; }
          SB();
          WAITVM2(now, due, NFLY - 1, NWIDE - 1);
          ring[i] = __builtin_amdgcn_raw_buffer_load_b128(rn, vn + (i & 3) * 1024, sn + (i >> 2) * 16384, AUX_NT);
          SB();
        });
      }
      const float mc = rows_max(mx) * CSC;
      float lsum = 0.f;
      f32x4 accLo = {0.f, 0.f, 0.f, 0.f}, accHi = {0.f, 0.f, 0.f, 0.f};
#pragma unroll 1
      for (int it = 0; it < NIT; ++it) {
        const bool last = it == NIT - 1;
        const u32 now = (u32)__builtin_amdgcn_s_memrealtime() - t0;
        due += TAU;
        const __amdgpu_buffer_rsrc_t rn =
            __builtin_amdgcn_make_buffer_rsrc((void*)(last ? p.k[1] : pv), 0, (last && br == 1) ? 0 : KV_BYTES, 0x00020000);
        const int vn = last ? voffK : voffV;
        const int sn = sbase + (last ? 0 : (it + 1) * TPI * 16384);
        if (last && br == 1) lds_post(L, F_LAST, w);
        f32x4 sv[TPI];
#pragma unroll
        for (int k = 0; k < TPI; ++k) sv[k] = *(LAS f32x4*)(scl + (it * TPI + k) * 256);
        static_for<RING>([&](auto I) AI {
          constexpr int i = decltype(I)::value;
          const float pe = __builtin_amdgcn_exp2f(fmaf(sv[i >> 2][i & 3], CSC, -mc));
          lsum += pe;
          asm volatile("" : "+v"(lsum));
          const u32 pkd = pack_bf16(pe, pe);
          const v2u a = {pkd & m0, pkd & m1};
          accLo = mfma16(a, v2u{ring[i].x, ring[i].y}, accLo);
          accHi = mfma16(a, v2u{ring[i].z, ring[i].w}, accHi);
          SB();
          WAITVM2(now, due, NFLY - 1, NWIDE - 1);
          ring[i] = __builtin_amdgcn_raw_buffer_load_b128(rn, vn + (i & 3) * 1024, sn + (i >> 2) * 16384, AUX_NT);
          SB();
        });
      }
      lsum = rows_sum(lsum);
      LAS float* mrg = (LAS float*)(L + L_MRG) + br * 16 * MRG_REC;
      LAS float* rec = mrg + (w * 4 + (lane >> 4)) * MRG_REC + 8 * (lane & 15);
      *(LAS f32x4*)rec = accLo;
      *(LAS f32x4*)(rec + 4) = accHi;
      if (lane < 16 && jq == 0) *(LAS f32x2*)(mrg + (w * 4 + (lane >> 2)) * MRG_REC + 128) = f32x2{mc, lsum};
      lds_post(L, F_REC + br, w);
    }
  } else {
    const int tid = w * 64 + lane;
    const v2u ep = __builtin_amdgcn_raw_buffer_load_b64(rw, 0, WS_EPOCH, AUX_SC1);
    v4u wr[2][4], xf[2];
#pragma unroll
    for (int br = 0; br < 2; ++br) {
      const __amdgpu_buffer_rsrc_t rq = __builtin_amdgcn_make_buffer_rsrc((void*)p.wq[br], 0, W_BYTES, 0x00020000);
      const __amdgpu_buffer_rsrc_t rx = __builtin_amdgcn_make_buffer_rsrc((void*)p.x[br], 0, NB * DM * 2, 0x00020000);
      const int so = (128 * qd + 32 * w) * (HQ * DH * 2) + (kvh * 512 + b * 64) * 2;
#pragma unroll
      for (int i = 0; i < 4; ++i)
        wr[br][i] = __builtin_amdgcn_raw_buffer_load_b128(rq, (lane >> 3) * (HQ * DH * 2) + (lane & 7) * 16, so + i * 8 * (HQ * DH * 2), AUX_NT);
      xf[br] = __builtin_amdgcn_raw_buffer_load_b128(rx, (lane & 7) * (DM * 2) + (lane >> 4) * 16, (128 * qd + 32 * w) * 2, 0);
      SB();
    }
    if (w == 0 && lane < 32) ((LAS u32*)(L + L_FLAG))[lane] = 0u;
    asm volatile("s_waitcnt lgkmcnt(0)" ::: "memory");
    __builtin_amdgcn_s_barrier();
    tagA = __builtin_amdgcn_readfirstlane(ep.x) + 1u;
    tagB = __builtin_amdgcn_readfirstlane(ep.y) + 0x9E3779B9u;
    if (lane == 0) *(LAS v2u*)(L + L_FLAG + 128) = v2u{tagA, tagB};
#pragma unroll
    for (int br = 0; br < 2; ++br) {
      LAS unsigned char* wl = L + L_W + br * 16384 + w * 4096;
#pragma unroll
      for (int i = 0; i < 4; ++i) *(LAS v4u*)(wl + i * 1024 + lane * 16) = wr[br][i];
      LAS unsigned char* rb = wl + (8 * (lane >> 4) + ((lane >> 2) & 3)) * 128 + (lane & 3) * 8;
      LAS f32x4* red = (LAS f32x4*)(L + L_RED + br * 8192);
#pragma unroll
      for (int ct = 0; ct < 4; ++ct) {
        const v2u lo = tr_read(rb + ct * 32), hi = tr_read(rb + ct * 32 + 512);
        const f32x4 d = mfma32(xf[br], v4u{lo.x, lo.y, hi.x, hi.y}, f32x4{0.f, 0.f, 0.f, 0.f});
        if (lane < 32) red[(w * 4 + ct) * 32 + lane] = d;
      }
    }
    lds_post(L, F_PROJ, w);
    lds_wait(L, F_PROJ, false);
    {
      const int pb = 4 * ((tid >> 3) & 1) + ((tid >> 4) & 3), pn = 16 * w + 2 * (tid & 7);
      const u32 tag8 = ((tid & 1) ? tagB : tagA) << 8;
#pragma unroll
      for (int br = 0; br < 2; ++br) {
        LAS float* rf = (LAS float*)(L + L_RED + br * 8192) + (w * 32 + 16 * ((tid >> 3) & 1) + 2 * (tid & 7)) * 4 + ((tid >> 4) & 3);
        float s0 = 0.f, s1 = 0.f;
#pragma unroll
        for (int ww = 0; ww < 4; ++ww) { s0 += rf[ww * 512]; s1 += rf[ww * 512 + 4]; }
        __builtin_amdgcn_raw_buffer_store_b64(packh(s0, s1, tag8), rw, WS_QG + (br * 8 + kvh) * 65536 + pb * 8192 + qd * 2048 + b * 256 + (pn >> 1) * 8, 0, AUX_SC1);
      }
    }
    SB();
    __builtin_amdgcn_s_sleep(Q_WAIT);
    SB();
    {
      const int qvo = WS_QG + (kvh * 8 + b) * 8192 + w * 512 + (lane >> 5) * 2048 + (lane & 31) * 16;
      const u32 ta = tagA << 8, tb = tagB << 8;
      v4u g[2][2];
      for (unsigned spins = 0; spins < SPIN_CAP; ++spins) {
        MEM_FENCE();
        u32 bad = 0u;
#pragma unroll
        for (int br = 0; br < 2; ++br)
#pragma unroll
          for (int i = 0; i < 2; ++i) g[br][i] = __builtin_amdgcn_raw_buffer_load_b128(rw, qvo, br * 8 * 65536 + i * 4096, AUX_SC1);
#pragma unroll
        for (int br = 0; br < 2; ++br)
#pragma unroll
          for (int i = 0; i < 2; ++i) bad |= (g[br][i].y ^ ta) | (g[br][i].w ^ tb);
        if (all_lanes((bad >> 8) == 0u)) break;
        __builtin_amdgcn_s_sleep(4);
      }
#pragma unroll
      for (int br = 0; br < 2; ++br) {
        const f32x4 s = unpack4(g[br][0]) + unpack4(g[br][1]);
        const float u0 = red32(s[0], s[2]), u1 = red32(s[1], s[3]);
        *(LAS u32*)(L + L_Q + br * 1024 + w * 256 + (lane & 31) * 8 + (lane >> 5) * 4) = pack_bf16(u0, u1);
      }
      lds_post(L, F_Q, w);
      if ((TOUCH & 1) && w == 0) touch();
    }
  }

  const int br = 1 - grp;
  const u32 ta = tagA << 8, tb = tagB << 8;
  auto bad4 = [=](const v4u& g) AI -> u32 { return (g.y ^ tagA) | (g.w ^ tagB); };
  auto badp = [=](const v4u& g) AI -> u32 { return ((g.y ^ ta) | (g.w ^ tb)) >> 8; };
  int a_po = WS_PG + (br * NWG + wg) * PG_WG + w * PG_HEAD + lane * 8;
  int a_pm = lane == 0 ? WS_ML + ((br * 8 + kvh) * 4 + w) * 512 + (b * 4 + qd) * 16 : OOB;
  int a_ml = WS_ML + ((br * 8 + kvh) * 4 + (ks >> 1)) * 512 + (w * 8 + (lane >> 4)) * 16;
  int a_o = WS_PG + (br * NWG + (2 * w * 8 + kvh) * 4 + (lane >> 4)) * PG_WG + (ks >> 1) * PG_HEAD + (ks & 1) * 256 + (lane & 15) * 16;
  const bool odd = lane & 1;
  int a_og = lane < 32 ? WS_OG + br * NWG * 4096 + (((2 * (b >> 1) + (w >> 1)) * 8 + 4 * (lane >> 4) + (odd ? 2 : 0)) * 4 + 2 * (w & 1)) * 4096 +
                             (kvh * 8 + ks) * 64 + ((lane & 15) >> 1) * 8
                       : OOB;
  u32 t_po = (lane & 1) ? tb : ta, t_og = (lane & 2) ? tb : ta;
  asm volatile("" : "+v"(a_po), "+v"(a_pm), "+v"(a_ml), "+v"(a_o), "+v"(a_og), "+v"(t_po), "+v"(t_og));

  lds_wait(L, F_REC + br, grp != 0);
  {
    LAS float* mrg = (LAS float*)(L + L_MRG) + br * 16 * MRG_REC;
    f32x2 ml4[4], o4[4];
#pragma unroll
    for (int ww = 0; ww < 4; ++ww) {
      ml4[ww] = *(LAS f32x2*)(mrg + (ww * 4 + w) * MRG_REC + 128);
      o4[ww] = *(LAS f32x2*)(mrg + (ww * 4 + w) * MRG_REC + 2 * lane);
    }
    float M = fmaxf(fmaxf(ml4[0].x, ml4[1].x), fmaxf(ml4[2].x, ml4[3].x));
    float Ls = 0.f, o0 = 0.f, o1 = 0.f;
#pragma unroll
    for (int ww = 0; ww < 4; ++ww) {
      const float f = __builtin_amdgcn_exp2f(ml4[ww].x - M);
      Ls = fmaf(f, ml4[ww].y, Ls);
      o0 = fmaf(f, o4[ww].x, o0);
      o1 = fmaf(f, o4[ww].y, o1);
    }
    v2u ph = packh(o0, o1, t_po);
    v4u wo4[2][4];
    if (grp == 1) {
      asm volatile("" : "+v"(ph), "+v"(M), "+v"(Ls));
      if ((TOUCH & 2) && w == 0) touch();
      lds_wait(L, F_LAST, true);
#pragma unroll
      for (int bq = 0; bq < 2; ++bq) {
        const __amdgpu_buffer_rsrc_t ro = __builtin_amdgcn_make_buffer_rsrc((void*)p.wo[bq], 0, W_BYTES, 0x00020000);
        const int so = (kvh * 512 + 64 * ks + 16 * w) * (DM * 2) + (b >> 1) * 256;
#pragma unroll
        for (int i = 0; i < 4; ++i)
          wo4[bq][i] = __builtin_amdgcn_raw_buffer_load_b128(ro, (lane >> 4) * (DM * 2) + (lane & 15) * 16, so + i * 4 * (DM * 2), AUX_NT);
      }
      SB();
      lds_wait(L, F_REC + 1, HSLEEP);
    }
    __builtin_amdgcn_raw_buffer_store_b64(ph, rw, a_po, 0, AUX_SC1);
    __builtin_amdgcn_raw_buffer_store_b128(v4u{f2u(M), tagA, f2u(Ls), tagB}, rw, a_pm, 0, AUX_SC1);
    if (grp == 1) {
#pragma unroll
      for (int bq = 0; bq < 2; ++bq)
#pragma unroll
        for (int i = 0; i < 4; ++i) *(LAS v4u*)(L + L_W + (2 + bq) * 16384 + w * 4096 + i * 1024 + lane * 16) = wo4[bq][i];
      lds_post(L, F_WO, w);
    }
    lds_wait(L, F_WO, false);
  }
  v4u bf[2][2];
  {
    LAS unsigned char* rb = L + L_W + (2 + br) * 16384 + (8 * (lane >> 4) + ((lane >> 2) & 3)) * 256 + w * 64 + (lane & 3) * 8;
#pragma unroll
    for (int s = 0; s < 2; ++s)
#pragma unroll
      for (int c = 0; c < 2; ++c) {
        const v2u lo = tr_read(rb + s * 8192 + c * 32), hi = tr_read(rb + s * 8192 + c * 32 + 1024);
        bf[s][c] = v4u{lo.x, lo.y, hi.x, hi.y};
      }
  }
  v4u ml[2], gr[2];
  {
#pragma unroll
    for (int j = 0; j < 2; ++j) { ml[j] = v4u{0u, ~tagA, 0u, 0u}; gr[j] = v4u{0u, ~ta, 0u, 0u}; }
    bool few = false;
    if (H1_WAIT) __builtin_amdgcn_s_sleep(H1_WAIT);
    for (unsigned spins = 0; spins < SPIN_CAP; ++spins) {
      MEM_FENCE();
      v4u tm[2], t[2];
      bool wm[2], want[2];
#pragma unroll
      for (int j = 0; j < 2; ++j) {
        wm[j] = bad4(ml[j]) != 0u;
        want[j] = badp(gr[j]) != 0u && (few || !wm[j]);
        tm[j] = __builtin_amdgcn_raw_buffer_load_b128(rw, wm[j] ? a_ml + j * 64 : OOB, 0, AUX_SC1);
        t[j] = __builtin_amdgcn_raw_buffer_load_b128(rw, want[j] ? a_o + j * 32 * PG_WG : OOB, 0, AUX_SC1);
      }
      SB();
      u32 bad = 0u;
      int miss = 0;
#pragma unroll
      for (int j = 0; j < 2; ++j) {
        ml[j] = wm[j] ? tm[j] : ml[j];
        gr[j] = want[j] ? t[j] : gr[j];
        const u32 bm = bad4(ml[j]);
        miss += __builtin_popcountll(__builtin_amdgcn_ballot_w64(bm != 0u));
        bad |= bm | badp(gr[j]);
      }
      if (all_lanes(bad == 0u)) break;
      few = miss <= 16 * FEW;
      if (!few) __builtin_amdgcn_s_sleep(POLL_SLEEP);
    }
  }
  {
    LAS unsigned char* ol = L + L_O + br * 1024;
    const int r = lane >> 4;
#pragma unroll
    for (int j = 0; j < 2; ++j) {
      const float m = u2f(ml[j].x);
      const float e = __builtin_amdgcn_exp2f(m - rows_max(m));
      const float fw = e * __builtin_amdgcn_rcpf(rows_sum(e * u2f(ml[j].z)));
      const f32x4 a = unpack4(gr[j]) * fw;
      const float o = red16(red32(a[0], a[1]), red32(a[2], a[3]));
      *(LAS unsigned short*)(ol + (2 * w + j) * 128 + ((lane & 15) * 4 + ((r & 1) * 2 + (r >> 1))) * 2) = (unsigned short)(pack_bf16(o, o) & 0xffffu);
    }
    lds_post(L, F_O + br, w);
    lds_wait(L, F_O + br, false);
    const v4u af0 = *(LAS v4u*)(ol + (lane & 7) * 128 + (lane >> 4) * 16), af1 = *(LAS v4u*)(ol + (lane & 7) * 128 + 64 + (lane >> 4) * 16);
#pragma unroll
    for (int c = 0; c < 2; ++c) {
      f32x4 d = mfma32(af0, bf[0][c], f32x4{0.f, 0.f, 0.f, 0.f});
      d = mfma32(af1, bf[1][c], d);
      const float e0 = dpp<0xB1>(d[0]), e1 = dpp<0xB1>(d[1]), e2 = dpp<0xB1>(d[2]), e3 = dpp<0xB1>(d[3]);
      __builtin_amdgcn_raw_buffer_store_b64(packh(odd ? e2 : d[0], odd ? d[2] : e0, t_og), rw, a_og + c * 4096, 0, AUX_SC1);
      __builtin_amdgcn_raw_buffer_store_b64(packh(odd ? e3 : d[1], odd ? d[3] : e1, t_og), rw, a_og + c * 4096 + 4 * 4096, 0, AUX_SC1);
    }
  }
  if (w == 0) {
    v4u a[4];
#pragma unroll
    for (int i = 0; i < 4; ++i) a[i] = v4u{0u, ~ta, 0u, 0u};
    const int vo = WS_OG + (br * NWG + wg) * 4096 + lane * 16;
    __builtin_amdgcn_s_sleep(H2_WAIT);
    for (unsigned spins = 0; spins < SPIN_CAP; ++spins) {
      MEM_FENCE();
      v4u t[4];
#pragma unroll
      for (int i = 0; i < 4; ++i) t[i] = __builtin_amdgcn_raw_buffer_load_b128(rw, badp(a[i]) != 0u ? vo + i * 1024 : OOB, 0, AUX_SC1);
      SB();
      u32 bad = 0u;
#pragma unroll
      for (int i = 0; i < 4; ++i) { a[i] = badp(a[i]) != 0u ? t[i] : a[i]; bad |= badp(a[i]); }
      if (all_lanes(bad == 0u)) break;
      __builtin_amdgcn_s_sleep(H2_SLEEP);
    }
    f32x4 s = (unpack4(a[0]) + unpack4(a[1])) + (unpack4(a[2]) + unpack4(a[3]));
#pragma unroll
    for (int j = 0; j < 4; ++j) { s[j] += dpp<0x124>(s[j]); s[j] += dpp<0x128>(s[j]); }
    const float o = red16(red32(s[0], s[1]), red32(s[2], s[3]));
    const int r = lane >> 4;
    const __amdgpu_buffer_rsrc_t rout = __builtin_amdgcn_make_buffer_rsrc(p.out, 0, NB * 2 * DM * 2, 0x00020000);
    __builtin_amdgcn_raw_buffer_store_b16((unsigned short)(pack_bf16(o, o) & 0xffffu), rout,
                                          (lane & 15) < 4 ? (kvh * 1024 + br * 512 + b * 64 + qd * 16 + (lane & 3) * 4 + (r & 1) * 2 + (r >> 1)) * 2 : OOB, 0, AUX_SC1);
    if (wg == 0 && grp == 1 && lane == 0) __builtin_amdgcn_raw_buffer_store_b64(v2u{tagA, tagB}, rw, 0, WS_EPOCH, AUX_SC1);
  }
}

extern "C" void kernel_launch(void* const* d_in, const int* in_sizes, int n_in, void* d_out, int out_size, void* d_ws, size_t ws_size,
                              hipStream_t stream) {
  Params p;
  p.x[0] = d_in[0]; p.k[0] = d_in[1]; p.v[0] = d_in[2]; p.wq[0] = d_in[3]; p.wo[0] = d_in[4];
  p.x[1] = d_in[5]; p.k[1] = d_in[6]; p.v[1] = d_in[7]; p.wq[1] = d_in[8]; p.wo[1] = d_in[9];
  p.out = d_out;
  p.ws = (unsigned char*)d_ws;
  k_fused<<<NWG, NTHR, 0, stream>>>(p);
}
```

```cpp
#include <hip/hip_runtime.h>
#include <hip/hip_bf16.h>
#include <stdint.h>
#include <utility>

typedef unsigned u32;
typedef u32 v4u __attribute__((ext_vector_type(4)));
typedef u32 v2u __attribute__((ext_vector_type(2)));
typedef float f32x4 __attribute__((ext_vector_type(4)));
typedef float f32x2 __attribute__((ext_vector_type(2)));
typedef __bf16 bf16x8 __attribute__((ext_vector_type(8)));
typedef __bf16 bf16x2 __attribute__((ext_vector_type(2)));
typedef short v4s __attribute__((ext_vector_type(4)));
#define LAS __attribute__((address_space(3)))

#define NFLY 5
#define NWIDE 6
#define NHEAD 3
#define TAU 217
#define TBASE 510
#define Q_WAIT 32
#define FEW 4
#define POLL_SLEEP 4
#define HSLEEP true
#define H1_WAIT 6
#define H2_WAIT 16
#define H2_SLEEP 2

constexpr int NB = 8, DM = 512, HQ = 32, HKV = 8, SK = 8192, DH = 128;
constexpr int NWG = 256, NTHR = 512;
constexpr int RING = 16, TPI = RING / 4, NIT = 32 / TPI;
constexpr int KV_BYTES = NB * HKV * SK * DH * 2;
constexpr int W_BYTES = DM * HQ * DH * 2;
constexpr float CSC = 0.08838834764831845f * 1.4426950408889634f;
constexpr int AUX_NT = 2, AUX_SC1 = 16;
constexpr unsigned SPIN_CAP = 1u << 17, LSPIN_CAP = 1u << 22;
constexpr int OOB = 0x40000000;

constexpr int WS_EPOCH = 0;
constexpr int WS_QG = 4096;
constexpr int PG_HEAD = 512, PG_WG = 4 * PG_HEAD;
constexpr int WS_PG = WS_QG + 2 * 8 * 8 * 8192;
constexpr int WS_ML = WS_PG + 2 * NWG * PG_WG;
constexpr int WS_OG = WS_ML + 2 * 8 * 4 * 512;
constexpr int WS_END = WS_OG + 2 * NWG * 4096;

constexpr int MRG_REC = 132;
constexpr int L_W = 0;
constexpr int L_RED = 65536;
constexpr int L_MRG = L_RED + 16384;
constexpr int L_Q = L_MRG + 2 * 16 * MRG_REC * 4;
constexpr int L_SC = L_Q + 2048;
constexpr int L_O = L_SC + 32768;
constexpr int L_FLAG = L_O + 2048;
constexpr int L_END = L_FLAG + 128 + 16;
enum { F_Q = 0, F_LAST = 1, F_REC = 2  , F_PROJ = 4, F_WO = 5, F_O = 6   };

struct Params {
  const void* x[2];
  const void* k[2];
  const void* v[2];
  const void* wq[2];
  const void* wo[2];
  void* out;
  unsigned char* ws;
};

__device__ __forceinline__ f32x4 mfma32(v4u a, v4u b, f32x4 c) {
  return __builtin_amdgcn_mfma_f32_16x16x32_bf16(__builtin_bit_cast(bf16x8, a), __builtin_bit_cast(bf16x8, b), c, 0, 0, 0);
}
__device__ __forceinline__ f32x4 mfma16(v2u a, v2u b, f32x4 c) {
  return __builtin_amdgcn_mfma_f32_16x16x16bf16_1k(__builtin_bit_cast(v4s, a), __builtin_bit_cast(v4s, b), c, 0, 0, 0);
}
__device__ __forceinline__ u32 pack_bf16(float lo, float hi) {
  f32x2 f = {lo, hi};
  return __builtin_bit_cast(u32, __builtin_convertvector(f, bf16x2));
}
__device__ __forceinline__ v2u tr_read(LAS unsigned char* p) {
  return __builtin_bit_cast(v2u, __builtin_amdgcn_ds_read_tr16_b64_v4i16((LAS v4s*)p));
}
__device__ __forceinline__ u32 f2u(float f) { return __builtin_bit_cast(u32, f); }
__device__ __forceinline__ float u2f(u32 u) { return __builtin_bit_cast(float, u); }
template <int CTRL> __device__ __forceinline__ float dpp(float x) {
  return u2f((u32)__builtin_amdgcn_mov_dpp((int)f2u(x), CTRL, 0xf, 0xf, true));
}
__device__ __forceinline__ v2u packh(float v0, float v1, u32 tag8) {
  const u32 a = f2u(v0) + 0x800u, c = f2u(v1) + 0x800u;
  return v2u{(a >> 12) | ((c << 8) & 0xFFF00000u), (c >> 24) | tag8};
}
__device__ __forceinline__ f32x4 unpack4(v4u g) {
  return f32x4{u2f(g.x << 12), u2f(((g.x >> 20) << 12) | (g.y << 24)), u2f(g.z << 12), u2f(((g.z >> 20) << 12) | (g.w << 24))};
}
__device__ __forceinline__ float red32(float a, float b) {
  const auto s = __builtin_amdgcn_permlane32_swap(f2u(a), f2u(b), false, false);
  return u2f(s[0]) + u2f(s[1]);
}
__device__ __forceinline__ float red16(float a, float b) {
  const auto s = __builtin_amdgcn_permlane16_swap(f2u(a), f2u(b), false, false);
  return u2f(s[0]) + u2f(s[1]);
}
__device__ __forceinline__ float rows_max(float x) {
  const auto s = __builtin_amdgcn_permlane16_swap(f2u(x), f2u(x), false, false);
  x = fmaxf(u2f(s[0]), u2f(s[1]));
  const auto t = __builtin_amdgcn_permlane32_swap(f2u(x), f2u(x), false, false);
  return fmaxf(u2f(t[0]), u2f(t[1]));
}
__device__ __forceinline__ float rows_sum(float x) { x = red16(x, x); return red32(x, x); }
#define MEM_FENCE() asm volatile("" ::: "memory")
#define SB() __builtin_amdgcn_sched_barrier(0)
#define WAITVM(n) do { asm volatile("s_waitcnt vmcnt(%0)" :: "i"(n) : "memory"); SB(); } while (0)
#define WAITVM2(x, y, n, m) do { asm volatile("s_cmp_gt_u32 %0, %1\n\ts_cbranch_scc1 1f\n\ts_waitcnt vmcnt(%2)\n1:\n\ts_waitcnt vmcnt(%3)" :: "s"(x), "s"(y), "i"(n), "i"(m) : "memory", "scc"); SB(); } while (0)
#define AI __attribute__((always_inline))
__device__ __forceinline__ bool all_lanes(bool c) { return __builtin_amdgcn_ballot_w64(c) == ~0ull; }
template <int... I, class F> __device__ __forceinline__ void static_for_impl(std::integer_sequence<int, I...>, F&& f) {
  (f(std::integral_constant<int, I>{}), ...);
}
template <int N, class F> __device__ __forceinline__ void static_for(F&& f) { static_for_impl(std::make_integer_sequence<int, N>{}, f); }

__device__ __forceinline__ void lds_post(LAS unsigned char* L, int slot, int w) {
  MEM_FENCE();
  *(volatile LAS u32*)(L + L_FLAG + slot * 16 + w * 4) = 1u;
  MEM_FENCE();
}
__device__ __forceinline__ void lds_wait(LAS unsigned char* L, int slot, bool sleep) {
  for (unsigned spins = 0; spins < LSPIN_CAP; ++spins) {
    MEM_FENCE();
    const v4u v = *(volatile LAS v4u*)(L + L_FLAG + slot * 16);
    if (__builtin_amdgcn_readfirstlane(v.x & v.y & v.z & v.w) != 0u) break;
    if (sleep) __builtin_amdgcn_s_sleep(1);
  }
  MEM_FENCE();
}

__global__ void __launch_bounds__(NTHR, 2) k_fused(Params p) {
  asm volatile("" :: "s"(p.x[0]), "s"(p.x[1]), "s"(p.k[0]), "s"(p.k[1]), "s"(p.v[0]), "s"(p.v[1]), "s"(p.wq[0]), "s"(p.wq[1]), "s"(p.wo[0]), "s"(p.wo[1]),
               "s"(p.out), "s"(p.ws));
  __shared__ __attribute__((aligned(16))) unsigned char smem[L_END];
  LAS unsigned char* L = (LAS unsigned char*)smem;
  const int lane = threadIdx.x & 63, w8 = __builtin_amdgcn_readfirstlane(threadIdx.x >> 6), w = w8 & 3;
  const int grp = w8 >> 2;
  const int wg = blockIdx.x, qd = wg & 3, ub = wg >> 2, b = ub >> 3, kvh = ub & 7;
  const int ks = (b & 1) * 4 + qd;
  const __amdgpu_buffer_rsrc_t rw = __builtin_amdgcn_make_buffer_rsrc((void*)p.ws, 0, WS_END, 0x00020000);
  u32 tagA, tagB;

  if (grp == 0) {
    const u32 t0 = (u32)__builtin_amdgcn_s_memrealtime();
    const int voffK = ((lane >> 2) & 3) * 256 + (lane >> 4) * 64 + (lane & 3) * 16;
    const int voffV = lane * 16;
    const int sbase = wg * 524288 + w * 4096;
    v4u ring[RING];
    {
      const __amdgpu_buffer_rsrc_t rk = __builtin_amdgcn_make_buffer_rsrc((void*)p.k[0], 0, KV_BYTES, 0x00020000);
      static_for<RING>([&](auto I) AI {
        constexpr int i = decltype(I)::value;
        if constexpr (i == NHEAD) __builtin_amdgcn_s_barrier();
        if constexpr (i >= NHEAD) WAITVM(NHEAD - 1);
        ring[i] = __builtin_amdgcn_raw_buffer_load_b128(rk, voffK + (i & 3) * 1024, sbase + (i >> 2) * 16384, AUX_NT);
        SB();
      });
    }
    const int jq = lane & 3;
    const u32 m0 = jq == 0 ? 0x0000ffffu : (jq == 1 ? 0xffff0000u : 0u);
    const u32 m1 = jq == 2 ? 0x0000ffffu : (jq == 3 ? 0xffff0000u : 0u);
    LAS unsigned char* scl = L + L_SC + w * 8192 + (lane >> 2) * 16;
    u32 due = TBASE - TAU;
    lds_wait(L, F_Q, false);
    tagA = __builtin_amdgcn_readfirstlane(*(LAS u32*)(L + L_FLAG + 128));
    tagB = __builtin_amdgcn_readfirstlane(*(LAS u32*)(L + L_FLAG + 132));
#pragma unroll 1
    for (int br = 0; br < 2; ++br) {
      const v4u qf = *(LAS v4u*)(L + L_Q + br * 1024 + voffK);
      const void* pk = p.k[br];
      const void* pv = p.v[br];
      float mx = -INFINITY;
#pragma unroll 1
      for (int it = 0; it < NIT; ++it) {
        const bool last = it == NIT - 1;
        const u32 now = (u32)__builtin_amdgcn_s_memrealtime() - t0;
        due += TAU;
        const __amdgpu_buffer_rsrc_t rn = __builtin_amdgcn_make_buffer_rsrc((void*)(last ? pv : pk), 0, KV_BYTES, 0x00020000);
        const int vn = last ? voffV : voffK;
        const int sn = sbase + (last ? 0 : (it + 1) * TPI * 16384);
        f32x4 st;
        static_for<RING>([&](auto I) AI {
          constexpr int i = decltype(I)::value;
          const f32x4 d = mfma32(ring[i], qf, f32x4{0.f, 0.f, 0.f, 0.f});
          float s = (dpp<0x00>(d[0]) + dpp<0x55>(d[1])) + (dpp<0xAA>(d[2]) + dpp<0xFF>(d[3]));
          mx = fmaxf(mx, s);
          asm volatile("" : "+v"(s), "+v"(mx));
          st[i & 3] = s;
          if constexpr ((i & 3) == 3) { if (jq == 0) *(LAS f32x4*)(scl + (it * TPI + (i >> 2)) * 256) = st; }
          SB();
          WAITVM2(now, due, NFLY - 1, NWIDE - 1);
          ring[i] = __builtin_amdgcn_raw_buffer_load_b128(rn, vn + (i & 3) * 1024, sn + (i >> 2) * 16384, AUX_NT);
          SB();
        });
      }
      const float mc = rows_max(mx) * CSC;
      float lsum = 0.f;
      f32x4 accLo = {0.f, 0.f, 0.f, 0.f}, accHi = {0.f, 0.f, 0.f, 0.f};
#pragma unroll 1
      for (int it = 0; it < NIT; ++it) {
        const bool last = it == NIT - 1;
        const u32 now = (u32)__builtin_amdgcn_s_memrealtime() - t0;
        due += TAU;
        const __amdgpu_buffer_rsrc_t rn =
            __builtin_amdgcn_make_buffer_rsrc((void*)(last ? p.k[1] : pv), 0, (last && br == 1) ? 0 : KV_BYTES, 0x00020000);
        const int vn = last ? voffK : voffV;
        const int sn = sbase + (last ? 0 : (it + 1) * TPI * 16384);
        if (last && br == 1) lds_post(L, F_LAST, w);
        f32x4 sv[TPI];
#pragma unroll
        for (int k = 0; k < TPI; ++k) sv[k] = *(LAS f32x4*)(scl + (it * TPI + k) * 256);
        static_for<RING>([&](auto I) AI {
          constexpr int i = decltype(I)::value;
          const float pe = __builtin_amdgcn_exp2f(fmaf(sv[i >> 2][i & 3], CSC, -mc));
          lsum += pe;
          asm volatile("" : "+v"(lsum));
          const u32 pkd = pack_bf16(pe, pe);
          const v2u a = {pkd & m0, pkd & m1};
          accLo = mfma16(a, v2u{ring[i].x, ring[i].y}, accLo);
          accHi = mfma16(a, v2u{ring[i].z, ring[i].w}, accHi);
          SB();
          WAITVM2(now, due, NFLY - 1, NWIDE - 1);
          ring[i] = __builtin_amdgcn_raw_buffer_load_b128(rn, vn + (i & 3) * 1024, sn + (i >> 2) * 16384, AUX_NT);
          SB();
        });
      }
      lsum = rows_sum(lsum);
      LAS float* mrg = (LAS float*)(L + L_MRG) + br * 16 * MRG_REC;
      LAS float* rec = mrg + (w * 4 + (lane >> 4)) * MRG_REC + 8 * (lane & 15);
      *(LAS f32x4*)rec = accLo;
      *(LAS f32x4*)(rec + 4) = accHi;
      if (lane < 16 && jq == 0) *(LAS f32x2*)(mrg + (w * 4 + (lane >> 2)) * MRG_REC + 128) = f32x2{mc, lsum};
      lds_post(L, F_REC + br, w);
    }
  } else {
    const int tid = w * 64 + lane;
    const v2u ep = __builtin_amdgcn_raw_buffer_load_b64(rw, 0, WS_EPOCH, AUX_SC1);
    v4u wr[2][4], xf[2];
#pragma unroll
    for (int br = 0; br < 2; ++br) {
      const __amdgpu_buffer_rsrc_t rq = __builtin_amdgcn_make_buffer_rsrc((void*)p.wq[br], 0, W_BYTES, 0x00020000);
      const __amdgpu_buffer_rsrc_t rx = __builtin_amdgcn_make_buffer_rsrc((void*)p.x[br], 0, NB * DM * 2, 0x00020000);
      const int so = (128 * qd + 32 * w) * (HQ * DH * 2) + (kvh * 512 + b * 64) * 2;
#pragma unroll
      for (int i = 0; i < 4; ++i)
        wr[br][i] = __builtin_amdgcn_raw_buffer_load_b128(rq, (lane >> 3) * (HQ * DH * 2) + (lane & 7) * 16, so + i * 8 * (HQ * DH * 2), AUX_NT);
      xf[br] = __builtin_amdgcn_raw_buffer_load_b128(rx, (lane & 7) * (DM * 2) + (lane >> 4) * 16, (128 * qd + 32 * w) * 2, 0);
      SB();
    }
    if (w == 0 && lane < 32) ((LAS u32*)(L + L_FLAG))[lane] = 0u;
    asm volatile("s_waitcnt lgkmcnt(0)" ::: "memory");
    __builtin_amdgcn_s_barrier();
    tagA = __builtin_amdgcn_readfirstlane(ep.x) + 1u;
    tagB = __builtin_amdgcn_readfirstlane(ep.y) + 0x9E3779B9u;
    if (lane == 0) *(LAS v2u*)(L + L_FLAG + 128) = v2u{tagA, tagB};
#pragma unroll
    for (int br = 0; br < 2; ++br) {
      LAS unsigned char* wl = L + L_W + br * 16384 + w * 4096;
#pragma unroll
      for (int i = 0; i < 4; ++i) *(LAS v4u*)(wl + i * 1024 + lane * 16) = wr[br][i];
      LAS unsigned char* rb = wl + (8 * (lane >> 4) + ((lane >> 2) & 3)) * 128 + (lane & 3) * 8;
      LAS f32x4* red = (LAS f32x4*)(L + L_RED + br * 8192);
#pragma unroll
      for (int ct = 0; ct < 4; ++ct) {
        const v2u lo = tr_read(rb + ct * 32), hi = tr_read(rb + ct * 32 + 512);
        const f32x4 d = mfma32(xf[br], v4u{lo.x, lo.y, hi.x, hi.y}, f32x4{0.f, 0.f, 0.f, 0.f});
        if (lane < 32) red[(w * 4 + ct) * 32 + lane] = d;
      }
    }
    lds_post(L, F_PROJ, w);
    lds_wait(L, F_PROJ, false);
    {
      const int pb = 4 * ((tid >> 3) & 1) + ((tid >> 4) & 3), pn = 16 * w + 2 * (tid & 7);
      const u32 tag8 = ((tid & 1) ? tagB : tagA) << 8;
#pragma unroll
      for (int br = 0; br < 2; ++br) {
        LAS float* rf = (LAS float*)(L + L_RED + br * 8192) + (w * 32 + 16 * ((tid >> 3) & 1) + 2 * (tid & 7)) * 4 + ((tid >> 4) & 3);
        float s0 = 0.f, s1 = 0.f;
#pragma unroll
        for (int ww = 0; ww < 4; ++ww) { s0 += rf[ww * 512]; s1 += rf[ww * 512 + 4]; }
        __builtin_amdgcn_raw_buffer_store_b64(packh(s0, s1, tag8), rw, WS_QG + (br * 8 + kvh) * 65536 + pb * 8192 + qd * 2048 + b * 256 + (pn >> 1) * 8, 0, AUX_SC1);
      }
    }
    SB();
    __builtin_amdgcn_s_sleep(Q_WAIT);
    SB();
    {
      const int qvo = WS_QG + (kvh * 8 + b) * 8192 + w * 512 + (lane >> 5) * 2048 + (lane & 31) * 16;
      const u32 ta = tagA << 8, tb = tagB << 8;
      v4u g[2][2];
      for (unsigned spins = 0; spins < SPIN_CAP; ++spins) {
        MEM_FENCE();
        u32 bad = 0u;
#pragma unroll
        for (int br = 0; br < 2; ++br)
#pragma unroll
          for (int i = 0; i < 2; ++i) g[br][i] = __builtin_amdgcn_raw_buffer_load_b128(rw, qvo, br * 8 * 65536 + i * 4096, AUX_SC1);
#pragma unroll
        for (int br = 0; br < 2; ++br)
#pragma unroll
          for (int i = 0; i < 2; ++i) bad |= (g[br][i].y ^ ta) | (g[br][i].w ^ tb);
        if (all_lanes((bad >> 8) == 0u)) break;
        __builtin_amdgcn_s_sleep(4);
      }
#pragma unroll
      for (int br = 0; br < 2; ++br) {
        const f32x4 s = unpack4(g[br][0]) + unpack4(g[br][1]);
        const float u0 = red32(s[0], s[2]), u1 = red32(s[1], s[3]);
        *(LAS u32*)(L + L_Q + br * 1024 + w * 256 + (lane & 31) * 8 + (lane >> 5) * 4) = pack_bf16(u0, u1);
      }
      lds_post(L, F_Q, w);
    }
  }

  const int br = 1 - grp;
  const u32 ta = tagA << 8, tb = tagB << 8;
  auto bad4 = [=](const v4u& g) AI -> u32 { return (g.y ^ tagA) | (g.w ^ tagB); };
  auto badp = [=](const v4u& g) AI -> u32 { return ((g.y ^ ta) | (g.w ^ tb)) >> 8; };
  int a_po = WS_PG + (br * NWG + wg) * PG_WG + w * PG_HEAD + lane * 8;
  int a_pm = lane == 0 ? WS_ML + ((br * 8 + kvh) * 4 + w) * 512 + (b * 4 + qd) * 16 : OOB;
  int a_ml = WS_ML + ((br * 8 + kvh) * 4 + (ks >> 1)) * 512 + (w * 8 + (lane >> 4)) * 16;
  int a_o = WS_PG + (br * NWG + (2 * w * 8 + kvh) * 4 + (lane >> 4)) * PG_WG + (ks >> 1) * PG_HEAD + (ks & 1) * 256 + (lane & 15) * 16;
  const bool odd = lane & 1;
  int a_og = lane < 32 ? WS_OG + br * NWG * 4096 + (((2 * (b >> 1) + (w >> 1)) * 8 + 4 * (lane >> 4) + (odd ? 2 : 0)) * 4 + 2 * (w & 1)) * 4096 +
                             (kvh * 8 + ks) * 64 + ((lane & 15) >> 1) * 8
                       : OOB;
  u32 t_po = (lane & 1) ? tb : ta, t_og = (lane & 2) ? tb : ta;
  asm volatile("" : "+v"(a_po), "+v"(a_pm), "+v"(a_ml), "+v"(a_o), "+v"(a_og), "+v"(t_po), "+v"(t_og));

  lds_wait(L, F_REC + br, grp != 0);
  {
    LAS float* mrg = (LAS float*)(L + L_MRG) + br * 16 * MRG_REC;
    f32x2 ml4[4], o4[4];
#pragma unroll
    for (int ww = 0; ww < 4; ++ww) {
      ml4[ww] = *(LAS f32x2*)(mrg + (ww * 4 + w) * MRG_REC + 128);
      o4[ww] = *(LAS f32x2*)(mrg + (ww * 4 + w) * MRG_REC + 2 * lane);
    }
    float M = fmaxf(fmaxf(ml4[0].x, ml4[1].x), fmaxf(ml4[2].x, ml4[3].x));
    float Ls = 0.f, o0 = 0.f, o1 = 0.f;
#pragma unroll
    for (int ww = 0; ww < 4; ++ww) {
      const float f = __builtin_amdgcn_exp2f(ml4[ww].x - M);
      Ls = fmaf(f, ml4[ww].y, Ls);
      o0 = fmaf(f, o4[ww].x, o0);
      o1 = fmaf(f, o4[ww].y, o1);
    }
    v2u ph = packh(o0, o1, t_po);
    v4u wo4[2][4];
    if (grp == 1) {
      asm volatile("" : "+v"(ph), "+v"(M), "+v"(Ls));
      lds_wait(L, F_LAST, true);
#pragma unroll
      for (int bq = 0; bq < 2; ++bq) {
        const __amdgpu_buffer_rsrc_t ro = __builtin_amdgcn_make_buffer_rsrc((void*)p.wo[bq], 0, W_BYTES, 0x00020000);
        const int so = (kvh * 512 + 64 * ks + 16 * w) * (DM * 2) + (b >> 1) * 256;
#pragma unroll
        for (int i = 0; i < 4; ++i)
          wo4[bq][i] = __builtin_amdgcn_raw_buffer_load_b128(ro, (lane >> 4) * (DM * 2) + (lane & 15) * 16, so + i * 4 * (DM * 2), AUX_NT);
      }
      SB();
      lds_wait(L, F_REC + 1, HSLEEP);
    }
    __builtin_amdgcn_raw_buffer_store_b64(ph, rw, a_po, 0, AUX_SC1);
    __builtin_amdgcn_raw_buffer_store_b128(v4u{f2u(M), tagA, f2u(Ls), tagB}, rw, a_pm, 0, AUX_SC1);
    if (grp == 1) {
#pragma unroll
      for (int bq = 0; bq < 2; ++bq)
#pragma unroll
        for (int i = 0; i < 4; ++i) *(LAS v4u*)(L + L_W + (2 + bq) * 16384 + w * 4096 + i * 1024 + lane * 16) = wo4[bq][i];
      lds_post(L, F_WO, w);
    }
    lds_wait(L, F_WO, false);
  }
  v4u bf[2][2];
  {
    LAS unsigned char* rb = L + L_W + (2 + br) * 16384 + (8 * (lane >> 4) + ((lane >> 2) & 3)) * 256 + w * 64 + (lane & 3) * 8;
#pragma unroll
    for (int s = 0; s < 2; ++s)
#pragma unroll
      for (int c = 0; c < 2; ++c) {
        const v2u lo = tr_read(rb + s * 8192 + c * 32), hi = tr_read(rb + s * 8192 + c * 32 + 1024);
        bf[s][c] = v4u{lo.x, lo.y, hi.x, hi.y};
      }
  }
  v4u ml[2], gr[2];
  {
#pragma unroll
    for (int j = 0; j < 2; ++j) { ml[j] = v4u{0u, ~tagA, 0u, 0u}; gr[j] = v4u{0u, ~ta, 0u, 0u}; }
    bool few = false;
    if (H1_WAIT) __builtin_amdgcn_s_sleep(H1_WAIT);
    for (unsigned spins = 0; spins < SPIN_CAP; ++spins) {
      MEM_FENCE();
      v4u tm[2], t[2];
      bool wm[2], want[2];
#pragma unroll
      for (int j = 0; j < 2; ++j) {
        wm[j] = bad4(ml[j]) != 0u;
        want[j] = badp(gr[j]) != 0u && (few || !wm[j]);
        tm[j] = __builtin_amdgcn_raw_buffer_load_b128(rw, wm[j] ? a_ml + j * 64 : OOB, 0, AUX_SC1);
        t[j] = __builtin_amdgcn_raw_buffer_load_b128(rw, want[j] ? a_o + j * 32 * PG_WG : OOB, 0, AUX_SC1);
      }
      SB();
      u32 bad = 0u;
      int miss = 0;
#pragma unroll
      for (int j = 0; j < 2; ++j) {
        ml[j] = wm[j] ? tm[j] : ml[j];
        gr[j] = want[j] ? t[j] : gr[j];
        const u32 bm = bad4(ml[j]);
        miss += __builtin_popcountll(__builtin_amdgcn_ballot_w64(bm != 0u));
        bad |= bm | badp(gr[j]);
      }
      if (all_lanes(bad == 0u)) break;
      few = miss <= 16 * FEW;
      if (!few) __builtin_amdgcn_s_sleep(POLL_SLEEP);
    }
  }
  {
    LAS unsigned char* ol = L + L_O + br * 1024;
    const int r = lane >> 4;
#pragma unroll
    for (int j = 0; j < 2; ++j) {
      const float m = u2f(ml[j].x);
      const float e = __builtin_amdgcn_exp2f(m - rows_max(m));
      const float fw = e * __builtin_amdgcn_rcpf(rows_sum(e * u2f(ml[j].z)));
      const f32x4 a = unpack4(gr[j]) * fw;
      const float o = red16(red32(a[0], a[1]), red32(a[2], a[3]));
      *(LAS unsigned short*)(ol + (2 * w + j) * 128 + ((lane & 15) * 4 + ((r & 1) * 2 + (r >> 1))) * 2) = (unsigned short)(pack_bf16(o, o) & 0xffffu);
    }
    lds_post(L, F_O + br, w);
    lds_wait(L, F_O + br, false);
    const v4u af0 = *(LAS v4u*)(ol + (lane & 7) * 128 + (lane >> 4) * 16), af1 = *(LAS v4u*)(ol + (lane & 7) * 128 + 64 + (lane >> 4) * 16);
#pragma unroll
    for (int c = 0; c < 2; ++c) {
      f32x4 d = mfma32(af0, bf[0][c], f32x4{0.f, 0.f, 0.f, 0.f});
      d = mfma32(af1, bf[1][c], d);
      const float e0 = dpp<0xB1>(d[0]), e1 = dpp<0xB1>(d[1]), e2 = dpp<0xB1>(d[2]), e3 = dpp<0xB1>(d[3]);
      __builtin_amdgcn_raw_buffer_store_b64(packh(odd ? e2 : d[0], odd ? d[2] : e0, t_og), rw, a_og + c * 4096, 0, AUX_SC1);
      __builtin_amdgcn_raw_buffer_store_b64(packh(odd ? e3 : d[1], odd ? d[3] : e1, t_og), rw, a_og + c * 4096 + 4 * 4096, 0, AUX_SC1);
    }
  }
  if (w == 0) {
    v4u a[4];
#pragma unroll
    for (int i = 0; i < 4; ++i) a[i] = v4u{0u, ~ta, 0u, 0u};
    const int vo = WS_OG + (br * NWG + wg) * 4096 + lane * 16;
    __builtin_amdgcn_s_sleep(H2_WAIT);
    for (unsigned spins = 0; spins < SPIN_CAP; ++spins) {
      MEM_FENCE();
      v4u t[4];
#pragma unroll
      for (int i = 0; i < 4; ++i) t[i] = __builtin_amdgcn_raw_buffer_load_b128(rw, badp(a[i]) != 0u ? vo + i * 1024 : OOB, 0, AUX_SC1);
      SB();
      u32 bad = 0u;
#pragma unroll
      for (int i = 0; i < 4; ++i) { a[i] = badp(a[i]) != 0u ? t[i] : a[i]; bad |= badp(a[i]); }
      if (all_lanes(bad == 0u)) break;
      __builtin_amdgcn_s_sleep(H2_SLEEP);
    }
    f32x4 s = (unpack4(a[0]) + unpack4(a[1])) + (unpack4(a[2]) + unpack4(a[3]));
#pragma unroll
    for (int j = 0; j < 4; ++j) { s[j] += dpp<0x124>(s[j]); s[j] += dpp<0x128>(s[j]); }
    const float o = red16(red32(s[0], s[1]), red32(s[2], s[3]));
    const int r = lane >> 4;
    const __amdgpu_buffer_rsrc_t rout = __builtin_amdgcn_make_buffer_rsrc(p.out, 0, NB * 2 * DM * 2, 0x00020000);
    __builtin_amdgcn_raw_buffer_store_b16((unsigned short)(pack_bf16(o, o) & 0xffffu), rout,
                                          (lane & 15) < 4 ? (kvh * 1024 + br * 512 + b * 64 + qd * 16 + (lane & 3) * 4 + (r & 1) * 2 + (r >> 1)) * 2 : OOB, 0, AUX_SC1);
    if (wg == 0 && grp == 1 && lane == 0) __builtin_amdgcn_raw_buffer_store_b64(v2u{tagA, tagB}, rw, 0, WS_EPOCH, AUX_SC1);
  }
}

extern "C" void kernel_launch(void* const* d_in, const int* in_sizes, int n_in, void* d_out, int out_size, void* d_ws, size_t ws_size,
                              hipStream_t stream) {
  Params p;
  p.x[0] = d_in[0]; p.k[0] = d_in[1]; p.v[0] = d_in[2]; p.wq[0] = d_in[3]; p.wo[0] = d_in[4];
  p.x[1] = d_in[5]; p.k[1] = d_in[6]; p.v[1] = d_in[7]; p.wq[1] = d_in[8]; p.wo[1] = d_in[9];
  p.out = d_out;
  p.ws = (unsigned char*)d_ws;
  k_fused<<<NWG, NTHR, 0, stream>>>(p);
}
```

```cpp
#include <hip/hip_runtime.h>
#include <hip/hip_bf16.h>
#include <stdint.h>
#include <utility>

typedef unsigned u32;
typedef u32 v4u __attribute__((ext_vector_type(4)));
typedef u32 v2u __attribute__((ext_vector_type(2)));
typedef float f32x4 __attribute__((ext_vector_type(4)));
typedef float f32x2 __attribute__((ext_vector_type(2)));
typedef __bf16 bf16x8 __attribute__((ext_vector_type(8)));
typedef __bf16 bf16x2 __attribute__((ext_vector_type(2)));
typedef short v4s __attribute__((ext_vector_type(4)));
#define LAS __attribute__((address_space(3)))

#define NFLY 5
#define NWIDE 6
#define NHEAD 3
#define TAU 217
#define TBASE 510
#define Q_WAIT 32
#define FEW 4
#define POLL_SLEEP 4
#define CODE_WARM 94
#define HSLEEP true
#define H1_WAIT 6
#define H2_WAIT 16
#define H2_SLEEP 2

constexpr int NB = 8, DM = 512, HQ = 32, HKV = 8, SK = 8192, DH = 128;
constexpr int NWG = 256, NTHR = 512;
constexpr int RING = 16, TPI = RING / 4, NIT = 32 / TPI;
constexpr int KV_BYTES = NB * HKV * SK * DH * 2;
constexpr int W_BYTES = DM * HQ * DH * 2;
constexpr float CSC = 0.08838834764831845f * 1.4426950408889634f;
constexpr int AUX_NT = 2, AUX_SC1 = 16;
constexpr unsigned SPIN_CAP = 1u << 17, LSPIN_CAP = 1u << 22;
constexpr int OOB = 0x40000000;

constexpr int WS_EPOCH = 0;
constexpr int WS_QG = 4096;
constexpr int PG_HEAD = 512, PG_WG = 4 * PG_HEAD;
constexpr int WS_PG = WS_QG + 2 * 8 * 8 * 8192;
constexpr int WS_ML = WS_PG + 2 * NWG * PG_WG;
constexpr int WS_OG = WS_ML + 2 * 8 * 4 * 512;
constexpr int WS_END = WS_OG + 2 * NWG * 4096;

constexpr int MRG_REC = 132;
constexpr int L_W = 0;
constexpr int L_RED = 65536;
constexpr int L_MRG = L_RED + 16384;
constexpr int L_Q = L_MRG + 2 * 16 * MRG_REC * 4;
constexpr int L_SC = L_Q + 2048;
constexpr int L_O = L_SC + 32768;
constexpr int L_FLAG = L_O + 2048;
constexpr int L_END = L_FLAG + 128 + 16;
enum { F_Q = 0, F_LAST = 1, F_REC = 2  , F_PROJ = 4, F_WO = 5, F_O = 6   };

struct Params {
  const void* x[2];
  const void* k[2];
  const void* v[2];
  const void* wq[2];
  const void* wo[2];
  void* out;
  unsigned char* ws;
};

__device__ __forceinline__ f32x4 mfma32(v4u a, v4u b, f32x4 c) {
  return __builtin_amdgcn_mfma_f32_16x16x32_bf16(__builtin_bit_cast(bf16x8, a), __builtin_bit_cast(bf16x8, b), c, 0, 0, 0);
}
__device__ __forceinline__ f32x4 mfma16(v2u a, v2u b, f32x4 c) {
  return __builtin_amdgcn_mfma_f32_16x16x16bf16_1k(__builtin_bit_cast(v4s, a), __builtin_bit_cast(v4s, b), c, 0, 0, 0);
}
__device__ __forceinline__ u32 pack_bf16(float lo, float hi) {
  f32x2 f = {lo, hi};
  return __builtin_bit_cast(u32, __builtin_convertvector(f, bf16x2));
}
__device__ __forceinline__ v2u tr_read(LAS unsigned char* p) {
  return __builtin_bit_cast(v2u, __builtin_amdgcn_ds_read_tr16_b64_v4i16((LAS v4s*)p));
}
__device__ __forceinline__ u32 f2u(float f) { return __builtin_bit_cast(u32, f); }
__device__ __forceinline__ float u2f(u32 u) { return __builtin_bit_cast(float, u); }
template <int CTRL> __device__ __forceinline__ float dpp(float x) {
  return u2f((u32)__builtin_amdgcn_mov_dpp((int)f2u(x), CTRL, 0xf, 0xf, true));
}
__device__ __forceinline__ v2u packh(float v0, float v1, u32 tag8) {
  const u32 a = f2u(v0) + 0x800u, c = f2u(v1) + 0x800u;
  return v2u{(a >> 12) | ((c << 8) & 0xFFF00000u), (c >> 24) | tag8};
}
__device__ __forceinline__ f32x4 unpack4(v4u g) {
  return f32x4{u2f(g.x << 12), u2f(((g.x >> 20) << 12) | (g.y << 24)), u2f(g.z << 12), u2f(((g.z >> 20) << 12) | (g.w << 24))};
}
__device__ __forceinline__ float red32(float a, float b) {
  const auto s = __builtin_amdgcn_permlane32_swap(f2u(a), f2u(b), false, false);
  return u2f(s[0]) + u2f(s[1]);
}
__device__ __forceinline__ float red16(float a, float b) {
  const auto s = __builtin_amdgcn_permlane16_swap(f2u(a), f2u(b), false, false);
  return u2f(s[0]) + u2f(s[1]);
}
__device__ __forceinline__ float rows_max(float x) {
  const auto s = __builtin_amdgcn_permlane16_swap(f2u(x), f2u(x), false, false);
  x = fmaxf(u2f(s[0]), u2f(s[1]));
  const auto t = __builtin_amdgcn_permlane32_swap(f2u(x), f2u(x), false, false);
  return fmaxf(u2f(t[0]), u2f(t[1]));
}
__device__ __forceinline__ float rows_sum(float x) { x = red16(x, x); return red32(x, x); }
#define MEM_FENCE() asm volatile("" ::: "memory")
#define SB() __builtin_amdgcn_sched_barrier(0)
#define WAITVM(n) do { asm volatile("s_waitcnt vmcnt(%0)" :: "i"(n) : "memory"); SB(); } while (0)
#define WAITVM2(x, y, n, m) do { asm volatile("s_cmp_gt_u32 %0, %1\n\ts_cbranch_scc1 1f\n\ts_waitcnt vmcnt(%2)\n1:\n\ts_waitcnt vmcnt(%3)" :: "s"(x), "s"(y), "i"(n), "i"(m) : "memory", "scc"); SB(); } while (0)
#define AI __attribute__((always_inline))
__device__ __forceinline__ bool all_lanes(bool c) { return __builtin_amdgcn_ballot_w64(c) == ~0ull; }
template <int... I, class F> __device__ __forceinline__ void static_for_impl(std::integer_sequence<int, I...>, F&& f) {
  (f(std::integral_constant<int, I>{}), ...);
}
template <int N, class F> __device__ __forceinline__ void static_for(F&& f) { static_for_impl(std::make_integer_sequence<int, N>{}, f); }

__device__ __forceinline__ void lds_post(LAS unsigned char* L, int slot, int w) {
  MEM_FENCE();
  *(volatile LAS u32*)(L + L_FLAG + slot * 16 + w * 4) = 1u;
  MEM_FENCE();
}
__device__ __forceinline__ void lds_wait(LAS unsigned char* L, int slot, bool sleep) {
  for (unsigned spins = 0; spins < LSPIN_CAP; ++spins) {
    MEM_FENCE();
    const v4u v = *(volatile LAS v4u*)(L + L_FLAG + slot * 16);
    if (__builtin_amdgcn_readfirstlane(v.x & v.y & v.z & v.w) != 0u) break;
    if (sleep) __builtin_amdgcn_s_sleep(1);
  }
  MEM_FENCE();
}

__global__ void __launch_bounds__(NTHR, 2) k_fused(Params p) {
  asm volatile("" :: "s"(p.x[0]), "s"(p.x[1]), "s"(p.k[0]), "s"(p.k[1]), "s"(p.v[0]), "s"(p.v[1]), "s"(p.wq[0]), "s"(p.wq[1]), "s"(p.wo[0]), "s"(p.wo[1]),
               "s"(p.out), "s"(p.ws));
  unsigned long long pc;
  asm volatile("s_getpc_b64 %0" : "=s"(pc));
  __shared__ __attribute__((aligned(16))) unsigned char smem[L_END];
  LAS unsigned char* L = (LAS unsigned char*)smem;
  const int lane = threadIdx.x & 63, w8 = __builtin_amdgcn_readfirstlane(threadIdx.x >> 6), w = w8 & 3;
  const int grp = w8 >> 2;
  const int wg = blockIdx.x, qd = wg & 3, ub = wg >> 2, b = ub >> 3, kvh = ub & 7;
  const int ks = (b & 1) * 4 + qd;
  const __amdgpu_buffer_rsrc_t rw = __builtin_amdgcn_make_buffer_rsrc((void*)p.ws, 0, WS_END, 0x00020000);
  u32 tagA, tagB;

  if (grp == 0) {
    const u32 t0 = (u32)__builtin_amdgcn_s_memrealtime();
    const int voffK = ((lane >> 2) & 3) * 256 + (lane >> 4) * 64 + (lane & 3) * 16;
    const int voffV = lane * 16;
    const int sbase = wg * 524288 + w * 4096;
    v4u ring[RING];
    {
      const __amdgpu_buffer_rsrc_t rk = __builtin_amdgcn_make_buffer_rsrc((void*)p.k[0], 0, KV_BYTES, 0x00020000);
      static_for<RING>([&](auto I) AI {
        constexpr int i = decltype(I)::value;
        if constexpr (i == NHEAD) __builtin_amdgcn_s_barrier();
        if constexpr (i >= NHEAD) WAITVM(NHEAD - 1);
        ring[i] = __builtin_amdgcn_raw_buffer_load_b128(rk, voffK + (i & 3) * 1024, sbase + (i >> 2) * 16384, AUX_NT);
        SB();
      });
    }
    const int jq = lane & 3;
    const u32 m0 = jq == 0 ? 0x0000ffffu : (jq == 1 ? 0xffff0000u : 0u);
    const u32 m1 = jq == 2 ? 0x0000ffffu : (jq == 3 ? 0xffff0000u : 0u);
    LAS unsigned char* scl = L + L_SC + w * 8192 + (lane >> 2) * 16;
    u32 due = TBASE - TAU;
    lds_wait(L, F_Q, false);
    tagA = __builtin_amdgcn_readfirstlane(*(LAS u32*)(L + L_FLAG + 128));
    tagB = __builtin_amdgcn_readfirstlane(*(LAS u32*)(L + L_FLAG + 132));
#pragma unroll 1
    for (int br = 0; br < 2; ++br) {
      const v4u qf = *(LAS v4u*)(L + L_Q + br * 1024 + voffK);
      const void* pk = p.k[br];
      const void* pv = p.v[br];
      float mx = -INFINITY;
#pragma unroll 1
      for (int it = 0; it < NIT; ++it) {
        const bool last = it == NIT - 1;
        const u32 now = (u32)__builtin_amdgcn_s_memrealtime() - t0;
        due += TAU;
        const __amdgpu_buffer_rsrc_t rn = __builtin_amdgcn_make_buffer_rsrc((void*)(last ? pv : pk), 0, KV_BYTES, 0x00020000);
        const int vn = last ? voffV : voffK;
        const int sn = sbase + (last ? 0 : (it + 1) * TPI * 16384);
        f32x4 st;
        static_for<RING>([&](auto I) AI {
          constexpr int i = decltype(I)::value;
          const f32x4 d = mfma32(ring[i], qf, f32x4{0.f, 0.f, 0.f, 0.f});
          float s = (dpp<0x00>(d[0]) + dpp<0x55>(d[1])) + (dpp<0xAA>(d[2]) + dpp<0xFF>(d[3]));
          mx = fmaxf(mx, s);
          asm volatile("" : "+v"(s), "+v"(mx));
          st[i & 3] = s;
          if constexpr ((i & 3) == 3) { if (jq == 0) *(LAS f32x4*)(scl + (it * TPI + (i >> 2)) * 256) = st; }
          SB();
          WAITVM2(now, due, NFLY - 1, NWIDE - 1);
          ring[i] = __builtin_amdgcn_raw_buffer_load_b128(rn, vn + (i & 3) * 1024, sn + (i >> 2) * 16384, AUX_NT);
          SB();
        });
      }
      const float mc = rows_max(mx) * CSC;
      float lsum = 0.f;
      f32x4 accLo = {0.f, 0.f, 0.f, 0.f}, accHi = {0.f, 0.f, 0.f, 0.f};
#pragma unroll 1
      for (int it = 0; it < NIT; ++it) {
        const bool last = it == NIT - 1;
        const u32 now = (u32)__builtin_amdgcn_s_memrealtime() - t0;
        due += TAU;
        const __amdgpu_buffer_rsrc_t rn =
            __builtin_amdgcn_make_buffer_rsrc((void*)(last ? p.k[1] : pv), 0, (last && br == 1) ? 0 : KV_BYTES, 0x00020000);
        const int vn = last ? voffK : voffV;
        const int sn = sbase + (last ? 0 : (it + 1) * TPI * 16384);
        if (last && br == 1) lds_post(L, F_LAST, w);
        f32x4 sv[TPI];
#pragma unroll
        for (int k = 0; k < TPI; ++k) sv[k] = *(LAS f32x4*)(scl + (it * TPI + k) * 256);
        static_for<RING>([&](auto I) AI {
          constexpr int i = decltype(I)::value;
          const float pe = __builtin_amdgcn_exp2f(fmaf(sv[i >> 2][i & 3], CSC, -mc));
          lsum += pe;
          asm volatile("" : "+v"(lsum));
          const u32 pkd = pack_bf16(pe, pe);
          const v2u a = {pkd & m0, pkd & m1};
          accLo = mfma16(a, v2u{ring[i].x, ring[i].y}, accLo);
          accHi = mfma16(a, v2u{ring[i].z, ring[i].w}, accHi);
          SB();
          WAITVM2(now, due, NFLY - 1, NWIDE - 1);
          ring[i] = __builtin_amdgcn_raw_buffer_load_b128(rn, vn + (i & 3) * 1024, sn + (i >> 2) * 16384, AUX_NT);
          SB();
        });
      }
      lsum = rows_sum(lsum);
      LAS float* mrg = (LAS float*)(L + L_MRG) + br * 16 * MRG_REC;
      LAS float* rec = mrg + (w * 4 + (lane >> 4)) * MRG_REC + 8 * (lane & 15);
      *(LAS f32x4*)rec = accLo;
      *(LAS f32x4*)(rec + 4) = accHi;
      if (lane < 16 && jq == 0) *(LAS f32x2*)(mrg + (w * 4 + (lane >> 2)) * MRG_REC + 128) = f32x2{mc, lsum};
      lds_post(L, F_REC + br, w);
    }
  } else {
    const int tid = w * 64 + lane;
    const v2u ep = __builtin_amdgcn_raw_buffer_load_b64(rw, 0, WS_EPOCH, AUX_SC1);
    v4u wr[2][4], xf[2];
#pragma unroll
    for (int br = 0; br < 2; ++br) {
      const __amdgpu_buffer_rsrc_t rq = __builtin_amdgcn_make_buffer_rsrc((void*)p.wq[br], 0, W_BYTES, 0x00020000);
      const __amdgpu_buffer_rsrc_t rx = __builtin_amdgcn_make_buffer_rsrc((void*)p.x[br], 0, NB * DM * 2, 0x00020000);
      const int so = (128 * qd + 32 * w) * (HQ * DH * 2) + (kvh * 512 + b * 64) * 2;
#pragma unroll
      for (int i = 0; i < 4; ++i)
        wr[br][i] = __builtin_amdgcn_raw_buffer_load_b128(rq, (lane >> 3) * (HQ * DH * 2) + (lane & 7) * 16, so + i * 8 * (HQ * DH * 2), AUX_NT);
      xf[br] = __builtin_amdgcn_raw_buffer_load_b128(rx, (lane & 7) * (DM * 2) + (lane >> 4) * 16, (128 * qd + 32 * w) * 2, 0);
      SB();
    }
    u32 cw0 = 0u, cw1 = 0u;
    if (CODE_WARM) {
      const __amdgpu_buffer_rsrc_t rc = __builtin_amdgcn_make_buffer_rsrc((void*)(pc & ~255ull), 0, w == 3 ? CODE_WARM * 128 : 0, 0x00020000);
      cw0 = __builtin_amdgcn_raw_buffer_load_b32(rc, lane * 128, 0, 0);
      cw1 = __builtin_amdgcn_raw_buffer_load_b32(rc, lane * 128, 8192, 0);
      SB();
    }
    if (w == 0 && lane < 32) ((LAS u32*)(L + L_FLAG))[lane] = 0u;
    asm volatile("s_waitcnt lgkmcnt(0)" ::: "memory");
    __builtin_amdgcn_s_barrier();
    tagA = __builtin_amdgcn_readfirstlane(ep.x) + 1u;
    tagB = __builtin_amdgcn_readfirstlane(ep.y) + 0x9E3779B9u;
    if (lane == 0) *(LAS v2u*)(L + L_FLAG + 128) = v2u{tagA, tagB};
#pragma unroll
    for (int br = 0; br < 2; ++br) {
      LAS unsigned char* wl = L + L_W + br * 16384 + w * 4096;
#pragma unroll
      for (int i = 0; i < 4; ++i) *(LAS v4u*)(wl + i * 1024 + lane * 16) = wr[br][i];
      LAS unsigned char* rb = wl + (8 * (lane >> 4) + ((lane >> 2) & 3)) * 128 + (lane & 3) * 8;
      LAS f32x4* red = (LAS f32x4*)(L + L_RED + br * 8192);
#pragma unroll
      for (int ct = 0; ct < 4; ++ct) {
        const v2u lo = tr_read(rb + ct * 32), hi = tr_read(rb + ct * 32 + 512);
        const f32x4 d = mfma32(xf[br], v4u{lo.x, lo.y, hi.x, hi.y}, f32x4{0.f, 0.f, 0.f, 0.f});
        if (lane < 32) red[(w * 4 + ct) * 32 + lane] = d;
      }
    }
    lds_post(L, F_PROJ, w);
    lds_wait(L, F_PROJ, false);
    {
      const int pb = 4 * ((tid >> 3) & 1) + ((tid >> 4) & 3), pn = 16 * w + 2 * (tid & 7);
      const u32 tag8 = ((tid & 1) ? tagB : tagA) << 8;
#pragma unroll
      for (int br = 0; br < 2; ++br) {
        LAS float* rf = (LAS float*)(L + L_RED + br * 8192) + (w * 32 + 16 * ((tid >> 3) & 1) + 2 * (tid & 7)) * 4 + ((tid >> 4) & 3);
        float s0 = 0.f, s1 = 0.f;
#pragma unroll
        for (int ww = 0; ww < 4; ++ww) { s0 += rf[ww * 512]; s1 += rf[ww * 512 + 4]; }
        __builtin_amdgcn_raw_buffer_store_b64(packh(s0, s1, tag8), rw, WS_QG + (br * 8 + kvh) * 65536 + pb * 8192 + qd * 2048 + b * 256 + (pn >> 1) * 8, 0, AUX_SC1);
      }
    }
    SB();
    __builtin_amdgcn_s_sleep(Q_WAIT);
    SB();
    {
      const int qvo = WS_QG + (kvh * 8 + b) * 8192 + w * 512 + (lane >> 5) * 2048 + (lane & 31) * 16;
      const u32 ta = tagA << 8, tb = tagB << 8;
      v4u g[2][2];
      for (unsigned spins = 0; spins < SPIN_CAP; ++spins) {
        MEM_FENCE();
        u32 bad = 0u;
#pragma unroll
        for (int br = 0; br < 2; ++br)
#pragma unroll
          for (int i = 0; i < 2; ++i) g[br][i] = __builtin_amdgcn_raw_buffer_load_b128(rw, qvo, br * 8 * 65536 + i * 4096, AUX_SC1);
#pragma unroll
        for (int br = 0; br < 2; ++br)
#pragma unroll
          for (int i = 0; i < 2; ++i) bad |= (g[br][i].y ^ ta) | (g[br][i].w ^ tb);
        if (all_lanes((bad >> 8) == 0u)) break;
        __builtin_amdgcn_s_sleep(4);
      }
#pragma unroll
      for (int br = 0; br < 2; ++br) {
        const f32x4 s = unpack4(g[br][0]) + unpack4(g[br][1]);
        const float u0 = red32(s[0], s[2]), u1 = red32(s[1], s[3]);
        *(LAS u32*)(L + L_Q + br * 1024 + w * 256 + (lane & 31) * 8 + (lane >> 5) * 4) = pack_bf16(u0, u1);
      }
      lds_post(L, F_Q, w);
      asm volatile("" :: "v"(cw0), "v"(cw1));
    }
  }

  const int br = 1 - grp;
  const u32 ta = tagA << 8, tb = tagB << 8;
  auto bad4 = [=](const v4u& g) AI -> u32 { return (g.y ^ tagA) | (g.w ^ tagB); };
  auto badp = [=](const v4u& g) AI -> u32 { return ((g.y ^ ta) | (g.w ^ tb)) >> 8; };
  int a_po = WS_PG + (br * NWG + wg) * PG_WG + w * PG_HEAD + lane * 8;
  int a_pm = lane == 0 ? WS_ML + ((br * 8 + kvh) * 4 + w) * 512 + (b * 4 + qd) * 16 : OOB;
  int a_ml = WS_ML + ((br * 8 + kvh) * 4 + (ks >> 1)) * 512 + (w * 8 + (lane >> 4)) * 16;
  int a_o = WS_PG + (br * NWG + (2 * w * 8 + kvh) * 4 + (lane >> 4)) * PG_WG + (ks >> 1) * PG_HEAD + (ks & 1) * 256 + (lane & 15) * 16;
  const bool odd = lane & 1;
  int a_og = lane < 32 ? WS_OG + br * NWG * 4096 + (((2 * (b >> 1) + (w >> 1)) * 8 + 4 * (lane >> 4) + (odd ? 2 : 0)) * 4 + 2 * (w & 1)) * 4096 +
                             (kvh * 8 + ks) * 64 + ((lane & 15) >> 1) * 8
                       : OOB;
  u32 t_po = (lane & 1) ? tb : ta, t_og = (lane & 2) ? tb : ta;
  asm volatile("" : "+v"(a_po), "+v"(a_pm), "+v"(a_ml), "+v"(a_o), "+v"(a_og), "+v"(t_po), "+v"(t_og));

  lds_wait(L, F_REC + br, grp != 0);
  {
    LAS float* mrg = (LAS float*)(L + L_MRG) + br * 16 * MRG_REC;
    f32x2 ml4[4], o4[4];
#pragma unroll
    for (int ww = 0; ww < 4; ++ww) {
      ml4[ww] = *(LAS f32x2*)(mrg + (ww * 4 + w) * MRG_REC + 128);
      o4[ww] = *(LAS f32x2*)(mrg + (ww * 4 + w) * MRG_REC + 2 * lane);
    }
    float M = fmaxf(fmaxf(ml4[0].x, ml4[1].x), fmaxf(ml4[2].x, ml4[3].x));
    float Ls = 0.f, o0 = 0.f, o1 = 0.f;
#pragma unroll
    for (int ww = 0; ww < 4; ++ww) {
      const float f = __builtin_amdgcn_exp2f(ml4[ww].x - M);
      Ls = fmaf(f, ml4[ww].y, Ls);
      o0 = fmaf(f, o4[ww].x, o0);
      o1 = fmaf(f, o4[ww].y, o1);
    }
    v2u ph = packh(o0, o1, t_po);
    v4u wo4[2][4];
    if (grp == 1) {
      asm volatile("" : "+v"(ph), "+v"(M), "+v"(Ls));
      lds_wait(L, F_LAST, true);
#pragma unroll
      for (int bq = 0; bq < 2; ++bq) {
        const __amdgpu_buffer_rsrc_t ro = __builtin_amdgcn_make_buffer_rsrc((void*)p.wo[bq], 0, W_BYTES, 0x00020000);
        const int so = (kvh * 512 + 64 * ks + 16 * w) * (DM * 2) + (b >> 1) * 256;
#pragma unroll
        for (int i = 0; i < 4; ++i)
          wo4[bq][i] = __builtin_amdgcn_raw_buffer_load_b128(ro, (lane >> 4) * (DM * 2) + (lane & 15) * 16, so + i * 4 * (DM * 2), AUX_NT);
      }
      SB();
      lds_wait(L, F_REC + 1, HSLEEP);
    }
    __builtin_amdgcn_raw_buffer_store_b64(ph, rw, a_po, 0, AUX_SC1);
    __builtin_amdgcn_raw_buffer_store_b128(v4u{f2u(M), tagA, f2u(Ls), tagB}, rw, a_pm, 0, AUX_SC1);
    if (grp == 1) {
#pragma unroll
      for (int bq = 0; bq < 2; ++bq)
#pragma unroll
        for (int i = 0; i < 4; ++i) *(LAS v4u*)(L + L_W + (2 + bq) * 16384 + w * 4096 + i * 1024 + lane * 16) = wo4[bq][i];
      lds_post(L, F_WO, w);
    }
    lds_wait(L, F_WO, false);
  }
  v4u bf[2][2];
  {
    LAS unsigned char* rb = L + L_W + (2 + br) * 16384 + (8 * (lane >> 4) + ((lane >> 2) & 3)) * 256 + w * 64 + (lane & 3) * 8;
#pragma unroll
    for (int s = 0; s < 2; ++s)
#pragma unroll
      for (int c = 0; c < 2; ++c) {
        const v2u lo = tr_read(rb + s * 8192 + c * 32), hi = tr_read(rb + s * 8192 + c * 32 + 1024);
        bf[s][c] = v4u{lo.x, lo.y, hi.x, hi.y};
      }
  }
  v4u ml[2], gr[2];
  {
#pragma unroll
    for (int j = 0; j < 2; ++j) { ml[j] = v4u{0u, ~tagA, 0u, 0u}; gr[j] = v4u{0u, ~ta, 0u, 0u}; }
    bool few = false;
    if (H1_WAIT) __builtin_amdgcn_s_sleep(H1_WAIT);
    for (unsigned spins = 0; spins < SPIN_CAP; ++spins) {
      MEM_FENCE();
      v4u tm[2], t[2];
      bool wm[2], want[2];
#pragma unroll
      for (int j = 0; j < 2; ++j) {
        wm[j] = bad4(ml[j]) != 0u;
        want[j] = badp(gr[j]) != 0u && (few || !wm[j]);
        tm[j] = __builtin_amdgcn_raw_buffer_load_b128(rw, wm[j] ? a_ml + j * 64 : OOB, 0, AUX_SC1);
        t[j] = __builtin_amdgcn_raw_buffer_load_b128(rw, want[j] ? a_o + j * 32 * PG_WG : OOB, 0, AUX_SC1);
      }
      SB();
      u32 bad = 0u;
      int miss = 0;
#pragma unroll
      for (int j = 0; j < 2; ++j) {
        ml[j] = wm[j] ? tm[j] : ml[j];
        gr[j] = want[j] ? t[j] : gr[j];
        const u32 bm = bad4(ml[j]);
        miss += __builtin_popcountll(__builtin_amdgcn_ballot_w64(bm != 0u));
        bad |= bm | badp(gr[j]);
      }
      if (all_lanes(bad == 0u)) break;
      few = miss <= 16 * FEW;
      if (!few) __builtin_amdgcn_s_sleep(POLL_SLEEP);
    }
  }
  {
    LAS unsigned char* ol = L + L_O + br * 1024;
    const int r = lane >> 4;
#pragma unroll
    for (int j = 0; j < 2; ++j) {
      const float m = u2f(ml[j].x);
      const float e = __builtin_amdgcn_exp2f(m - rows_max(m));
      const float fw = e * __builtin_amdgcn_rcpf(rows_sum(e * u2f(ml[j].z)));
      const f32x4 a = unpack4(gr[j]) * fw;
      const float o = red16(red32(a[0], a[1]), red32(a[2], a[3]));
      *(LAS unsigned short*)(ol + (2 * w + j) * 128 + ((lane & 15) * 4 + ((r & 1) * 2 + (r >> 1))) * 2) = (unsigned short)(pack_bf16(o, o) & 0xffffu);
    }
    lds_post(L, F_O + br, w);
    lds_wait(L, F_O + br, false);
    const v4u af0 = *(LAS v4u*)(ol + (lane & 7) * 128 + (lane >> 4) * 16), af1 = *(LAS v4u*)(ol + (lane & 7) * 128 + 64 + (lane >> 4) * 16);
#pragma unroll
    for (int c = 0; c < 2; ++c) {
      f32x4 d = mfma32(af0, bf[0][c], f32x4{0.f, 0.f, 0.f, 0.f});
      d = mfma32(af1, bf[1][c], d);
      const float e0 = dpp<0xB1>(d[0]), e1 = dpp<0xB1>(d[1]), e2 = dpp<0xB1>(d[2]), e3 = dpp<0xB1>(d[3]);
      __builtin_amdgcn_raw_buffer_store_b64(packh(odd ? e2 : d[0], odd ? d[2] : e0, t_og), rw, a_og + c * 4096, 0, AUX_SC1);
      __builtin_amdgcn_raw_buffer_store_b64(packh(odd ? e3 : d[1], odd ? d[3] : e1, t_og), rw, a_og + c * 4096 + 4 * 4096, 0, AUX_SC1);
    }
  }
  if (w == 0) {
    v4u a[4];
#pragma unroll
    for (int i = 0; i < 4; ++i) a[i] = v4u{0u, ~ta, 0u, 0u};
    const int vo = WS_OG + (br * NWG + wg) * 4096 + lane * 16;
    __builtin_amdgcn_s_sleep(H2_WAIT);
    for (unsigned spins = 0; spins < SPIN_CAP; ++spins) {
      MEM_FENCE();
      v4u t[4];
#pragma unroll
      for (int i = 0; i < 4; ++i) t[i] = __builtin_amdgcn_raw_buffer_load_b128(rw, badp(a[i]) != 0u ? vo + i * 1024 : OOB, 0, AUX_SC1);
      SB();
      u32 bad = 0u;
#pragma unroll
      for (int i = 0; i < 4; ++i) { a[i] = badp(a[i]) != 0u ? t[i] : a[i]; bad |= badp(a[i]); }
      if (all_lanes(bad == 0u)) break;
      __builtin_amdgcn_s_sleep(H2_SLEEP);
    }
    f32x4 s = (unpack4(a[0]) + unpack4(a[1])) + (unpack4(a[2]) + unpack4(a[3]));
#pragma unroll
    for (int j = 0; j < 4; ++j) { s[j] += dpp<0x124>(s[j]); s[j] += dpp<0x128>(s[j]); }
    const float o = red16(red32(s[0], s[1]), red32(s[2], s[3]));
    const int r = lane >> 4;
    const __amdgpu_buffer_rsrc_t rout = __builtin_amdgcn_make_buffer_rsrc(p.out, 0, NB * 2 * DM * 2, 0x00020000);
    __builtin_amdgcn_raw_buffer_store_b16((unsigned short)(pack_bf16(o, o) & 0xffffu), rout,
                                          (lane & 15) < 4 ? (kvh * 1024 + br * 512 + b * 64 + qd * 16 + (lane & 3) * 4 + (r & 1) * 2 + (r >> 1)) * 2 : OOB, 0, AUX_SC1);
    if (wg == 0 && grp == 1 && lane == 0) __builtin_amdgcn_raw_buffer_store_b64(v2u{tagA, tagB}, rw, 0, WS_EPOCH, AUX_SC1);
  }
}

extern "C" void kernel_launch(void* const* d_in, const int* in_sizes, int n_in, void* d_out, int out_size, void* d_ws, size_t ws_size,
                              hipStream_t stream) {
  Params p;
  p.x[0] = d_in[0]; p.k[0] = d_in[1]; p.v[0] = d_in[2]; p.wq[0] = d_in[3]; p.wo[0] = d_in[4];
  p.x[1] = d_in[5]; p.k[1] = d_in[6]; p.v[1] = d_in[7]; p.wq[1] = d_in[8]; p.wo[1] = d_in[9];
  p.out = d_out;
  p.ws = (unsigned char*)d_ws;
  k_fused<<<NWG, NTHR, 0, stream>>>(p);
}
```

```cpp
#include <hip/hip_runtime.h>
#include <hip/hip_bf16.h>
#include <stdint.h>
#include <utility>

typedef unsigned u32;
typedef u32 v4u __attribute__((ext_vector_type(4)));
typedef u32 v2u __attribute__((ext_vector_type(2)));
typedef float f32x4 __attribute__((ext_vector_type(4)));
typedef float f32x2 __attribute__((ext_vector_type(2)));
typedef __bf16 bf16x8 __attribute__((ext_vector_type(8)));
typedef __bf16 bf16x2 __attribute__((ext_vector_type(2)));
typedef short v4s __attribute__((ext_vector_type(4)));
#define LAS __attribute__((address_space(3)))

#define NFLY 5
#define NWIDE 6
#define NHEAD 3
#define TAU 217
#define TBASE 510
#define Q_WAIT 32
#define FEW 4
#define POLL_SLEEP 4
#define CODE_WARM 94
#define CW_WGS 32
#define HSLEEP true
#define H1_WAIT 6
#define H2_WAIT 16
#define H2_SLEEP 2

constexpr int NB = 8, DM = 512, HQ = 32, HKV = 8, SK = 8192, DH = 128;
constexpr int NWG = 256, NTHR = 512;
constexpr int RING = 16, TPI = RING / 4, NIT = 32 / TPI;
constexpr int KV_BYTES = NB * HKV * SK * DH * 2;
constexpr int W_BYTES = DM * HQ * DH * 2;
constexpr float CSC = 0.08838834764831845f * 1.4426950408889634f;
constexpr int AUX_NT = 2, AUX_SC1 = 16;
constexpr unsigned SPIN_CAP = 1u << 17, LSPIN_CAP = 1u << 22;
constexpr int OOB = 0x40000000;

constexpr int WS_EPOCH = 0;
constexpr int WS_QG = 4096;
constexpr int PG_HEAD = 512, PG_WG = 4 * PG_HEAD;
constexpr int WS_PG = WS_QG + 2 * 8 * 8 * 8192;
constexpr int WS_ML = WS_PG + 2 * NWG * PG_WG;
constexpr int WS_OG = WS_ML + 2 * 8 * 4 * 512;
constexpr int WS_END = WS_OG + 2 * NWG * 4096;

constexpr int MRG_REC = 132;
constexpr int L_W = 0;
constexpr int L_RED = 65536;
constexpr int L_MRG = L_RED + 16384;
constexpr int L_Q = L_MRG + 2 * 16 * MRG_REC * 4;
constexpr int L_SC = L_Q + 2048;
constexpr int L_O = L_SC + 32768;
constexpr int L_FLAG = L_O + 2048;
constexpr int L_END = L_FLAG + 128 + 16;
enum { F_Q = 0, F_LAST = 1, F_REC = 2  , F_PROJ = 4, F_WO = 5, F_O = 6   };

struct Params {
  const void* x[2];
  const void* k[2];
  const void* v[2];
  const void* wq[2];
  const void* wo[2];
  void* out;
  unsigned char* ws;
};

__device__ __forceinline__ f32x4 mfma32(v4u a, v4u b, f32x4 c) {
  return __builtin_amdgcn_mfma_f32_16x16x32_bf16(__builtin_bit_cast(bf16x8, a), __builtin_bit_cast(bf16x8, b), c, 0, 0, 0);
}
__device__ __forceinline__ f32x4 mfma16(v2u a, v2u b, f32x4 c) {
  return __builtin_amdgcn_mfma_f32_16x16x16bf16_1k(__builtin_bit_cast(v4s, a), __builtin_bit_cast(v4s, b), c, 0, 0, 0);
}
__device__ __forceinline__ u32 pack_bf16(float lo, float hi) {
  f32x2 f = {lo, hi};
  return __builtin_bit_cast(u32, __builtin_convertvector(f, bf16x2));
}
__device__ __forceinline__ v2u tr_read(LAS unsigned char* p) {
  return __builtin_bit_cast(v2u, __builtin_amdgcn_ds_read_tr16_b64_v4i16((LAS v4s*)p));
}
__device__ __forceinline__ u32 f2u(float f) { return __builtin_bit_cast(u32, f); }
__device__ __forceinline__ float u2f(u32 u) { return __builtin_bit_cast(float, u); }
template <int CTRL> __device__ __forceinline__ float dpp(float x) {
  return u2f((u32)__builtin_amdgcn_mov_dpp((int)f2u(x), CTRL, 0xf, 0xf, true));
}
__device__ __forceinline__ v2u packh(float v0, float v1, u32 tag8) {
  const u32 a = f2u(v0) + 0x800u, c = f2u(v1) + 0x800u;
  return v2u{(a >> 12) | ((c << 8) & 0xFFF00000u), (c >> 24) | tag8};
}
__device__ __forceinline__ f32x4 unpack4(v4u g) {
  return f32x4{u2f(g.x << 12), u2f(((g.x >> 20) << 12) | (g.y << 24)), u2f(g.z << 12), u2f(((g.z >> 20) << 12) | (g.w << 24))};
}
__device__ __forceinline__ float red32(float a, float b) {
  const auto s = __builtin_amdgcn_permlane32_swap(f2u(a), f2u(b), false, false);
  return u2f(s[0]) + u2f(s[1]);
}
__device__ __forceinline__ float red16(float a, float b) {
  const auto s = __builtin_amdgcn_permlane16_swap(f2u(a), f2u(b), false, false);
  return u2f(s[0]) + u2f(s[1]);
}
__device__ __forceinline__ float rows_max(float x) {
  const auto s = __builtin_amdgcn_permlane16_swap(f2u(x), f2u(x), false, false);
  x = fmaxf(u2f(s[0]), u2f(s[1]));
  const auto t = __builtin_amdgcn_permlane32_swap(f2u(x), f2u(x), false, false);
  return fmaxf(u2f(t[0]), u2f(t[1]));
}
__device__ __forceinline__ float rows_sum(float x) { x = red16(x, x); return red32(x, x); }
#define MEM_FENCE() asm volatile("" ::: "memory")
#define SB() __builtin_amdgcn_sched_barrier(0)
#define WAITVM(n) do { asm volatile("s_waitcnt vmcnt(%0)" :: "i"(n) : "memory"); SB(); } while (0)
#define WAITVM2(x, y, n, m) do { asm volatile("s_cmp_gt_u32 %0, %1\n\ts_cbranch_scc1 1f\n\ts_waitcnt vmcnt(%2)\n1:\n\ts_waitcnt vmcnt(%3)" :: "s"(x), "s"(y), "i"(n), "i"(m) : "memory", "scc"); SB(); } while (0)
#define AI __attribute__((always_inline))
__device__ __forceinline__ bool all_lanes(bool c) { return __builtin_amdgcn_ballot_w64(c) == ~0ull; }
template <int... I, class F> __device__ __forceinline__ void static_for_impl(std::integer_sequence<int, I...>, F&& f) {
  (f(std::integral_constant<int, I>{}), ...);
}
template <int N, class F> __device__ __forceinline__ void static_for(F&& f) { static_for_impl(std::make_integer_sequence<int, N>{}, f); }

__device__ __forceinline__ void lds_post(LAS unsigned char* L, int slot, int w) {
  MEM_FENCE();
  *(volatile LAS u32*)(L + L_FLAG + slot * 16 + w * 4) = 1u;
  MEM_FENCE();
}
__device__ __forceinline__ void lds_wait(LAS unsigned char* L, int slot, bool sleep) {
  for (unsigned spins = 0; spins < LSPIN_CAP; ++spins) {
    MEM_FENCE();
    const v4u v = *(volatile LAS v4u*)(L + L_FLAG + slot * 16);
    if (__builtin_amdgcn_readfirstlane(v.x & v.y & v.z & v.w) != 0u) break;
    if (sleep) __builtin_amdgcn_s_sleep(1);
  }
  MEM_FENCE();
}

__global__ void __launch_bounds__(NTHR, 2) k_fused(Params p) {
  unsigned long long pc;
  asm volatile("s_getpc_b64 %0" : "=s"(pc));
  const __amdgpu_buffer_rsrc_t rc = __builtin_amdgcn_make_buffer_rsrc(
      (void*)(pc & ~255ull), 0, (__builtin_amdgcn_readfirstlane(threadIdx.x >> 6) == 7 && blockIdx.x < CW_WGS) ? CODE_WARM * 128 : 0, 0x00020000);
  const u32 cw0 = __builtin_amdgcn_raw_buffer_load_b32(rc, (threadIdx.x & 63) * 128, 0, 0);
  const u32 cw1 = __builtin_amdgcn_raw_buffer_load_b32(rc, (threadIdx.x & 63) * 128, 8192, 0);
  SB();
  asm volatile("" :: "s"(p.x[0]), "s"(p.x[1]), "s"(p.k[0]), "s"(p.k[1]), "s"(p.v[0]), "s"(p.v[1]), "s"(p.wq[0]), "s"(p.wq[1]), "s"(p.wo[0]), "s"(p.wo[1]),
               "s"(p.out), "s"(p.ws));
  __shared__ __attribute__((aligned(16))) unsigned char smem[L_END];
  LAS unsigned char* L = (LAS unsigned char*)smem;
  const int lane = threadIdx.x & 63, w8 = __builtin_amdgcn_readfirstlane(threadIdx.x >> 6), w = w8 & 3;
  const int grp = w8 >> 2;
  const int wg = blockIdx.x, qd = wg & 3, ub = wg >> 2, b = ub >> 3, kvh = ub & 7;
  const int ks = (b & 1) * 4 + qd;
  const __amdgpu_buffer_rsrc_t rw = __builtin_amdgcn_make_buffer_rsrc((void*)p.ws, 0, WS_END, 0x00020000);
  u32 tagA, tagB;

  if (grp == 0) {
    const u32 t0 = (u32)__builtin_amdgcn_s_memrealtime();
    const int voffK = ((lane >> 2) & 3) * 256 + (lane >> 4) * 64 + (lane & 3) * 16;
    const int voffV = lane * 16;
    const int sbase = wg * 524288 + w * 4096;
    v4u ring[RING];
    {
      const __amdgpu_buffer_rsrc_t rk = __builtin_amdgcn_make_buffer_rsrc((void*)p.k[0], 0, KV_BYTES, 0x00020000);
      static_for<RING>([&](auto I) AI {
        constexpr int i = decltype(I)::value;
        if constexpr (i == NHEAD) __builtin_amdgcn_s_barrier();
        if constexpr (i >= NHEAD) WAITVM(NHEAD - 1);
        ring[i] = __builtin_amdgcn_raw_buffer_load_b128(rk, voffK + (i & 3) * 1024, sbase + (i >> 2) * 16384, AUX_NT);
        SB();
      });
    }
    const int jq = lane & 3;
    const u32 m0 = jq == 0 ? 0x0000ffffu : (jq == 1 ? 0xffff0000u : 0u);
    const u32 m1 = jq == 2 ? 0x0000ffffu : (jq == 3 ? 0xffff0000u : 0u);
    LAS unsigned char* scl = L + L_SC + w * 8192 + (lane >> 2) * 16;
    u32 due = TBASE - TAU;
    lds_wait(L, F_Q, false);
    tagA = __builtin_amdgcn_readfirstlane(*(LAS u32*)(L + L_FLAG + 128));
    tagB = __builtin_amdgcn_readfirstlane(*(LAS u32*)(L + L_FLAG + 132));
#pragma unroll 1
    for (int br = 0; br < 2; ++br) {
      const v4u qf = *(LAS v4u*)(L + L_Q + br * 1024 + voffK);
      const void* pk = p.k[br];
      const void* pv = p.v[br];
      float mx = -INFINITY;
#pragma unroll 1
      for (int it = 0; it < NIT; ++it) {
        const bool last = it == NIT - 1;
        const u32 now = (u32)__builtin_amdgcn_s_memrealtime() - t0;
        due += TAU;
        const __amdgpu_buffer_rsrc_t rn = __builtin_amdgcn_make_buffer_rsrc((void*)(last ? pv : pk), 0, KV_BYTES, 0x00020000);
        const int vn = last ? voffV : voffK;
        const int sn = sbase + (last ? 0 : (it + 1) * TPI * 16384);
        f32x4 st;
        static_for<RING>([&](auto I) AI {
          constexpr int i = decltype(I)::value;
          const f32x4 d = mfma32(ring[i], qf, f32x4{0.f, 0.f, 0.f, 0.f});
          float s = (dpp<0x00>(d[0]) + dpp<0x55>(d[1])) + (dpp<0xAA>(d[2]) + dpp<0xFF>(d[3]));
          mx = fmaxf(mx, s);
          asm volatile("" : "+v"(s), "+v"(mx));
          st[i & 3] = s;
          if constexpr ((i & 3) == 3) { if (jq == 0) *(LAS f32x4*)(scl + (it * TPI + (i >> 2)) * 256) = st; }
          SB();
          WAITVM2(now, due, NFLY - 1, NWIDE - 1);
          ring[i] = __builtin_amdgcn_raw_buffer_load_b128(rn, vn + (i & 3) * 1024, sn + (i >> 2) * 16384, AUX_NT);
          SB();
        });
      }
      const float mc = rows_max(mx) * CSC;
      float lsum = 0.f;
      f32x4 accLo = {0.f, 0.f, 0.f, 0.f}, accHi = {0.f, 0.f, 0.f, 0.f};
#pragma unroll 1
      for (int it = 0; it < NIT; ++it) {
        const bool last = it == NIT - 1;
        const u32 now = (u32)__builtin_amdgcn_s_memrealtime() - t0;
        due += TAU;
        const __amdgpu_buffer_rsrc_t rn =
            __builtin_amdgcn_make_buffer_rsrc((void*)(last ? p.k[1] : pv), 0, (last && br == 1) ? 0 : KV_BYTES, 0x00020000);
        const int vn = last ? voffK : voffV;
        const int sn = sbase + (last ? 0 : (it + 1) * TPI * 16384);
        if (last && br == 1) lds_post(L, F_LAST, w);
        f32x4 sv[TPI];
#pragma unroll
        for (int k = 0; k < TPI; ++k) sv[k] = *(LAS f32x4*)(scl + (it * TPI + k) * 256);
        static_for<RING>([&](auto I) AI {
          constexpr int i = decltype(I)::value;
          const float pe = __builtin_amdgcn_exp2f(fmaf(sv[i >> 2][i & 3], CSC, -mc));
          lsum += pe;
          asm volatile("" : "+v"(lsum));
          const u32 pkd = pack_bf16(pe, pe);
          const v2u a = {pkd & m0, pkd & m1};
          accLo = mfma16(a, v2u{ring[i].x, ring[i].y}, accLo);
          accHi = mfma16(a, v2u{ring[i].z, ring[i].w}, accHi);
          SB();
          WAITVM2(now, due, NFLY - 1, NWIDE - 1);
          ring[i] = __builtin_amdgcn_raw_buffer_load_b128(rn, vn + (i & 3) * 1024, sn + (i >> 2) * 16384, AUX_NT);
          SB();
        });
      }
      lsum = rows_sum(lsum);
      LAS float* mrg = (LAS float*)(L + L_MRG) + br * 16 * MRG_REC;
      LAS float* rec = mrg + (w * 4 + (lane >> 4)) * MRG_REC + 8 * (lane & 15);
      *(LAS f32x4*)rec = accLo;
      *(LAS f32x4*)(rec + 4) = accHi;
      if (lane < 16 && jq == 0) *(LAS f32x2*)(mrg + (w * 4 + (lane >> 2)) * MRG_REC + 128) = f32x2{mc, lsum};
      lds_post(L, F_REC + br, w);
    }
  } else {
    const int tid = w * 64 + lane;
    const v2u ep = __builtin_amdgcn_raw_buffer_load_b64(rw, 0, WS_EPOCH, AUX_SC1);
    v4u wr[2][4], xf[2];
#pragma unroll
    for (int br = 0; br < 2; ++br) {
      const __amdgpu_buffer_rsrc_t rq = __builtin_amdgcn_make_buffer_rsrc((void*)p.wq[br], 0, W_BYTES, 0x00020000);
      const __amdgpu_buffer_rsrc_t rx = __builtin_amdgcn_make_buffer_rsrc((void*)p.x[br], 0, NB * DM * 2, 0x00020000);
      const int so = (128 * qd + 32 * w) * (HQ * DH * 2) + (kvh * 512 + b * 64) * 2;
#pragma unroll
      for (int i = 0; i < 4; ++i)
        wr[br][i] = __builtin_amdgcn_raw_buffer_load_b128(rq, (lane >> 3) * (HQ * DH * 2) + (lane & 7) * 16, so + i * 8 * (HQ * DH * 2), AUX_NT);
      xf[br] = __builtin_amdgcn_raw_buffer_load_b128(rx, (lane & 7) * (DM * 2) + (lane >> 4) * 16, (128 * qd + 32 * w) * 2, 0);
      SB();
    }
    if (w == 0 && lane < 32) ((LAS u32*)(L + L_FLAG))[lane] = 0u;
    asm volatile("s_waitcnt lgkmcnt(0)" ::: "memory");
    __builtin_amdgcn_s_barrier();
    tagA = __builtin_amdgcn_readfirstlane(ep.x) + 1u;
    tagB = __builtin_amdgcn_readfirstlane(ep.y) + 0x9E3779B9u;
    if (lane == 0) *(LAS v2u*)(L + L_FLAG + 128) = v2u{tagA, tagB};
#pragma unroll
    for (int br = 0; br < 2; ++br) {
      LAS unsigned char* wl = L + L_W + br * 16384 + w * 4096;
#pragma unroll
      for (int i = 0; i < 4; ++i) *(LAS v4u*)(wl + i * 1024 + lane * 16) = wr[br][i];
      LAS unsigned char* rb = wl + (8 * (lane >> 4) + ((lane >> 2) & 3)) * 128 + (lane & 3) * 8;
      LAS f32x4* red = (LAS f32x4*)(L + L_RED + br * 8192);
#pragma unroll
      for (int ct = 0; ct < 4; ++ct) {
        const v2u lo = tr_read(rb + ct * 32), hi = tr_read(rb + ct * 32 + 512);
        const f32x4 d = mfma32(xf[br], v4u{lo.x, lo.y, hi.x, hi.y}, f32x4{0.f, 0.f, 0.f, 0.f});
        if (lane < 32) red[(w * 4 + ct) * 32 + lane] = d;
      }
    }
    lds_post(L, F_PROJ, w);
    lds_wait(L, F_PROJ, false);
    {
      const int pb = 4 * ((tid >> 3) & 1) + ((tid >> 4) & 3), pn = 16 * w + 2 * (tid & 7);
      const u32 tag8 = ((tid & 1) ? tagB : tagA) << 8;
#pragma unroll
      for (int br = 0; br < 2; ++br) {
        LAS float* rf = (LAS float*)(L + L_RED + br * 8192) + (w * 32 + 16 * ((tid >> 3) & 1) + 2 * (tid & 7)) * 4 + ((tid >> 4) & 3);
        float s0 = 0.f, s1 = 0.f;
#pragma unroll
        for (int ww = 0; ww < 4; ++ww) { s0 += rf[ww * 512]; s1 += rf[ww * 512 + 4]; }
        __builtin_amdgcn_raw_buffer_store_b64(packh(s0, s1, tag8), rw, WS_QG + (br * 8 + kvh) * 65536 + pb * 8192 + qd * 2048 + b * 256 + (pn >> 1) * 8, 0, AUX_SC1);
      }
    }
    SB();
    __builtin_amdgcn_s_sleep(Q_WAIT);
    SB();
    {
      const int qvo = WS_QG + (kvh * 8 + b) * 8192 + w * 512 + (lane >> 5) * 2048 + (lane & 31) * 16;
      const u32 ta = tagA << 8, tb = tagB << 8;
      v4u g[2][2];
      for (unsigned spins = 0; spins < SPIN_CAP; ++spins) {
        MEM_FENCE();
        u32 bad = 0u;
#pragma unroll
        for (int br = 0; br < 2; ++br)
#pragma unroll
          for (int i = 0; i < 2; ++i) g[br][i] = __builtin_amdgcn_raw_buffer_load_b128(rw, qvo, br * 8 * 65536 + i * 4096, AUX_SC1);
#pragma unroll
        for (int br = 0; br < 2; ++br)
#pragma unroll
          for (int i = 0; i < 2; ++i) bad |= (g[br][i].y ^ ta) | (g[br][i].w ^ tb);
        if (all_lanes((bad >> 8) == 0u)) break;
        __builtin_amdgcn_s_sleep(4);
      }
#pragma unroll
      for (int br = 0; br < 2; ++br) {
        const f32x4 s = unpack4(g[br][0]) + unpack4(g[br][1]);
        const float u0 = red32(s[0], s[2]), u1 = red32(s[1], s[3]);
        *(LAS u32*)(L + L_Q + br * 1024 + w * 256 + (lane & 31) * 8 + (lane >> 5) * 4) = pack_bf16(u0, u1);
      }
      lds_post(L, F_Q, w);
    }
  }

  asm volatile("" :: "v"(cw0), "v"(cw1));
  const int br = 1 - grp;
  const u32 ta = tagA << 8, tb = tagB << 8;
  auto bad4 = [=](const v4u& g) AI -> u32 { return (g.y ^ tagA) | (g.w ^ tagB); };
  auto badp = [=](const v4u& g) AI -> u32 { return ((g.y ^ ta) | (g.w ^ tb)) >> 8; };
  int a_po = WS_PG + (br * NWG + wg) * PG_WG + w * PG_HEAD + lane * 8;
  int a_pm = lane == 0 ? WS_ML + ((br * 8 + kvh) * 4 + w) * 512 + (b * 4 + qd) * 16 : OOB;
  int a_ml = WS_ML + ((br * 8 + kvh) * 4 + (ks >> 1)) * 512 + (w * 8 + (lane >> 4)) * 16;
  int a_o = WS_PG + (br * NWG + (2 * w * 8 + kvh) * 4 + (lane >> 4)) * PG_WG + (ks >> 1) * PG_HEAD + (ks & 1) * 256 + (lane & 15) * 16;
  const bool odd = lane & 1;
  int a_og = lane < 32 ? WS_OG + br * NWG * 4096 + (((2 * (b >> 1) + (w >> 1)) * 8 + 4 * (lane >> 4) + (odd ? 2 : 0)) * 4 + 2 * (w & 1)) * 4096 +
                             (kvh * 8 + ks) * 64 + ((lane & 15) >> 1) * 8
                       : OOB;
  u32 t_po = (lane & 1) ? tb : ta, t_og = (lane & 2) ? tb : ta;
  asm volatile("" : "+v"(a_po), "+v"(a_pm), "+v"(a_ml), "+v"(a_o), "+v"(a_og), "+v"(t_po), "+v"(t_og));

  lds_wait(L, F_REC + br, grp != 0);
  {
    LAS float* mrg = (LAS float*)(L + L_MRG) + br * 16 * MRG_REC;
    f32x2 ml4[4], o4[4];
#pragma unroll
    for (int ww = 0; ww < 4; ++ww) {
      ml4[ww] = *(LAS f32x2*)(mrg + (ww * 4 + w) * MRG_REC + 128);
      o4[ww] = *(LAS f32x2*)(mrg + (ww * 4 + w) * MRG_REC + 2 * lane);
    }
    float M = fmaxf(fmaxf(ml4[0].x, ml4[1].x), fmaxf(ml4[2].x, ml4[3].x));
    float Ls = 0.f, o0 = 0.f, o1 = 0.f;
#pragma unroll
    for (int ww = 0; ww < 4; ++ww) {
      const float f = __builtin_amdgcn_exp2f(ml4[ww].x - M);
      Ls = fmaf(f, ml4[ww].y, Ls);
      o0 = fmaf(f, o4[ww].x, o0);
      o1 = fmaf(f, o4[ww].y, o1);
    }
    v2u ph = packh(o0, o1, t_po);
    v4u wo4[2][4];
    if (grp == 1) {
      asm volatile("" : "+v"(ph), "+v"(M), "+v"(Ls));
      lds_wait(L, F_LAST, true);
#pragma unroll
      for (int bq = 0; bq < 2; ++bq) {
        const __amdgpu_buffer_rsrc_t ro = __builtin_amdgcn_make_buffer_rsrc((void*)p.wo[bq], 0, W_BYTES, 0x00020000);
        const int so = (kvh * 512 + 64 * ks + 16 * w) * (DM * 2) + (b >> 1) * 256;
#pragma unroll
        for (int i = 0; i < 4; ++i)
          wo4[bq][i] = __builtin_amdgcn_raw_buffer_load_b128(ro, (lane >> 4) * (DM * 2) + (lane & 15) * 16, so + i * 4 * (DM * 2), AUX_NT);
      }
      SB();
      lds_wait(L, F_REC + 1, HSLEEP);
    }
    __builtin_amdgcn_raw_buffer_store_b64(ph, rw, a_po, 0, AUX_SC1);
    __builtin_amdgcn_raw_buffer_store_b128(v4u{f2u(M), tagA, f2u(Ls), tagB}, rw, a_pm, 0, AUX_SC1);
    if (grp == 1) {
#pragma unroll
      for (int bq = 0; bq < 2; ++bq)
#pragma unroll
        for (int i = 0; i < 4; ++i) *(LAS v4u*)(L + L_W + (2 + bq) * 16384 + w * 4096 + i * 1024 + lane * 16) = wo4[bq][i];
      lds_post(L, F_WO, w);
    }
    lds_wait(L, F_WO, false);
  }
  v4u bf[2][2];
  {
    LAS unsigned char* rb = L + L_W + (2 + br) * 16384 + (8 * (lane >> 4) + ((lane >> 2) & 3)) * 256 + w * 64 + (lane & 3) * 8;
#pragma unroll
    for (int s = 0; s < 2; ++s)
#pragma unroll
      for (int c = 0; c < 2; ++c) {
        const v2u lo = tr_read(rb + s * 8192 + c * 32), hi = tr_read(rb + s * 8192 + c * 32 + 1024);
        bf[s][c] = v4u{lo.x, lo.y, hi.x, hi.y};
      }
  }
  v4u ml[2], gr[2];
  {
#pragma unroll
    for (int j = 0; j < 2; ++j) { ml[j] = v4u{0u, ~tagA, 0u, 0u}; gr[j] = v4u{0u, ~ta, 0u, 0u}; }
    bool few = false;
    if (H1_WAIT) __builtin_amdgcn_s_sleep(H1_WAIT);
    for (unsigned spins = 0; spins < SPIN_CAP; ++spins) {
      MEM_FENCE();
      v4u tm[2], t[2];
      bool wm[2], want[2];
#pragma unroll
      for (int j = 0; j < 2; ++j) {
        wm[j] = bad4(ml[j]) != 0u;
        want[j] = badp(gr[j]) != 0u && (few || !wm[j]);
        tm[j] = __builtin_amdgcn_raw_buffer_load_b128(rw, wm[j] ? a_ml + j * 64 : OOB, 0, AUX_SC1);
        t[j] = __builtin_amdgcn_raw_buffer_load_b128(rw, want[j] ? a_o + j * 32 * PG_WG : OOB, 0, AUX_SC1);
      }
      SB();
      u32 bad = 0u;
      int miss = 0;
#pragma unroll
      for (int j = 0; j < 2; ++j) {
        ml[j] = wm[j] ? tm[j] : ml[j];
        gr[j] = want[j] ? t[j] : gr[j];
        const u32 bm = bad4(ml[j]);
        miss += __builtin_popcountll(__builtin_amdgcn_ballot_w64(bm != 0u));
        bad |= bm | badp(gr[j]);
      }
      if (all_lanes(bad == 0u)) break;
      few = miss <= 16 * FEW;
      if (!few) __builtin_amdgcn_s_sleep(POLL_SLEEP);
    }
  }
  {
    LAS unsigned char* ol = L + L_O + br * 1024;
    const int r = lane >> 4;
#pragma unroll
    for (int j = 0; j < 2; ++j) {
      const float m = u2f(ml[j].x);
      const float e = __builtin_amdgcn_exp2f(m - rows_max(m));
      const float fw = e * __builtin_amdgcn_rcpf(rows_sum(e * u2f(ml[j].z)));
      const f32x4 a = unpack4(gr[j]) * fw;
      const float o = red16(red32(a[0], a[1]), red32(a[2], a[3]));
      *(LAS unsigned short*)(ol + (2 * w + j) * 128 + ((lane & 15) * 4 + ((r & 1) * 2 + (r >> 1))) * 2) = (unsigned short)(pack_bf16(o, o) & 0xffffu);
    }
    lds_post(L, F_O + br, w);
    lds_wait(L, F_O + br, false);
    const v4u af0 = *(LAS v4u*)(ol + (lane & 7) * 128 + (lane >> 4) * 16), af1 = *(LAS v4u*)(ol + (lane & 7) * 128 + 64 + (lane >> 4) * 16);
#pragma unroll
    for (int c = 0; c < 2; ++c) {
      f32x4 d = mfma32(af0, bf[0][c], f32x4{0.f, 0.f, 0.f, 0.f});
      d = mfma32(af1, bf[1][c], d);
      const float e0 = dpp<0xB1>(d[0]), e1 = dpp<0xB1>(d[1]), e2 = dpp<0xB1>(d[2]), e3 = dpp<0xB1>(d[3]);
      __builtin_amdgcn_raw_buffer_store_b64(packh(odd ? e2 : d[0], odd ? d[2] : e0, t_og), rw, a_og + c * 4096, 0, AUX_SC1);
      __builtin_amdgcn_raw_buffer_store_b64(packh(odd ? e3 : d[1], odd ? d[3] : e1, t_og), rw, a_og + c * 4096 + 4 * 4096, 0, AUX_SC1);
    }
  }
  if (w == 0) {
    v4u a[4];
#pragma unroll
    for (int i = 0; i < 4; ++i) a[i] = v4u{0u, ~ta, 0u, 0u};
    const int vo = WS_OG + (br * NWG + wg) * 4096 + lane * 16;
    __builtin_amdgcn_s_sleep(H2_WAIT);
    for (unsigned spins = 0; spins < SPIN_CAP; ++spins) {
      MEM_FENCE();
      v4u t[4];
#pragma unroll
      for (int i = 0; i < 4; ++i) t[i] = __builtin_amdgcn_raw_buffer_load_b128(rw, badp(a[i]) != 0u ? vo + i * 1024 : OOB, 0, AUX_SC1);
      SB();
      u32 bad = 0u;
#pragma unroll
      for (int i = 0; i < 4; ++i) { a[i] = badp(a[i]) != 0u ? t[i] : a[i]; bad |= badp(a[i]); }
      if (all_lanes(bad == 0u)) break;
      __builtin_amdgcn_s_sleep(H2_SLEEP);
    }
    f32x4 s = (unpack4(a[0]) + unpack4(a[1])) + (unpack4(a[2]) + unpack4(a[3]));
#pragma unroll
    for (int j = 0; j < 4; ++j) { s[j] += dpp<0x124>(s[j]); s[j] += dpp<0x128>(s[j]); }
    const float o = red16(red32(s[0], s[1]), red32(s[2], s[3]));
    const int r = lane >> 4;
    const __amdgpu_buffer_rsrc_t rout = __builtin_amdgcn_make_buffer_rsrc(p.out, 0, NB * 2 * DM * 2, 0x00020000);
    __builtin_amdgcn_raw_buffer_store_b16((unsigned short)(pack_bf16(o, o) & 0xffffu), rout,
                                          (lane & 15) < 4 ? (kvh * 1024 + br * 512 + b * 64 + qd * 16 + (lane & 3) * 4 + (r & 1) * 2 + (r >> 1)) * 2 : OOB, 0, AUX_SC1);
    if (wg == 0 && grp == 1 && lane == 0) __builtin_amdgcn_raw_buffer_store_b64(v2u{tagA, tagB}, rw, 0, WS_EPOCH, AUX_SC1);
  }
}

extern "C" void kernel_launch(void* const* d_in, const int* in_sizes, int n_in, void* d_out, int out_size, void* d_ws, size_t ws_size,
                              hipStream_t stream) {
  Params p;
  p.x[0] = d_in[0]; p.k[0] = d_in[1]; p.v[0] = d_in[2]; p.wq[0] = d_in[3]; p.wo[0] = d_in[4];
  p.x[1] = d_in[5]; p.k[1] = d_in[6]; p.v[1] = d_in[7]; p.wq[1] = d_in[8]; p.wo[1] = d_in[9];
  p.out = d_out;
  p.ws = (unsigned char*)d_ws;
  k_fused<<<NWG, NTHR, 0, stream>>>(p);
}
```

```cpp
#include <hip/hip_runtime.h>
#include <hip/hip_bf16.h>
#include <stdint.h>
#include <utility>

typedef unsigned u32;
typedef u32 v4u __attribute__((ext_vector_type(4)));
typedef u32 v2u __attribute__((ext_vector_type(2)));
typedef float f32x4 __attribute__((ext_vector_type(4)));
typedef float f32x2 __attribute__((ext_vector_type(2)));
typedef __bf16 bf16x8 __attribute__((ext_vector_type(8)));
typedef __bf16 bf16x2 __attribute__((ext_vector_type(2)));
typedef short v4s __attribute__((ext_vector_type(4)));
#define LAS __attribute__((address_space(3)))

#define NFLY 5
#define NWIDE 6
#define NHEAD 3
#define NSLOW 12
#define TAU 218
#define TBASE 400
#define Q_WAIT 32
#define FEW 4
#define POLL_SLEEP 4
#define HSLEEP true
#define H1_WAIT 6
#define H2_WAIT 16
#define H2_SLEEP 2

constexpr int NB = 8, DM = 512, HQ = 32, HKV = 8, SK = 8192, DH = 128;
constexpr int NWG = 256, NTHR = 512;
constexpr int RING = 16, TPI = RING / 4, NIT = 32 / TPI;
constexpr int KV_BYTES = NB * HKV * SK * DH * 2;
constexpr int W_BYTES = DM * HQ * DH * 2;
constexpr float CSC = 0.08838834764831845f * 1.4426950408889634f;
constexpr int AUX_NT = 2, AUX_SC1 = 16;
constexpr unsigned SPIN_CAP = 1u << 17, LSPIN_CAP = 1u << 22;
constexpr int OOB = 0x40000000;

constexpr int WS_EPOCH = 0;
constexpr int WS_QG = 4096;
constexpr int PG_HEAD = 512, PG_WG = 4 * PG_HEAD;
constexpr int WS_PG = WS_QG + 2 * 8 * 8 * 8192;
constexpr int WS_ML = WS_PG + 2 * NWG * PG_WG;
constexpr int WS_OG = WS_ML + 2 * 8 * 4 * 512;
constexpr int WS_END = WS_OG + 2 * NWG * 4096;

constexpr int MRG_REC = 132;
constexpr int L_W = 0;
constexpr int L_RED = 65536;
constexpr int L_MRG = L_RED + 16384;
constexpr int L_Q = L_MRG + 2 * 16 * MRG_REC * 4;
constexpr int L_SC = L_Q + 2048;
constexpr int L_O = L_SC + 32768;
constexpr int L_FLAG = L_O + 2048;
constexpr int L_END = L_FLAG + 128 + 16;
enum { F_Q = 0, F_LAST = 1, F_REC = 2  , F_PROJ = 4, F_WO = 5, F_O = 6   };

struct Params {
  const void* x[2];
  const void* k[2];
  const void* v[2];
  const void* wq[2];
  const void* wo[2];
  void* out;
  unsigned char* ws;
};

__device__ __forceinline__ f32x4 mfma32(v4u a, v4u b, f32x4 c) {
  return __builtin_amdgcn_mfma_f32_16x16x32_bf16(__builtin_bit_cast(bf16x8, a), __builtin_bit_cast(bf16x8, b), c, 0, 0, 0);
}
__device__ __forceinline__ f32x4 mfma16(v2u a, v2u b, f32x4 c) {
  return __builtin_amdgcn_mfma_f32_16x16x16bf16_1k(__builtin_bit_cast(v4s, a), __builtin_bit_cast(v4s, b), c, 0, 0, 0);
}
__device__ __forceinline__ u32 pack_bf16(float lo, float hi) {
  f32x2 f = {lo, hi};
  return __builtin_bit_cast(u32, __builtin_convertvector(f, bf16x2));
}
__device__ __forceinline__ v2u tr_read(LAS unsigned char* p) {
  return __builtin_bit_cast(v2u, __builtin_amdgcn_ds_read_tr16_b64_v4i16((LAS v4s*)p));
}
__device__ __forceinline__ u32 f2u(float f) { return __builtin_bit_cast(u32, f); }
__device__ __forceinline__ float u2f(u32 u) { return __builtin_bit_cast(float, u); }
template <int CTRL> __device__ __forceinline__ float dpp(float x) {
  return u2f((u32)__builtin_amdgcn_mov_dpp((int)f2u(x), CTRL, 0xf, 0xf, true));
}
__device__ __forceinline__ v2u packh(float v0, float v1, u32 tag8) {
  const u32 a = f2u(v0) + 0x800u, c = f2u(v1) + 0x800u;
  return v2u{(a >> 12) | ((c << 8) & 0xFFF00000u), (c >> 24) | tag8};
}
__device__ __forceinline__ f32x4 unpack4(v4u g) {
  return f32x4{u2f(g.x << 12), u2f(((g.x >> 20) << 12) | (g.y << 24)), u2f(g.z << 12), u2f(((g.z >> 20) << 12) | (g.w << 24))};
}
__device__ __forceinline__ float red32(float a, float b) {
  const auto s = __builtin_amdgcn_permlane32_swap(f2u(a), f2u(b), false, false);
  return u2f(s[0]) + u2f(s[1]);
}
__device__ __forceinline__ float red16(float a, float b) {
  const auto s = __builtin_amdgcn_permlane16_swap(f2u(a), f2u(b), false, false);
  return u2f(s[0]) + u2f(s[1]);
}
__device__ __forceinline__ float rows_max(float x) {
  const auto s = __builtin_amdgcn_permlane16_swap(f2u(x), f2u(x), false, false);
  x = fmaxf(u2f(s[0]), u2f(s[1]));
  const auto t = __builtin_amdgcn_permlane32_swap(f2u(x), f2u(x), false, false);
  return fmaxf(u2f(t[0]), u2f(t[1]));
}
__device__ __forceinline__ float rows_sum(float x) { x = red16(x, x); return red32(x, x); }
#define MEM_FENCE() asm volatile("" ::: "memory")
#define SB() __builtin_amdgcn_sched_barrier(0)
#define WAITVM(n) do { asm volatile("s_waitcnt vmcnt(%0)" :: "i"(n) : "memory"); SB(); } while (0)
#define WAITVM2(x, y, n, m) do { asm volatile("s_cmp_gt_u32 %0, %1\n\ts_cbranch_scc1 1f\n\ts_waitcnt vmcnt(%2)\n1:\n\ts_waitcnt vmcnt(%3)" :: "s"(x), "s"(y), "i"(n), "i"(m) : "memory", "scc"); SB(); } while (0)
#define AI __attribute__((always_inline))
__device__ __forceinline__ bool all_lanes(bool c) { return __builtin_amdgcn_ballot_w64(c) == ~0ull; }
template <int... I, class F> __device__ __forceinline__ void static_for_impl(std::integer_sequence<int, I...>, F&& f) {
  (f(std::integral_constant<int, I>{}), ...);
}
template <int N, class F> __device__ __forceinline__ void static_for(F&& f) { static_for_impl(std::make_integer_sequence<int, N>{}, f); }

__device__ __forceinline__ void lds_post(LAS unsigned char* L, int slot, int w) {
  MEM_FENCE();
  *(volatile LAS u32*)(L + L_FLAG + slot * 16 + w * 4) = 1u;
  MEM_FENCE();
}
__device__ __forceinline__ void lds_wait(LAS unsigned char* L, int slot, bool sleep) {
  for (unsigned spins = 0; spins < LSPIN_CAP; ++spins) {
    MEM_FENCE();
    const v4u v = *(volatile LAS v4u*)(L + L_FLAG + slot * 16);
    if (__builtin_amdgcn_readfirstlane(v.x & v.y & v.z & v.w) != 0u) break;
    if (sleep) __builtin_amdgcn_s_sleep(1);
  }
  MEM_FENCE();
}

__global__ void __launch_bounds__(NTHR, 2) k_fused(Params p) {
  asm volatile("" :: "s"(p.x[0]), "s"(p.x[1]), "s"(p.k[0]), "s"(p.k[1]), "s"(p.v[0]), "s"(p.v[1]), "s"(p.wq[0]), "s"(p.wq[1]), "s"(p.wo[0]), "s"(p.wo[1]),
               "s"(p.out), "s"(p.ws));
  __shared__ __attribute__((aligned(16))) unsigned char smem[L_END];
  LAS unsigned char* L = (LAS unsigned char*)smem;
  const int lane = threadIdx.x & 63, w8 = __builtin_amdgcn_readfirstlane(threadIdx.x >> 6), w = w8 & 3;
  const int grp = w8 >> 2;
  const int wg = blockIdx.x, qd = wg & 3, ub = wg >> 2, b = ub >> 3, kvh = ub & 7;
  const int ks = (b & 1) * 4 + qd;
  const __amdgpu_buffer_rsrc_t rw = __builtin_amdgcn_make_buffer_rsrc((void*)p.ws, 0, WS_END, 0x00020000);
  u32 tagA, tagB;

  if (grp == 0) {
    const u32 t0 = (u32)__builtin_amdgcn_s_memrealtime();
    const int voffK = ((lane >> 2) & 3) * 256 + (lane >> 4) * 64 + (lane & 3) * 16;
    const int voffV = lane * 16;
    const int sbase = wg * 524288 + w * 4096;
    v4u ring[RING], pre[RING];
    const __amdgpu_buffer_rsrc_t rk0 = __builtin_amdgcn_make_buffer_rsrc((void*)p.k[0], 0, KV_BYTES, 0x00020000);
    static_for<2 * RING>([&](auto I) AI {
      constexpr int i = decltype(I)::value;
      if constexpr (i == NHEAD) __builtin_amdgcn_s_barrier();
      if constexpr (i >= NHEAD && i < NSLOW) WAITVM(NHEAD - 1);
      if constexpr (i >= NSLOW) WAITVM(NFLY - 1);
      const v4u t = __builtin_amdgcn_raw_buffer_load_b128(rk0, voffK + (i & 3) * 1024, sbase + (i >> 2) * 16384, AUX_NT);
      if constexpr (i < RING) pre[i] = t; else ring[i - RING] = t;
      SB();
    });
    const int jq = lane & 3;
    const u32 m0 = jq == 0 ? 0x0000ffffu : (jq == 1 ? 0xffff0000u : 0u);
    const u32 m1 = jq == 2 ? 0x0000ffffu : (jq == 3 ? 0xffff0000u : 0u);
    LAS unsigned char* scl = L + L_SC + w * 8192 + (lane >> 2) * 16;
    u32 due = TBASE;
    lds_wait(L, F_Q, false);
    tagA = __builtin_amdgcn_readfirstlane(*(LAS u32*)(L + L_FLAG + 128));
    tagB = __builtin_amdgcn_readfirstlane(*(LAS u32*)(L + L_FLAG + 132));
    auto score = [&](v4u k, v4u q, float& mx) AI -> float {
      const f32x4 d = mfma32(k, q, f32x4{0.f, 0.f, 0.f, 0.f});
      float s = (dpp<0x00>(d[0]) + dpp<0x55>(d[1])) + (dpp<0xAA>(d[2]) + dpp<0xFF>(d[3]));
      mx = fmaxf(mx, s);
      asm volatile("" : "+v"(s), "+v"(mx));
      return s;
    };
    float mx0 = -INFINITY;
    {
      const v4u qf = *(LAS v4u*)(L + L_Q + voffK);
      const u32 now = (u32)__builtin_amdgcn_s_memrealtime() - t0;
      due += TAU;
      f32x4 st, sp;
      static_for<RING>([&](auto I) AI {
        constexpr int i = decltype(I)::value;
        st[i & 3] = score(ring[i], qf, mx0);
        SB();
        WAITVM2(now, due, NFLY - 1, NWIDE - 1);
        ring[i] = __builtin_amdgcn_raw_buffer_load_b128(rk0, voffK + (i & 3) * 1024, sbase + (2 * TPI + (i >> 2)) * 16384, AUX_NT);
        SB();
        sp[i & 3] = score(pre[i], qf, mx0);
        if constexpr ((i & 3) == 3) {
          if (jq == 0) {
            *(LAS f32x4*)(scl + (i >> 2) * 256) = sp;
            *(LAS f32x4*)(scl + (TPI + (i >> 2)) * 256) = st;
          }
        }
        SB();
      });
    }
#pragma unroll 1
    for (int br = 0; br < 2; ++br) {
      const v4u qf = *(LAS v4u*)(L + L_Q + br * 1024 + voffK);
      const void* pk = p.k[br];
      const void* pv = p.v[br];
      float mx = br == 0 ? mx0 : -INFINITY;
#pragma unroll 1
      for (int it = br == 0 ? 2 : 0; it < NIT; ++it) {
        const bool last = it == NIT - 1;
        const u32 now = (u32)__builtin_amdgcn_s_memrealtime() - t0;
        due += TAU;
        const __amdgpu_buffer_rsrc_t rn = __builtin_amdgcn_make_buffer_rsrc((void*)(last ? pv : pk), 0, KV_BYTES, 0x00020000);
        const int vn = last ? voffV : voffK;
        const int sn = sbase + (last ? 0 : (it + 1) * TPI * 16384);
        f32x4 st;
        static_for<RING>([&](auto I) AI {
          constexpr int i = decltype(I)::value;
          st[i & 3] = score(ring[i], qf, mx);
          if constexpr ((i & 3) == 3) { if (jq == 0) *(LAS f32x4*)(scl + (it * TPI + (i >> 2)) * 256) = st; }
          SB();
          WAITVM2(now, due, NFLY - 1, NWIDE - 1);
          ring[i] = __builtin_amdgcn_raw_buffer_load_b128(rn, vn + (i & 3) * 1024, sn + (i >> 2) * 16384, AUX_NT);
          SB();
        });
      }
      const float mc = rows_max(mx) * CSC;
      float lsum = 0.f;
      f32x4 accLo = {0.f, 0.f, 0.f, 0.f}, accHi = {0.f, 0.f, 0.f, 0.f};
#pragma unroll 1
      for (int it = 0; it < NIT; ++it) {
        const bool last = it == NIT - 1;
        const u32 now = (u32)__builtin_amdgcn_s_memrealtime() - t0;
        due += TAU;
        const __amdgpu_buffer_rsrc_t rn =
            __builtin_amdgcn_make_buffer_rsrc((void*)(last ? p.k[1] : pv), 0, (last && br == 1) ? 0 : KV_BYTES, 0x00020000);
        const int vn = last ? voffK : voffV;
        const int sn = sbase + (last ? 0 : (it + 1) * TPI * 16384);
        if (last && br == 1) lds_post(L, F_LAST, w);
        f32x4 sv[TPI];
#pragma unroll
        for (int k = 0; k < TPI; ++k) sv[k] = *(LAS f32x4*)(scl + (it * TPI + k) * 256);
        static_for<RING>([&](auto I) AI {
          constexpr int i = decltype(I)::value;
          const float pe = __builtin_amdgcn_exp2f(fmaf(sv[i >> 2][i & 3], CSC, -mc));
          lsum += pe;
          asm volatile("" : "+v"(lsum));
          const u32 pkd = pack_bf16(pe, pe);
          const v2u a = {pkd & m0, pkd & m1};
          accLo = mfma16(a, v2u{ring[i].x, ring[i].y}, accLo);
          accHi = mfma16(a, v2u{ring[i].z, ring[i].w}, accHi);
          SB();
          WAITVM2(now, due, NFLY - 1, NWIDE - 1);
          ring[i] = __builtin_amdgcn_raw_buffer_load_b128(rn, vn + (i & 3) * 1024, sn + (i >> 2) * 16384, AUX_NT);
          SB();
        });
      }
      lsum = rows_sum(lsum);
      LAS float* mrg = (LAS float*)(L + L_MRG) + br * 16 * MRG_REC;
      LAS float* rec = mrg + (w * 4 + (lane >> 4)) * MRG_REC + 8 * (lane & 15);
      *(LAS f32x4*)rec = accLo;
      *(LAS f32x4*)(rec + 4) = accHi;
      if (lane < 16 && jq == 0) *(LAS f32x2*)(mrg + (w * 4 + (lane >> 2)) * MRG_REC + 128) = f32x2{mc, lsum};
      lds_post(L, F_REC + br, w);
    }
  } else {
    const int tid = w * 64 + lane;
    const v2u ep = __builtin_amdgcn_raw_buffer_load_b64(rw, 0, WS_EPOCH, AUX_SC1);
    v4u wr[2][4], xf[2];
#pragma unroll
    for (int br = 0; br < 2; ++br) {
      const __amdgpu_buffer_rsrc_t rq = __builtin_amdgcn_make_buffer_rsrc((void*)p.wq[br], 0, W_BYTES, 0x00020000);
      const __amdgpu_buffer_rsrc_t rx = __builtin_amdgcn_make_buffer_rsrc((void*)p.x[br], 0, NB * DM * 2, 0x00020000);
      const int so = (128 * qd + 32 * w) * (HQ * DH * 2) + (kvh * 512 + b * 64) * 2;
#pragma unroll
      for (int i = 0; i < 4; ++i)
        wr[br][i] = __builtin_amdgcn_raw_buffer_load_b128(rq, (lane >> 3) * (HQ * DH * 2) + (lane & 7) * 16, so + i * 8 * (HQ * DH * 2), AUX_NT);
      xf[br] = __builtin_amdgcn_raw_buffer_load_b128(rx, (lane & 7) * (DM * 2) + (lane >> 4) * 16, (128 * qd + 32 * w) * 2, 0);
      SB();
    }
    if (w == 0 && lane < 32) ((LAS u32*)(L + L_FLAG))[lane] = 0u;
    asm volatile("s_waitcnt lgkmcnt(0)" ::: "memory");
    __builtin_amdgcn_s_barrier();
    tagA = __builtin_amdgcn_readfirstlane(ep.x) + 1u;
    tagB = __builtin_amdgcn_readfirstlane(ep.y) + 0x9E3779B9u;
    if (lane == 0) *(LAS v2u*)(L + L_FLAG + 128) = v2u{tagA, tagB};
#pragma unroll
    for (int br = 0; br < 2; ++br) {
      LAS unsigned char* wl = L + L_W + br * 16384 + w * 4096;
#pragma unroll
      for (int i = 0; i < 4; ++i) *(LAS v4u*)(wl + i * 1024 + lane * 16) = wr[br][i];
      LAS unsigned char* rb = wl + (8 * (lane >> 4) + ((lane >> 2) & 3)) * 128 + (lane & 3) * 8;
      LAS f32x4* red = (LAS f32x4*)(L + L_RED + br * 8192);
#pragma unroll
      for (int ct = 0; ct < 4; ++ct) {
        const v2u lo = tr_read(rb + ct * 32), hi = tr_read(rb + ct * 32 + 512);
        const f32x4 d = mfma32(xf[br], v4u{lo.x, lo.y, hi.x, hi.y}, f32x4{0.f, 0.f, 0.f, 0.f});
        if (lane < 32) red[(w * 4 + ct) * 32 + lane] = d;
      }
    }
    lds_post(L, F_PROJ, w);
    lds_wait(L, F_PROJ, false);
    {
      const int pb = 4 * ((tid >> 3) & 1) + ((tid >> 4) & 3), pn = 16 * w + 2 * (tid & 7);
      const u32 tag8 = ((tid & 1) ? tagB : tagA) << 8;
#pragma unroll
      for (int br = 0; br < 2; ++br) {
        LAS float* rf = (LAS float*)(L + L_RED + br * 8192) + (w * 32 + 16 * ((tid >> 3) & 1) + 2 * (tid & 7)) * 4 + ((tid >> 4) & 3);
        float s0 = 0.f, s1 = 0.f;
#pragma unroll
        for (int ww = 0; ww < 4; ++ww) { s0 += rf[ww * 512]; s1 += rf[ww * 512 + 4]; }
        __builtin_amdgcn_raw_buffer_store_b64(packh(s0, s1, tag8), rw, WS_QG + (br * 8 + kvh) * 65536 + pb * 8192 + qd * 2048 + b * 256 + (pn >> 1) * 8, 0, AUX_SC1);
      }
    }
    SB();
    __builtin_amdgcn_s_sleep(Q_WAIT);
    SB();
    {
      const int qvo = WS_QG + (kvh * 8 + b) * 8192 + w * 512 + (lane >> 5) * 2048 + (lane & 31) * 16;
      const u32 ta = tagA << 8, tb = tagB << 8;
      v4u g[2][2];
      for (unsigned spins = 0; spins < SPIN_CAP; ++spins) {
        MEM_FENCE();
        u32 bad = 0u;
#pragma unroll
        for (int br = 0; br < 2; ++br)
#pragma unroll
          for (int i = 0; i < 2; ++i) g[br][i] = __builtin_amdgcn_raw_buffer_load_b128(rw, qvo, br * 8 * 65536 + i * 4096, AUX_SC1);
#pragma unroll
        for (int br = 0; br < 2; ++br)
#pragma unroll
          for (int i = 0; i < 2; ++i) bad |= (g[br][i].y ^ ta) | (g[br][i].w ^ tb);
        if (all_lanes((bad >> 8) == 0u)) break;
        __builtin_amdgcn_s_sleep(4);
      }
#pragma unroll
      for (int br = 0; br < 2; ++br) {
        const f32x4 s = unpack4(g[br][0]) + unpack4(g[br][1]);
        const float u0 = red32(s[0], s[2]), u1 = red32(s[1], s[3]);
        *(LAS u32*)(L + L_Q + br * 1024 + w * 256 + (lane & 31) * 8 + (lane >> 5) * 4) = pack_bf16(u0, u1);
      }
      lds_post(L, F_Q, w);
    }
  }

  const int br = 1 - grp;
  const u32 ta = tagA << 8, tb = tagB << 8;
  auto bad4 = [=](const v4u& g) AI -> u32 { return (g.y ^ tagA) | (g.w ^ tagB); };
  auto badp = [=](const v4u& g) AI -> u32 { return ((g.y ^ ta) | (g.w ^ tb)) >> 8; };
  int a_po = WS_PG + (br * NWG + wg) * PG_WG + w * PG_HEAD + lane * 8;
  int a_pm = lane == 0 ? WS_ML + ((br * 8 + kvh) * 4 + w) * 512 + (b * 4 + qd) * 16 : OOB;
  int a_ml = WS_ML + ((br * 8 + kvh) * 4 + (ks >> 1)) * 512 + (w * 8 + (lane >> 4)) * 16;
  int a_o = WS_PG + (br * NWG + (2 * w * 8 + kvh) * 4 + (lane >> 4)) * PG_WG + (ks >> 1) * PG_HEAD + (ks & 1) * 256 + (lane & 15) * 16;
  const bool odd = lane & 1;
  int a_og = lane < 32 ? WS_OG + br * NWG * 4096 + (((2 * (b >> 1) + (w >> 1)) * 8 + 4 * (lane >> 4) + (odd ? 2 : 0)) * 4 + 2 * (w & 1)) * 4096 +
                             (kvh * 8 + ks) * 64 + ((lane & 15) >> 1) * 8
                       : OOB;
  u32 t_po = (lane & 1) ? tb : ta, t_og = (lane & 2) ? tb : ta;
  asm volatile("" : "+v"(a_po), "+v"(a_pm), "+v"(a_ml), "+v"(a_o), "+v"(a_og), "+v"(t_po), "+v"(t_og));

  lds_wait(L, F_REC + br, grp != 0);
  {
    LAS float* mrg = (LAS float*)(L + L_MRG) + br * 16 * MRG_REC;
    f32x2 ml4[4], o4[4];
#pragma unroll
    for (int ww = 0; ww < 4; ++ww) {
      ml4[ww] = *(LAS f32x2*)(mrg + (ww * 4 + w) * MRG_REC + 128);
      o4[ww] = *(LAS f32x2*)(mrg + (ww * 4 + w) * MRG_REC + 2 * lane);
    }
    float M = fmaxf(fmaxf(ml4[0].x, ml4[1].x), fmaxf(ml4[2].x, ml4[3].x));
    float Ls = 0.f, o0 = 0.f, o1 = 0.f;
#pragma unroll
    for (int ww = 0; ww < 4; ++ww) {
      const float f = __builtin_amdgcn_exp2f(ml4[ww].x - M);
      Ls = fmaf(f, ml4[ww].y, Ls);
      o0 = fmaf(f, o4[ww].x, o0);
      o1 = fmaf(f, o4[ww].y, o1);
    }
    v2u ph = packh(o0, o1, t_po);
    v4u wo4[2][4];
    if (grp == 1) {
      asm volatile("" : "+v"(ph), "+v"(M), "+v"(Ls));
      lds_wait(L, F_LAST, true);
#pragma unroll
      for (int bq = 0; bq < 2; ++bq) {
        const __amdgpu_buffer_rsrc_t ro = __builtin_amdgcn_make_buffer_rsrc((void*)p.wo[bq], 0, W_BYTES, 0x00020000);
        const int so = (kvh * 512 + 64 * ks + 16 * w) * (DM * 2) + (b >> 1) * 256;
#pragma unroll
        for (int i = 0; i < 4; ++i)
          wo4[bq][i] = __builtin_amdgcn_raw_buffer_load_b128(ro, (lane >> 4) * (DM * 2) + (lane & 15) * 16, so + i * 4 * (DM * 2), AUX_NT);
      }
      SB();
      lds_wait(L, F_REC + 1, HSLEEP);
    }
    __builtin_amdgcn_raw_buffer_store_b64(ph, rw, a_po, 0, AUX_SC1);
    __builtin_amdgcn_raw_buffer_store_b128(v4u{f2u(M), tagA, f2u(Ls), tagB}, rw, a_pm, 0, AUX_SC1);
    if (grp == 1) {
#pragma unroll
      for (int bq = 0; bq < 2; ++bq)
#pragma unroll
        for (int i = 0; i < 4; ++i) *(LAS v4u*)(L + L_W + (2 + bq) * 16384 + w * 4096 + i * 1024 + lane * 16) = wo4[bq][i];
      lds_post(L, F_WO, w);
    }
    lds_wait(L, F_WO, false);
  }
  v4u bf[2][2];
  {
    LAS unsigned char* rb = L + L_W + (2 + br) * 16384 + (8 * (lane >> 4) + ((lane >> 2) & 3)) * 256 + w * 64 + (lane & 3) * 8;
#pragma unroll
    for (int s = 0; s < 2; ++s)
#pragma unroll
      for (int c = 0; c < 2; ++c) {
        const v2u lo = tr_read(rb + s * 8192 + c * 32), hi = tr_read(rb + s * 8192 + c * 32 + 1024);
        bf[s][c] = v4u{lo.x, lo.y, hi.x, hi.y};
      }
  }
  v4u ml[2], gr[2];
  {
#pragma unroll
    for (int j = 0; j < 2; ++j) { ml[j] = v4u{0u, ~tagA, 0u, 0u}; gr[j] = v4u{0u, ~ta, 0u, 0u}; }
    bool few = false;
    if (H1_WAIT) __builtin_amdgcn_s_sleep(H1_WAIT);
    for (unsigned spins = 0; spins < SPIN_CAP; ++spins) {
      MEM_FENCE();
      v4u tm[2], t[2];
      bool wm[2], want[2];
#pragma unroll
      for (int j = 0; j < 2; ++j) {
        wm[j] = bad4(ml[j]) != 0u;
        want[j] = badp(gr[j]) != 0u && (few || !wm[j]);
        tm[j] = __builtin_amdgcn_raw_buffer_load_b128(rw, wm[j] ? a_ml + j * 64 : OOB, 0, AUX_SC1);
        t[j] = __builtin_amdgcn_raw_buffer_load_b128(rw, want[j] ? a_o + j * 32 * PG_WG : OOB, 0, AUX_SC1);
      }
      SB();
      u32 bad = 0u;
      int miss = 0;
#pragma unroll
      for (int j = 0; j < 2; ++j) {
        ml[j] = wm[j] ? tm[j] : ml[j];
        gr[j] = want[j] ? t[j] : gr[j];
        const u32 bm = bad4(ml[j]);
        miss += __builtin_popcountll(__builtin_amdgcn_ballot_w64(bm != 0u));
        bad |= bm | badp(gr[j]);
      }
      if (all_lanes(bad == 0u)) break;
      few = miss <= 16 * FEW;
      if (!few) __builtin_amdgcn_s_sleep(POLL_SLEEP);
    }
  }
  {
    LAS unsigned char* ol = L + L_O + br * 1024;
    const int r = lane >> 4;
#pragma unroll
    for (int j = 0; j < 2; ++j) {
      const float m = u2f(ml[j].x);
      const float e = __builtin_amdgcn_exp2f(m - rows_max(m));
      const float fw = e * __builtin_amdgcn_rcpf(rows_sum(e * u2f(ml[j].z)));
      const f32x4 a = unpack4(gr[j]) * fw;
      const float o = red16(red32(a[0], a[1]), red32(a[2], a[3]));
      *(LAS unsigned short*)(ol + (2 * w + j) * 128 + ((lane & 15) * 4 + ((r & 1) * 2 + (r >> 1))) * 2) = (unsigned short)(pack_bf16(o, o) & 0xffffu);
    }
    lds_post(L, F_O + br, w);
    lds_wait(L, F_O + br, false);
    const v4u af0 = *(LAS v4u*)(ol + (lane & 7) * 128 + (lane >> 4) * 16), af1 = *(LAS v4u*)(ol + (lane & 7) * 128 + 64 + (lane >> 4) * 16);
#pragma unroll
    for (int c = 0; c < 2; ++c) {
      f32x4 d = mfma32(af0, bf[0][c], f32x4{0.f, 0.f, 0.f, 0.f});
      d = mfma32(af1, bf[1][c], d);
      const float e0 = dpp<0xB1>(d[0]), e1 = dpp<0xB1>(d[1]), e2 = dpp<0xB1>(d[2]), e3 = dpp<0xB1>(d[3]);
      __builtin_amdgcn_raw_buffer_store_b64(packh(odd ? e2 : d[0], odd ? d[2] : e0, t_og), rw, a_og + c * 4096, 0, AUX_SC1);
      __builtin_amdgcn_raw_buffer_store_b64(packh(odd ? e3 : d[1], odd ? d[3] : e1, t_og), rw, a_og + c * 4096 + 4 * 4096, 0, AUX_SC1);
    }
  }
  if (w == 0) {
    v4u a[4];
#pragma unroll
    for (int i = 0; i < 4; ++i) a[i] = v4u{0u, ~ta, 0u, 0u};
    const int vo = WS_OG + (br * NWG + wg) * 4096 + lane * 16;
    __builtin_amdgcn_s_sleep(H2_WAIT);
    for (unsigned spins = 0; spins < SPIN_CAP; ++spins) {
      MEM_FENCE();
      v4u t[4];
#pragma unroll
      for (int i = 0; i < 4; ++i) t[i] = __builtin_amdgcn_raw_buffer_load_b128(rw, badp(a[i]) != 0u ? vo + i * 1024 : OOB, 0, AUX_SC1);
      SB();
      u32 bad = 0u;
#pragma unroll
      for (int i = 0; i < 4; ++i) { a[i] = badp(a[i]) != 0u ? t[i] : a[i]; bad |= badp(a[i]); }
      if (all_lanes(bad == 0u)) break;
      __builtin_amdgcn_s_sleep(H2_SLEEP);
    }
    f32x4 s = (unpack4(a[0]) + unpack4(a[1])) + (unpack4(a[2]) + unpack4(a[3]));
#pragma unroll
    for (int j = 0; j < 4; ++j) { s[j] += dpp<0x124>(s[j]); s[j] += dpp<0x128>(s[j]); }
    const float o = red16(red32(s[0], s[1]), red32(s[2], s[3]));
    const int r = lane >> 4;
    const __amdgpu_buffer_rsrc_t rout = __builtin_amdgcn_make_buffer_rsrc(p.out, 0, NB * 2 * DM * 2, 0x00020000);
    __builtin_amdgcn_raw_buffer_store_b16((unsigned short)(pack_bf16(o, o) & 0xffffu), rout,
                                          (lane & 15) < 4 ? (kvh * 1024 + br * 512 + b * 64 + qd * 16 + (lane & 3) * 4 + (r & 1) * 2 + (r >> 1)) * 2 : OOB, 0, AUX_SC1);
    if (wg == 0 && grp == 1 && lane == 0) __builtin_amdgcn_raw_buffer_store_b64(v2u{tagA, tagB}, rw, 0, WS_EPOCH, AUX_SC1);
  }
}

extern "C" void kernel_launch(void* const* d_in, const int* in_sizes, int n_in, void* d_out, int out_size, void* d_ws, size_t ws_size,
                              hipStream_t stream) {
  Params p;
  p.x[0] = d_in[0]; p.k[0] = d_in[1]; p.v[0] = d_in[2]; p.wq[0] = d_in[3]; p.wo[0] = d_in[4];
  p.x[1] = d_in[5]; p.k[1] = d_in[6]; p.v[1] = d_in[7]; p.wq[1] = d_in[8]; p.wo[1] = d_in[9];
  p.out = d_out;
  p.ws = (unsigned char*)d_ws;
  k_fused<<<NWG, NTHR, 0, stream>>>(p);
}
```

```cpp
#include <hip/hip_runtime.h>
#include <hip/hip_bf16.h>
#include <stdint.h>
#include <utility>

typedef unsigned u32;
typedef u32 v4u __attribute__((ext_vector_type(4)));
typedef u32 v2u __attribute__((ext_vector_type(2)));
typedef float f32x4 __attribute__((ext_vector_type(4)));
typedef float f32x2 __attribute__((ext_vector_type(2)));
typedef __bf16 bf16x8 __attribute__((ext_vector_type(8)));
typedef __bf16 bf16x2 __attribute__((ext_vector_type(2)));
typedef short v4s __attribute__((ext_vector_type(4)));
#define LAS __attribute__((address_space(3)))

#define NFLY 5
#define NWIDE 6
#define NHEAD 3
#define NSLOW 12
#define TAU 218
#define TBASE 400
#define Q_WAIT 32
#define FEW 4
#define POLL_SLEEP 4
#define HSLEEP true
#define H1_WAIT 6
#define H2_WAIT 16
#define H2_SLEEP 2

constexpr int NB = 8, DM = 512, HQ = 32, HKV = 8, SK = 8192, DH = 128;
constexpr int NWG = 256, NTHR = 512;
constexpr int RING = 16, TPI = RING / 4, NIT = 32 / TPI;
constexpr int KV_BYTES = NB * HKV * SK * DH * 2;
constexpr int W_BYTES = DM * HQ * DH * 2;
constexpr float CSC = 0.08838834764831845f * 1.4426950408889634f;
constexpr int AUX_NT = 2, AUX_SC1 = 16;
constexpr unsigned SPIN_CAP = 1u << 17, LSPIN_CAP = 1u << 22;
constexpr int OOB = 0x40000000;

constexpr int WS_EPOCH = 0;
constexpr int WS_QG = 4096;
constexpr int PG_HEAD = 512, PG_WG = 4 * PG_HEAD;
constexpr int WS_PG = WS_QG + 2 * 8 * 8 * 8192;
constexpr int WS_ML = WS_PG + 2 * NWG * PG_WG;
constexpr int WS_OG = WS_ML + 2 * 8 * 4 * 512;
constexpr int WS_END = WS_OG + 2 * NWG * 4096;

constexpr int MRG_REC = 132;
constexpr int L_W = 0;
constexpr int L_RED = 65536;
constexpr int L_MRG = L_RED + 16384;
constexpr int L_Q = L_MRG + 2 * 16 * MRG_REC * 4;
constexpr int L_SC = L_Q + 2048;
constexpr int L_O = L_SC + 32768;
constexpr int L_FLAG = L_O + 2048;
constexpr int L_END = L_FLAG + 128 + 16;
enum { F_Q = 0, F_LAST = 1, F_REC = 2  , F_PROJ = 4, F_WO = 5, F_O = 6   };

struct Params {
  const void* x[2];
  const void* k[2];
  const void* v[2];
  const void* wq[2];
  const void* wo[2];
  void* out;
  unsigned char* ws;
};

__device__ __forceinline__ f32x4 mfma32(v4u a, v4u b, f32x4 c) {
  return __builtin_amdgcn_mfma_f32_16x16x32_bf16(__builtin_bit_cast(bf16x8, a), __builtin_bit_cast(bf16x8, b), c, 0, 0, 0);
}
__device__ __forceinline__ f32x4 mfma16(v2u a, v2u b, f32x4 c) {
  return __builtin_amdgcn_mfma_f32_16x16x16bf16_1k(__builtin_bit_cast(v4s, a), __builtin_bit_cast(v4s, b), c, 0, 0, 0);
}
__device__ __forceinline__ u32 pack_bf16(float lo, float hi) {
  f32x2 f = {lo, hi};
  return __builtin_bit_cast(u32, __builtin_convertvector(f, bf16x2));
}
__device__ __forceinline__ v2u tr_read(LAS unsigned char* p) {
  return __builtin_bit_cast(v2u, __builtin_amdgcn_ds_read_tr16_b64_v4i16((LAS v4s*)p));
}
__device__ __forceinline__ u32 f2u(float f) { return __builtin_bit_cast(u32, f); }
__device__ __forceinline__ float u2f(u32 u) { return __builtin_bit_cast(float, u); }
template <int CTRL> __device__ __forceinline__ float dpp(float x) {
  return u2f((u32)__builtin_amdgcn_mov_dpp((int)f2u(x), CTRL, 0xf, 0xf, true));
}
__device__ __forceinline__ v2u packh(float v0, float v1, u32 tag8) {
  const u32 a = f2u(v0) + 0x800u, c = f2u(v1) + 0x800u;
  return v2u{(a >> 12) | ((c << 8) & 0xFFF00000u), (c >> 24) | tag8};
}
__device__ __forceinline__ f32x4 unpack4(v4u g) {
  return f32x4{u2f(g.x << 12), u2f(((g.x >> 20) << 12) | (g.y << 24)), u2f(g.z << 12), u2f(((g.z >> 20) << 12) | (g.w << 24))};
}
__device__ __forceinline__ float red32(float a, float b) {
  const auto s = __builtin_amdgcn_permlane32_swap(f2u(a), f2u(b), false, false);
  return u2f(s[0]) + u2f(s[1]);
}
__device__ __forceinline__ float red16(float a, float b) {
  const auto s = __builtin_amdgcn_permlane16_swap(f2u(a), f2u(b), false, false);
  return u2f(s[0]) + u2f(s[1]);
}
__device__ __forceinline__ float rows_max(float x) {
  const auto s = __builtin_amdgcn_permlane16_swap(f2u(x), f2u(x), false, false);
  x = fmaxf(u2f(s[0]), u2f(s[1]));
  const auto t = __builtin_amdgcn_permlane32_swap(f2u(x), f2u(x), false, false);
  return fmaxf(u2f(t[0]), u2f(t[1]));
}
__device__ __forceinline__ float rows_sum(float x) { x = red16(x, x); return red32(x, x); }
#define MEM_FENCE() asm volatile("" ::: "memory")
#define SB() __builtin_amdgcn_sched_barrier(0)
#define WAITVM(n) do { asm volatile("s_waitcnt vmcnt(%0)" :: "i"(n) : "memory"); SB(); } while (0)
#define WAITVM2(x, y, n, m) do { asm volatile("s_cmp_gt_u32 %0, %1\n\ts_cbranch_scc1 1f\n\ts_waitcnt vmcnt(%2)\n1:\n\ts_waitcnt vmcnt(%3)" :: "s"(x), "s"(y), "i"(n), "i"(m) : "memory", "scc"); SB(); } while (0)
#define AI __attribute__((always_inline))
__device__ __forceinline__ bool all_lanes(bool c) { return __builtin_amdgcn_ballot_w64(c) == ~0ull; }
template <int... I, class F> __device__ __forceinline__ void static_for_impl(std::integer_sequence<int, I...>, F&& f) {
  (f(std::integral_constant<int, I>{}), ...);
}
template <int N, class F> __device__ __forceinline__ void static_for(F&& f) { static_for_impl(std::make_integer_sequence<int, N>{}, f); }

__device__ __forceinline__ void lds_post(LAS unsigned char* L, int slot, int w) {
  MEM_FENCE();
  *(volatile LAS u32*)(L + L_FLAG + slot * 16 + w * 4) = 1u;
  MEM_FENCE();
}
__device__ __forceinline__ void lds_wait(LAS unsigned char* L, int slot, bool sleep) {
  for (unsigned spins = 0; spins < LSPIN_CAP; ++spins) {
    MEM_FENCE();
    const v4u v = *(volatile LAS v4u*)(L + L_FLAG + slot * 16);
    if (__builtin_amdgcn_readfirstlane(v.x & v.y & v.z & v.w) != 0u) break;
    if (sleep) __builtin_amdgcn_s_sleep(1);
  }
  MEM_FENCE();
}

__global__ void __launch_bounds__(NTHR, 2) k_fused(Params p) {
  asm volatile("" :: "s"(p.x[0]), "s"(p.x[1]), "s"(p.k[0]), "s"(p.k[1]), "s"(p.v[0]), "s"(p.v[1]), "s"(p.wq[0]), "s"(p.wq[1]), "s"(p.wo[0]), "s"(p.wo[1]),
               "s"(p.out), "s"(p.ws));
  __shared__ __attribute__((aligned(16))) unsigned char smem[L_END];
  LAS unsigned char* L = (LAS unsigned char*)smem;
  const int lane = threadIdx.x & 63, w8 = __builtin_amdgcn_readfirstlane(threadIdx.x >> 6), w = w8 & 3;
  const int grp = w8 >> 2;
  const int wg = blockIdx.x, qd = wg & 3, ub = wg >> 2, b = ub >> 3, kvh = ub & 7;
  const int ks = (b & 1) * 4 + qd;
  const __amdgpu_buffer_rsrc_t rw = __builtin_amdgcn_make_buffer_rsrc((void*)p.ws, 0, WS_END, 0x00020000);
  u32 tagA, tagB; const u32 pt0 = (u32)__builtin_amdgcn_s_memrealtime(); u32 pst = 0;
#define PROBE(i) do { if (99 == (i)) pst = (u32)__builtin_amdgcn_s_memrealtime() - pt0; } while (0)

  if (grp == 0) {
    const u32 t0 = (u32)__builtin_amdgcn_s_memrealtime();
    const int voffK = ((lane >> 2) & 3) * 256 + (lane >> 4) * 64 + (lane & 3) * 16;
    const int voffV = lane * 16;
    const int sbase = wg * 524288 + w * 4096;
    v4u ring[RING], pre[RING];
    const __amdgpu_buffer_rsrc_t rk0 = __builtin_amdgcn_make_buffer_rsrc((void*)p.k[0], 0, KV_BYTES, 0x00020000);
    static_for<2 * RING>([&](auto I) AI {
      constexpr int i = decltype(I)::value;
      if constexpr (i == NHEAD) __builtin_amdgcn_s_barrier();
      if constexpr (i >= NHEAD && i < NSLOW) WAITVM(NHEAD - 1);
      if constexpr (i >= NSLOW) WAITVM(NFLY - 1);
      const v4u t = __builtin_amdgcn_raw_buffer_load_b128(rk0, voffK + (i & 3) * 1024, sbase + (i >> 2) * 16384, AUX_NT);
      if constexpr (i < RING) pre[i] = t; else ring[i - RING] = t;
      SB();
    });
    const int jq = lane & 3;
    const u32 m0 = jq == 0 ? 0x0000ffffu : (jq == 1 ? 0xffff0000u : 0u);
    const u32 m1 = jq == 2 ? 0x0000ffffu : (jq == 3 ? 0xffff0000u : 0u);
    LAS unsigned char* scl = L + L_SC + w * 8192 + (lane >> 2) * 16;
    u32 due = TBASE;
    PROBE(2);
    lds_wait(L, F_Q, false);
    PROBE(3);
    tagA = __builtin_amdgcn_readfirstlane(*(LAS u32*)(L + L_FLAG + 128));
    tagB = __builtin_amdgcn_readfirstlane(*(LAS u32*)(L + L_FLAG + 132));
    auto score = [&](v4u k, v4u q, float& mx) AI -> float {
      const f32x4 d = mfma32(k, q, f32x4{0.f, 0.f, 0.f, 0.f});
      float s = (dpp<0x00>(d[0]) + dpp<0x55>(d[1])) + (dpp<0xAA>(d[2]) + dpp<0xFF>(d[3]));
      mx = fmaxf(mx, s);
      asm volatile("" : "+v"(s), "+v"(mx));
      return s;
    };
    float mx0 = -INFINITY;
    {
      const v4u qf = *(LAS v4u*)(L + L_Q + voffK);
      const u32 now = (u32)__builtin_amdgcn_s_memrealtime() - t0;
      due += TAU;
      f32x4 st, sp;
      static_for<RING>([&](auto I) AI {
        constexpr int i = decltype(I)::value;
        st[i & 3] = score(ring[i], qf, mx0);
        SB();
        WAITVM2(now, due, NFLY - 1, NWIDE - 1);
        ring[i] = __builtin_amdgcn_raw_buffer_load_b128(rk0, voffK + (i & 3) * 1024, sbase + (2 * TPI + (i >> 2)) * 16384, AUX_NT);
        SB();
        sp[i & 3] = score(pre[i], qf, mx0);
        if constexpr ((i & 3) == 3) {
          if (jq == 0) {
            *(LAS f32x4*)(scl + (i >> 2) * 256) = sp;
            *(LAS f32x4*)(scl + (TPI + (i >> 2)) * 256) = st;
          }
        }
        SB();
      });
    }
#pragma unroll 1
    for (int br = 0; br < 2; ++br) {
      const v4u qf = *(LAS v4u*)(L + L_Q + br * 1024 + voffK);
      const void* pk = p.k[br];
      const void* pv = p.v[br];
      float mx = br == 0 ? mx0 : -INFINITY;
#pragma unroll 1
      for (int it = br == 0 ? 2 : 0; it < NIT; ++it) {
        const bool last = it == NIT - 1;
        const u32 now = (u32)__builtin_amdgcn_s_memrealtime() - t0;
        due += TAU;
        const __amdgpu_buffer_rsrc_t rn = __builtin_amdgcn_make_buffer_rsrc((void*)(last ? pv : pk), 0, KV_BYTES, 0x00020000);
        const int vn = last ? voffV : voffK;
        const int sn = sbase + (last ? 0 : (it + 1) * TPI * 16384);
        f32x4 st;
        static_for<RING>([&](auto I) AI {
          constexpr int i = decltype(I)::value;
          st[i & 3] = score(ring[i], qf, mx);
          if constexpr ((i & 3) == 3) { if (jq == 0) *(LAS f32x4*)(scl + (it * TPI + (i >> 2)) * 256) = st; }
          SB();
          WAITVM2(now, due, NFLY - 1, NWIDE - 1);
          ring[i] = __builtin_amdgcn_raw_buffer_load_b128(rn, vn + (i & 3) * 1024, sn + (i >> 2) * 16384, AUX_NT);
          SB();
        });
      }
      const float mc = rows_max(mx) * CSC;
      float lsum = 0.f;
      f32x4 accLo = {0.f, 0.f, 0.f, 0.f}, accHi = {0.f, 0.f, 0.f, 0.f};
#pragma unroll 1
      for (int it = 0; it < NIT; ++it) {
        const bool last = it == NIT - 1;
        const u32 now = (u32)__builtin_amdgcn_s_memrealtime() - t0;
        due += TAU;
        const __amdgpu_buffer_rsrc_t rn =
            __builtin_amdgcn_make_buffer_rsrc((void*)(last ? p.k[1] : pv), 0, (last && br == 1) ? 0 : KV_BYTES, 0x00020000);
        const int vn = last ? voffK : voffV;
        const int sn = sbase + (last ? 0 : (it + 1) * TPI * 16384);
        if (last && br == 1) lds_post(L, F_LAST, w);
        f32x4 sv[TPI];
#pragma unroll
        for (int k = 0; k < TPI; ++k) sv[k] = *(LAS f32x4*)(scl + (it * TPI + k) * 256);
        static_for<RING>([&](auto I) AI {
          constexpr int i = decltype(I)::value;
          const float pe = __builtin_amdgcn_exp2f(fmaf(sv[i >> 2][i & 3], CSC, -mc));
          lsum += pe;
          asm volatile("" : "+v"(lsum));
          const u32 pkd = pack_bf16(pe, pe);
          const v2u a = {pkd & m0, pkd & m1};
          accLo = mfma16(a, v2u{ring[i].x, ring[i].y}, accLo);
          accHi = mfma16(a, v2u{ring[i].z, ring[i].w}, accHi);
          SB();
          WAITVM2(now, due, NFLY - 1, NWIDE - 1);
          ring[i] = __builtin_amdgcn_raw_buffer_load_b128(rn, vn + (i & 3) * 1024, sn + (i >> 2) * 16384, AUX_NT);
          SB();
        });
      }
      lsum = rows_sum(lsum);
      LAS float* mrg = (LAS float*)(L + L_MRG) + br * 16 * MRG_REC;
      LAS float* rec = mrg + (w * 4 + (lane >> 4)) * MRG_REC + 8 * (lane & 15);
      *(LAS f32x4*)rec = accLo;
      *(LAS f32x4*)(rec + 4) = accHi;
      if (lane < 16 && jq == 0) *(LAS f32x2*)(mrg + (w * 4 + (lane >> 2)) * MRG_REC + 128) = f32x2{mc, lsum};
      lds_post(L, F_REC + br, w);
      if (br == 0) PROBE(4); else PROBE(5);
    }
  } else {
    const int tid = w * 64 + lane;
    const v2u ep = __builtin_amdgcn_raw_buffer_load_b64(rw, 0, WS_EPOCH, AUX_SC1);
    v4u wr[2][4], xf[2];
#pragma unroll
    for (int br = 0; br < 2; ++br) {
      const __amdgpu_buffer_rsrc_t rq = __builtin_amdgcn_make_buffer_rsrc((void*)p.wq[br], 0, W_BYTES, 0x00020000);
      const __amdgpu_buffer_rsrc_t rx = __builtin_amdgcn_make_buffer_rsrc((void*)p.x[br], 0, NB * DM * 2, 0x00020000);
      const int so = (128 * qd + 32 * w) * (HQ * DH * 2) + (kvh * 512 + b * 64) * 2;
#pragma unroll
      for (int i = 0; i < 4; ++i)
        wr[br][i] = __builtin_amdgcn_raw_buffer_load_b128(rq, (lane >> 3) * (HQ * DH * 2) + (lane & 7) * 16, so + i * 8 * (HQ * DH * 2), AUX_NT);
      xf[br] = __builtin_amdgcn_raw_buffer_load_b128(rx, (lane & 7) * (DM * 2) + (lane >> 4) * 16, (128 * qd + 32 * w) * 2, 0);
      SB();
    }
    if (w == 0 && lane < 32) ((LAS u32*)(L + L_FLAG))[lane] = 0u;
    asm volatile("s_waitcnt lgkmcnt(0)" ::: "memory");
    __builtin_amdgcn_s_barrier();
    tagA = __builtin_amdgcn_readfirstlane(ep.x) + 1u;
    tagB = __builtin_amdgcn_readfirstlane(ep.y) + 0x9E3779B9u;
    if (lane == 0) *(LAS v2u*)(L + L_FLAG + 128) = v2u{tagA, tagB};
#pragma unroll
    for (int br = 0; br < 2; ++br) {
      LAS unsigned char* wl = L + L_W + br * 16384 + w * 4096;
#pragma unroll
      for (int i = 0; i < 4; ++i) *(LAS v4u*)(wl + i * 1024 + lane * 16) = wr[br][i];
      LAS unsigned char* rb = wl + (8 * (lane >> 4) + ((lane >> 2) & 3)) * 128 + (lane & 3) * 8;
      LAS f32x4* red = (LAS f32x4*)(L + L_RED + br * 8192);
#pragma unroll
      for (int ct = 0; ct < 4; ++ct) {
        const v2u lo = tr_read(rb + ct * 32), hi = tr_read(rb + ct * 32 + 512);
        const f32x4 d = mfma32(xf[br], v4u{lo.x, lo.y, hi.x, hi.y}, f32x4{0.f, 0.f, 0.f, 0.f});
        if (lane < 32) red[(w * 4 + ct) * 32 + lane] = d;
      }
    }
    lds_post(L, F_PROJ, w);
    lds_wait(L, F_PROJ, false);
    {
      const int pb = 4 * ((tid >> 3) & 1) + ((tid >> 4) & 3), pn = 16 * w + 2 * (tid & 7);
      const u32 tag8 = ((tid & 1) ? tagB : tagA) << 8;
#pragma unroll
      for (int br = 0; br < 2; ++br) {
        LAS float* rf = (LAS float*)(L + L_RED + br * 8192) + (w * 32 + 16 * ((tid >> 3) & 1) + 2 * (tid & 7)) * 4 + ((tid >> 4) & 3);
        float s0 = 0.f, s1 = 0.f;
#pragma unroll
        for (int ww = 0; ww < 4; ++ww) { s0 += rf[ww * 512]; s1 += rf[ww * 512 + 4]; }
        __builtin_amdgcn_raw_buffer_store_b64(packh(s0, s1, tag8), rw, WS_QG + (br * 8 + kvh) * 65536 + pb * 8192 + qd * 2048 + b * 256 + (pn >> 1) * 8, 0, AUX_SC1);
      }
    }
    SB();
    __builtin_amdgcn_s_sleep(Q_WAIT);
    SB();
    {
      const int qvo = WS_QG + (kvh * 8 + b) * 8192 + w * 512 + (lane >> 5) * 2048 + (lane & 31) * 16;
      const u32 ta = tagA << 8, tb = tagB << 8;
      v4u g[2][2];
      for (unsigned spins = 0; spins < SPIN_CAP; ++spins) {
        MEM_FENCE();
        u32 bad = 0u;
#pragma unroll
        for (int br = 0; br < 2; ++br)
#pragma unroll
          for (int i = 0; i < 2; ++i) g[br][i] = __builtin_amdgcn_raw_buffer_load_b128(rw, qvo, br * 8 * 65536 + i * 4096, AUX_SC1);
#pragma unroll
        for (int br = 0; br < 2; ++br)
#pragma unroll
          for (int i = 0; i < 2; ++i) bad |= (g[br][i].y ^ ta) | (g[br][i].w ^ tb);
        if (all_lanes((bad >> 8) == 0u)) break;
        __builtin_amdgcn_s_sleep(4);
      }
#pragma unroll
      for (int br = 0; br < 2; ++br) {
        const f32x4 s = unpack4(g[br][0]) + unpack4(g[br][1]);
        const float u0 = red32(s[0], s[2]), u1 = red32(s[1], s[3]);
        *(LAS u32*)(L + L_Q + br * 1024 + w * 256 + (lane & 31) * 8 + (lane >> 5) * 4) = pack_bf16(u0, u1);
      }
      lds_post(L, F_Q, w);
    }
  }

  const int br = 1 - grp;
  const u32 ta = tagA << 8, tb = tagB << 8;
  auto bad4 = [=](const v4u& g) AI -> u32 { return (g.y ^ tagA) | (g.w ^ tagB); };
  auto badp = [=](const v4u& g) AI -> u32 { return ((g.y ^ ta) | (g.w ^ tb)) >> 8; };
  int a_po = WS_PG + (br * NWG + wg) * PG_WG + w * PG_HEAD + lane * 8;
  int a_pm = lane == 0 ? WS_ML + ((br * 8 + kvh) * 4 + w) * 512 + (b * 4 + qd) * 16 : OOB;
  int a_ml = WS_ML + ((br * 8 + kvh) * 4 + (ks >> 1)) * 512 + (w * 8 + (lane >> 4)) * 16;
  int a_o = WS_PG + (br * NWG + (2 * w * 8 + kvh) * 4 + (lane >> 4)) * PG_WG + (ks >> 1) * PG_HEAD + (ks & 1) * 256 + (lane & 15) * 16;
  const bool odd = lane & 1;
  int a_og = lane < 32 ? WS_OG + br * NWG * 4096 + (((2 * (b >> 1) + (w >> 1)) * 8 + 4 * (lane >> 4) + (odd ? 2 : 0)) * 4 + 2 * (w & 1)) * 4096 +
                             (kvh * 8 + ks) * 64 + ((lane & 15) >> 1) * 8
                       : OOB;
  u32 t_po = (lane & 1) ? tb : ta, t_og = (lane & 2) ? tb : ta;
  asm volatile("" : "+v"(a_po), "+v"(a_pm), "+v"(a_ml), "+v"(a_o), "+v"(a_og), "+v"(t_po), "+v"(t_og));

  lds_wait(L, F_REC + br, grp != 0);
  {
    LAS float* mrg = (LAS float*)(L + L_MRG) + br * 16 * MRG_REC;
    f32x2 ml4[4], o4[4];
#pragma unroll
    for (int ww = 0; ww < 4; ++ww) {
      ml4[ww] = *(LAS f32x2*)(mrg + (ww * 4 + w) * MRG_REC + 128);
      o4[ww] = *(LAS f32x2*)(mrg + (ww * 4 + w) * MRG_REC + 2 * lane);
    }
    float M = fmaxf(fmaxf(ml4[0].x, ml4[1].x), fmaxf(ml4[2].x, ml4[3].x));
    float Ls = 0.f, o0 = 0.f, o1 = 0.f;
#pragma unroll
    for (int ww = 0; ww < 4; ++ww) {
      const float f = __builtin_amdgcn_exp2f(ml4[ww].x - M);
      Ls = fmaf(f, ml4[ww].y, Ls);
      o0 = fmaf(f, o4[ww].x, o0);
      o1 = fmaf(f, o4[ww].y, o1);
    }
    v2u ph = packh(o0, o1, t_po);
    v4u wo4[2][4];
    if (grp == 1) {
      asm volatile("" : "+v"(ph), "+v"(M), "+v"(Ls));
      lds_wait(L, F_LAST, true);
#pragma unroll
      for (int bq = 0; bq < 2; ++bq) {
        const __amdgpu_buffer_rsrc_t ro = __builtin_amdgcn_make_buffer_rsrc((void*)p.wo[bq], 0, W_BYTES, 0x00020000);
        const int so = (kvh * 512 + 64 * ks + 16 * w) * (DM * 2) + (b >> 1) * 256;
#pragma unroll
        for (int i = 0; i < 4; ++i)
          wo4[bq][i] = __builtin_amdgcn_raw_buffer_load_b128(ro, (lane >> 4) * (DM * 2) + (lane & 15) * 16, so + i * 4 * (DM * 2), AUX_NT);
      }
      SB();
      lds_wait(L, F_REC + 1, HSLEEP);
    }
    __builtin_amdgcn_raw_buffer_store_b64(ph, rw, a_po, 0, AUX_SC1);
    __builtin_amdgcn_raw_buffer_store_b128(v4u{f2u(M), tagA, f2u(Ls), tagB}, rw, a_pm, 0, AUX_SC1);
    if (grp == 1) {
#pragma unroll
      for (int bq = 0; bq < 2; ++bq)
#pragma unroll
        for (int i = 0; i < 4; ++i) *(LAS v4u*)(L + L_W + (2 + bq) * 16384 + w * 4096 + i * 1024 + lane * 16) = wo4[bq][i];
      lds_post(L, F_WO, w);
    }
    lds_wait(L, F_WO, false);
  }
  v4u bf[2][2];
  {
    LAS unsigned char* rb = L + L_W + (2 + br) * 16384 + (8 * (lane >> 4) + ((lane >> 2) & 3)) * 256 + w * 64 + (lane & 3) * 8;
#pragma unroll
    for (int s = 0; s < 2; ++s)
#pragma unroll
      for (int c = 0; c < 2; ++c) {
        const v2u lo = tr_read(rb + s * 8192 + c * 32), hi = tr_read(rb + s * 8192 + c * 32 + 1024);
        bf[s][c] = v4u{lo.x, lo.y, hi.x, hi.y};
      }
  }
  v4u ml[2], gr[2];
  {
#pragma unroll
    for (int j = 0; j < 2; ++j) { ml[j] = v4u{0u, ~tagA, 0u, 0u}; gr[j] = v4u{0u, ~ta, 0u, 0u}; }
    bool few = false;
    if (H1_WAIT) __builtin_amdgcn_s_sleep(H1_WAIT);
    for (unsigned spins = 0; spins < SPIN_CAP; ++spins) {
      MEM_FENCE();
      v4u tm[2], t[2];
      bool wm[2], want[2];
#pragma unroll
      for (int j = 0; j < 2; ++j) {
        wm[j] = bad4(ml[j]) != 0u;
        want[j] = badp(gr[j]) != 0u && (few || !wm[j]);
        tm[j] = __builtin_amdgcn_raw_buffer_load_b128(rw, wm[j] ? a_ml + j * 64 : OOB, 0, AUX_SC1);
        t[j] = __builtin_amdgcn_raw_buffer_load_b128(rw, want[j] ? a_o + j * 32 * PG_WG : OOB, 0, AUX_SC1);
      }
      SB();
      u32 bad = 0u;
      int miss = 0;
#pragma unroll
      for (int j = 0; j < 2; ++j) {
        ml[j] = wm[j] ? tm[j] : ml[j];
        gr[j] = want[j] ? t[j] : gr[j];
        const u32 bm = bad4(ml[j]);
        miss += __builtin_popcountll(__builtin_amdgcn_ballot_w64(bm != 0u));
        bad |= bm | badp(gr[j]);
      }
      if (all_lanes(bad == 0u)) break;
      few = miss <= 16 * FEW;
      if (!few) __builtin_amdgcn_s_sleep(POLL_SLEEP);
    }
  }
  PROBE(8);
  {
    LAS unsigned char* ol = L + L_O + br * 1024;
    const int r = lane >> 4;
#pragma unroll
    for (int j = 0; j < 2; ++j) {
      const float m = u2f(ml[j].x);
      const float e = __builtin_amdgcn_exp2f(m - rows_max(m));
      const float fw = e * __builtin_amdgcn_rcpf(rows_sum(e * u2f(ml[j].z)));
      const f32x4 a = unpack4(gr[j]) * fw;
      const float o = red16(red32(a[0], a[1]), red32(a[2], a[3]));
      *(LAS unsigned short*)(ol + (2 * w + j) * 128 + ((lane & 15) * 4 + ((r & 1) * 2 + (r >> 1))) * 2) = (unsigned short)(pack_bf16(o, o) & 0xffffu);
    }
    lds_post(L, F_O + br, w);
    lds_wait(L, F_O + br, false);
    const v4u af0 = *(LAS v4u*)(ol + (lane & 7) * 128 + (lane >> 4) * 16), af1 = *(LAS v4u*)(ol + (lane & 7) * 128 + 64 + (lane >> 4) * 16);
#pragma unroll
    for (int c = 0; c < 2; ++c) {
      f32x4 d = mfma32(af0, bf[0][c], f32x4{0.f, 0.f, 0.f, 0.f});
      d = mfma32(af1, bf[1][c], d);
      const float e0 = dpp<0xB1>(d[0]), e1 = dpp<0xB1>(d[1]), e2 = dpp<0xB1>(d[2]), e3 = dpp<0xB1>(d[3]);
      __builtin_amdgcn_raw_buffer_store_b64(packh(odd ? e2 : d[0], odd ? d[2] : e0, t_og), rw, a_og + c * 4096, 0, AUX_SC1);
      __builtin_amdgcn_raw_buffer_store_b64(packh(odd ? e3 : d[1], odd ? d[3] : e1, t_og), rw, a_og + c * 4096 + 4 * 4096, 0, AUX_SC1);
    }
  }
  if (w == 0) {
    v4u a[4];
#pragma unroll
    for (int i = 0; i < 4; ++i) a[i] = v4u{0u, ~ta, 0u, 0u};
    const int vo = WS_OG + (br * NWG + wg) * 4096 + lane * 16;
    __builtin_amdgcn_s_sleep(H2_WAIT);
    for (unsigned spins = 0; spins < SPIN_CAP; ++spins) {
      MEM_FENCE();
      v4u t[4];
#pragma unroll
      for (int i = 0; i < 4; ++i) t[i] = __builtin_amdgcn_raw_buffer_load_b128(rw, badp(a[i]) != 0u ? vo + i * 1024 : OOB, 0, AUX_SC1);
      SB();
      u32 bad = 0u;
#pragma unroll
      for (int i = 0; i < 4; ++i) { a[i] = badp(a[i]) != 0u ? t[i] : a[i]; bad |= badp(a[i]); }
      if (all_lanes(bad == 0u)) break;
      __builtin_amdgcn_s_sleep(H2_SLEEP);
    }
    f32x4 s = (unpack4(a[0]) + unpack4(a[1])) + (unpack4(a[2]) + unpack4(a[3]));
#pragma unroll
    for (int j = 0; j < 4; ++j) { s[j] += dpp<0x124>(s[j]); s[j] += dpp<0x128>(s[j]); }
    const float o = red16(red32(s[0], s[1]), red32(s[2], s[3]));
    const int r = lane >> 4;
    const __amdgpu_buffer_rsrc_t rout = __builtin_amdgcn_make_buffer_rsrc(p.out, 0, NB * 2 * DM * 2, 0x00020000);
    __builtin_amdgcn_raw_buffer_store_b16((unsigned short)(pack_bf16(o, o) & 0xffffu), rout,
                                          (lane & 15) < 4 ? (kvh * 1024 + br * 512 + b * 64 + qd * 16 + (lane & 3) * 4 + (r & 1) * 2 + (r >> 1)) * 2 : OOB, 0, AUX_SC1);
    if (wg == 0 && grp == 1 && lane == 0) __builtin_amdgcn_raw_buffer_store_b64(v2u{tagA, tagB}, rw, 0, WS_EPOCH, AUX_SC1);
  }
  PROBE(13);
  { const u32 tgt = 10000u + 0u * pst; for (unsigned k = 0; k < (1u << 20); ++k) { if ((u32)__builtin_amdgcn_s_memrealtime() - pt0 >= tgt) break; __builtin_amdgcn_s_sleep(1); } }
}

extern "C" void kernel_launch(void* const* d_in, const int* in_sizes, int n_in, void* d_out, int out_size, void* d_ws, size_t ws_size,
                              hipStream_t stream) {
  Params p;
  p.x[0] = d_in[0]; p.k[0] = d_in[1]; p.v[0] = d_in[2]; p.wq[0] = d_in[3]; p.wo[0] = d_in[4];
  p.x[1] = d_in[5]; p.k[1] = d_in[6]; p.v[1] = d_in[7]; p.wq[1] = d_in[8]; p.wo[1] = d_in[9];
  p.out = d_out;
  p.ws = (unsigned char*)d_ws;
  k_fused<<<NWG, NTHR, 0, stream>>>(p);
}
```

```cpp
#include <hip/hip_runtime.h>
#include <hip/hip_bf16.h>
#include <stdint.h>
#include <utility>

typedef unsigned u32;
typedef u32 v4u __attribute__((ext_vector_type(4)));
typedef u32 v2u __attribute__((ext_vector_type(2)));
typedef float f32x4 __attribute__((ext_vector_type(4)));
typedef float f32x2 __attribute__((ext_vector_type(2)));
typedef __bf16 bf16x8 __attribute__((ext_vector_type(8)));
typedef __bf16 bf16x2 __attribute__((ext_vector_type(2)));
typedef short v4s __attribute__((ext_vector_type(4)));
#define LAS __attribute__((address_space(3)))

#define NFLY 5
#define NWIDE 6
#define NHEAD 3
#define NSLOW 12
#define TAU 218
#define TBASE 400
#define Q_WAIT 32
#define FEW 4
#define POLL_SLEEP 4
#define TOUCH 1
#define CODEW 24
#define HSLEEP true
#define H1_WAIT 6
#define H2_WAIT 16
#define H2_SLEEP 2

constexpr int NB = 8, DM = 512, HQ = 32, HKV = 8, SK = 8192, DH = 128;
constexpr int NWG = 256, NTHR = 512;
constexpr int RING = 16, TPI = RING / 4, NIT = 32 / TPI;
constexpr int KV_BYTES = NB * HKV * SK * DH * 2;
constexpr int W_BYTES = DM * HQ * DH * 2;
constexpr float CSC = 0.08838834764831845f * 1.4426950408889634f;
constexpr int AUX_NT = 2, AUX_SC1 = 16;
constexpr unsigned SPIN_CAP = 1u << 17, LSPIN_CAP = 1u << 22;
constexpr int OOB = 0x40000000;

constexpr int WS_EPOCH = 0;
constexpr int WS_QG = 4096;
constexpr int PG_HEAD = 512, PG_WG = 4 * PG_HEAD;
constexpr int WS_PG = WS_QG + 2 * 8 * 8 * 8192;
constexpr int WS_ML = WS_PG + 2 * NWG * PG_WG;
constexpr int WS_OG = WS_ML + 2 * 8 * 4 * 512;
constexpr int WS_END = WS_OG + 2 * NWG * 4096;

constexpr int MRG_REC = 132;
constexpr int L_W = 0;
constexpr int L_RED = 65536;
constexpr int L_MRG = L_RED + 16384;
constexpr int L_Q = L_MRG + 2 * 16 * MRG_REC * 4;
constexpr int L_SC = L_Q + 2048;
constexpr int L_O = L_SC + 32768;
constexpr int L_FLAG = L_O + 2048;
constexpr int L_END = L_FLAG + 128 + 16;
enum { F_Q = 0, F_LAST = 1, F_REC = 2  , F_PROJ = 4, F_WO = 5, F_O = 6   };

struct Params {
  const void* x[2];
  const void* k[2];
  const void* v[2];
  const void* wq[2];
  const void* wo[2];
  void* out;
  unsigned char* ws;
};

__device__ __forceinline__ f32x4 mfma32(v4u a, v4u b, f32x4 c) {
  return __builtin_amdgcn_mfma_f32_16x16x32_bf16(__builtin_bit_cast(bf16x8, a), __builtin_bit_cast(bf16x8, b), c, 0, 0, 0);
}
__device__ __forceinline__ f32x4 mfma16(v2u a, v2u b, f32x4 c) {
  return __builtin_amdgcn_mfma_f32_16x16x16bf16_1k(__builtin_bit_cast(v4s, a), __builtin_bit_cast(v4s, b), c, 0, 0, 0);
}
__device__ __forceinline__ u32 pack_bf16(float lo, float hi) {
  f32x2 f = {lo, hi};
  return __builtin_bit_cast(u32, __builtin_convertvector(f, bf16x2));
}
__device__ __forceinline__ v2u tr_read(LAS unsigned char* p) {
  return __builtin_bit_cast(v2u, __builtin_amdgcn_ds_read_tr16_b64_v4i16((LAS v4s*)p));
}
__device__ __forceinline__ u32 f2u(float f) { return __builtin_bit_cast(u32, f); }
__device__ __forceinline__ float u2f(u32 u) { return __builtin_bit_cast(float, u); }
template <int CTRL> __device__ __forceinline__ float dpp(float x) {
  return u2f((u32)__builtin_amdgcn_mov_dpp((int)f2u(x), CTRL, 0xf, 0xf, true));
}
__device__ __forceinline__ v2u packh(float v0, float v1, u32 tag8) {
  const u32 a = f2u(v0) + 0x800u, c = f2u(v1) + 0x800u;
  return v2u{(a >> 12) | ((c << 8) & 0xFFF00000u), (c >> 24) | tag8};
}
__device__ __forceinline__ f32x4 unpack4(v4u g) {
  return f32x4{u2f(g.x << 12), u2f(((g.x >> 20) << 12) | (g.y << 24)), u2f(g.z << 12), u2f(((g.z >> 20) << 12) | (g.w << 24))};
}
__device__ __forceinline__ float red32(float a, float b) {
  const auto s = __builtin_amdgcn_permlane32_swap(f2u(a), f2u(b), false, false);
  return u2f(s[0]) + u2f(s[1]);
}
__device__ __forceinline__ float red16(float a, float b) {
  const auto s = __builtin_amdgcn_permlane16_swap(f2u(a), f2u(b), false, false);
  return u2f(s[0]) + u2f(s[1]);
}
__device__ __forceinline__ float rows_max(float x) {
  const auto s = __builtin_amdgcn_permlane16_swap(f2u(x), f2u(x), false, false);
  x = fmaxf(u2f(s[0]), u2f(s[1]));
  const auto t = __builtin_amdgcn_permlane32_swap(f2u(x), f2u(x), false, false);
  return fmaxf(u2f(t[0]), u2f(t[1]));
}
__device__ __forceinline__ float rows_sum(float x) { x = red16(x, x); return red32(x, x); }
#define MEM_FENCE() asm volatile("" ::: "memory")
#define SB() __builtin_amdgcn_sched_barrier(0)
#define WAITVM(n) do { asm volatile("s_waitcnt vmcnt(%0)" :: "i"(n) : "memory"); SB(); } while (0)
#define WAITVM2(x, y, n, m) do { asm volatile("s_cmp_gt_u32 %0, %1\n\ts_cbranch_scc1 1f\n\ts_waitcnt vmcnt(%2)\n1:\n\ts_waitcnt vmcnt(%3)" :: "s"(x), "s"(y), "i"(n), "i"(m) : "memory", "scc"); SB(); } while (0)
#define AI __attribute__((always_inline))
__device__ __forceinline__ bool all_lanes(bool c) { return __builtin_amdgcn_ballot_w64(c) == ~0ull; }
template <int... I, class F> __device__ __forceinline__ void static_for_impl(std::integer_sequence<int, I...>, F&& f) {
  (f(std::integral_constant<int, I>{}), ...);
}
template <int N, class F> __device__ __forceinline__ void static_for(F&& f) { static_for_impl(std::make_integer_sequence<int, N>{}, f); }

__device__ __forceinline__ void lds_post(LAS unsigned char* L, int slot, int w) {
  MEM_FENCE();
  *(volatile LAS u32*)(L + L_FLAG + slot * 16 + w * 4) = 1u;
  MEM_FENCE();
}
__device__ __forceinline__ void lds_wait(LAS unsigned char* L, int slot, bool sleep) {
  for (unsigned spins = 0; spins < LSPIN_CAP; ++spins) {
    MEM_FENCE();
    const v4u v = *(volatile LAS v4u*)(L + L_FLAG + slot * 16);
    if (__builtin_amdgcn_readfirstlane(v.x & v.y & v.z & v.w) != 0u) break;
    if (sleep) __builtin_amdgcn_s_sleep(1);
  }
  MEM_FENCE();
}

__global__ void __launch_bounds__(NTHR, 2) k_fused(Params p) {
  asm volatile("" :: "s"(p.x[0]), "s"(p.x[1]), "s"(p.k[0]), "s"(p.k[1]), "s"(p.v[0]), "s"(p.v[1]), "s"(p.wq[0]), "s"(p.wq[1]), "s"(p.wo[0]), "s"(p.wo[1]),
               "s"(p.out), "s"(p.ws));
  __shared__ __attribute__((aligned(16))) unsigned char smem[L_END];
  LAS unsigned char* L = (LAS unsigned char*)smem;
  const int lane = threadIdx.x & 63, w8 = __builtin_amdgcn_readfirstlane(threadIdx.x >> 6), w = w8 & 3;
  const int grp = w8 >> 2;
  const int wg = blockIdx.x, qd = wg & 3, ub = wg >> 2, b = ub >> 3, kvh = ub & 7;
  const int ks = (b & 1) * 4 + qd;
  const __amdgpu_buffer_rsrc_t rw = __builtin_amdgcn_make_buffer_rsrc((void*)p.ws, 0, WS_END, 0x00020000);
  u32 tagA, tagB; const u32 pt0 = (u32)__builtin_amdgcn_s_memrealtime(); u32 pst = 0;

  if (grp == 0) {
    const u32 t0 = (u32)__builtin_amdgcn_s_memrealtime();
    const int voffK = ((lane >> 2) & 3) * 256 + (lane >> 4) * 64 + (lane & 3) * 16;
    const int voffV = lane * 16;
    const int sbase = wg * 524288 + w * 4096;
    v4u ring[RING], pre[RING];
    const __amdgpu_buffer_rsrc_t rk0 = __builtin_amdgcn_make_buffer_rsrc((void*)p.k[0], 0, KV_BYTES, 0x00020000);
    static_for<2 * RING>([&](auto I) AI {
      constexpr int i = decltype(I)::value;
      if constexpr (i == NHEAD) __builtin_amdgcn_s_barrier();
      if constexpr (i >= NHEAD && i < NSLOW) WAITVM(NHEAD - 1);
      if constexpr (i >= NSLOW) WAITVM(NFLY - 1);
      const v4u t = __builtin_amdgcn_raw_buffer_load_b128(rk0, voffK + (i & 3) * 1024, sbase + (i >> 2) * 16384, AUX_NT);
      if constexpr (i < RING) pre[i] = t; else ring[i - RING] = t;
      SB();
    });
    const int jq = lane & 3;
    const u32 m0 = jq == 0 ? 0x0000ffffu : (jq == 1 ? 0xffff0000u : 0u);
    const u32 m1 = jq == 2 ? 0x0000ffffu : (jq == 3 ? 0xffff0000u : 0u);
    LAS unsigned char* scl = L + L_SC + w * 8192 + (lane >> 2) * 16;
    u32 due = TBASE;
    lds_wait(L, F_Q, false);
    tagA = __builtin_amdgcn_readfirstlane(*(LAS u32*)(L + L_FLAG + 128));
    tagB = __builtin_amdgcn_readfirstlane(*(LAS u32*)(L + L_FLAG + 132));
    auto score = [&](v4u k, v4u q, float& mx) AI -> float {
      const f32x4 d = mfma32(k, q, f32x4{0.f, 0.f, 0.f, 0.f});
      float s = (dpp<0x00>(d[0]) + dpp<0x55>(d[1])) + (dpp<0xAA>(d[2]) + dpp<0xFF>(d[3]));
      mx = fmaxf(mx, s);
      asm volatile("" : "+v"(s), "+v"(mx));
      return s;
    };
    float mx0 = -INFINITY;
    {
      const v4u qf = *(LAS v4u*)(L + L_Q + voffK);
      const u32 now = (u32)__builtin_amdgcn_s_memrealtime() - t0;
      due += TAU;
      f32x4 st, sp;
      static_for<RING>([&](auto I) AI {
        constexpr int i = decltype(I)::value;
        st[i & 3] = score(ring[i], qf, mx0);
        SB();
        WAITVM2(now, due, NFLY - 1, NWIDE - 1);
        ring[i] = __builtin_amdgcn_raw_buffer_load_b128(rk0, voffK + (i & 3) * 1024, sbase + (2 * TPI + (i >> 2)) * 16384, AUX_NT);
        SB();
        sp[i & 3] = score(pre[i], qf, mx0);
        if constexpr ((i & 3) == 3) {
          if (jq == 0) {
            *(LAS f32x4*)(scl + (i >> 2) * 256) = sp;
            *(LAS f32x4*)(scl + (TPI + (i >> 2)) * 256) = st;
          }
        }
        SB();
      });
    }
#pragma unroll 1
    for (int br = 0; br < 2; ++br) {
      const v4u qf = *(LAS v4u*)(L + L_Q + br * 1024 + voffK);
      const void* pk = p.k[br];
      const void* pv = p.v[br];
      float mx = br == 0 ? mx0 : -INFINITY;
#pragma unroll 1
      for (int it = br == 0 ? 2 : 0; it < NIT; ++it) {
        const bool last = it == NIT - 1;
        const u32 now = (u32)__builtin_amdgcn_s_memrealtime() - t0;
        due += TAU;
        const __amdgpu_buffer_rsrc_t rn = __builtin_amdgcn_make_buffer_rsrc((void*)(last ? pv : pk), 0, KV_BYTES, 0x00020000);
        const int vn = last ? voffV : voffK;
        const int sn = sbase + (last ? 0 : (it + 1) * TPI * 16384);
        f32x4 st;
        static_for<RING>([&](auto I) AI {
          constexpr int i = decltype(I)::value;
          st[i & 3] = score(ring[i], qf, mx);
          if constexpr ((i & 3) == 3) { if (jq == 0) *(LAS f32x4*)(scl + (it * TPI + (i >> 2)) * 256) = st; }
          SB();
          WAITVM2(now, due, NFLY - 1, NWIDE - 1);
          ring[i] = __builtin_amdgcn_raw_buffer_load_b128(rn, vn + (i & 3) * 1024, sn + (i >> 2) * 16384, AUX_NT);
          SB();
        });
      }
      const float mc = rows_max(mx) * CSC;
      float lsum = 0.f;
      f32x4 accLo = {0.f, 0.f, 0.f, 0.f}, accHi = {0.f, 0.f, 0.f, 0.f};
#pragma unroll 1
      for (int it = 0; it < NIT; ++it) {
        const bool last = it == NIT - 1;
        const u32 now = (u32)__builtin_amdgcn_s_memrealtime() - t0;
        due += TAU;
        const __amdgpu_buffer_rsrc_t rn =
            __builtin_amdgcn_make_buffer_rsrc((void*)(last ? p.k[1] : pv), 0, (last && br == 1) ? 0 : KV_BYTES, 0x00020000);
        const int vn = last ? voffK : voffV;
        const int sn = sbase + (last ? 0 : (it + 1) * TPI * 16384);
        if (last && br == 1) lds_post(L, F_LAST, w);
        f32x4 sv[TPI];
#pragma unroll
        for (int k = 0; k < TPI; ++k) sv[k] = *(LAS f32x4*)(scl + (it * TPI + k) * 256);
        static_for<RING>([&](auto I) AI {
          constexpr int i = decltype(I)::value;
          const float pe = __builtin_amdgcn_exp2f(fmaf(sv[i >> 2][i & 3], CSC, -mc));
          lsum += pe;
          asm volatile("" : "+v"(lsum));
          const u32 pkd = pack_bf16(pe, pe);
          const v2u a = {pkd & m0, pkd & m1};
          accLo = mfma16(a, v2u{ring[i].x, ring[i].y}, accLo);
          accHi = mfma16(a, v2u{ring[i].z, ring[i].w}, accHi);
          SB();
          WAITVM2(now, due, NFLY - 1, NWIDE - 1);
          ring[i] = __builtin_amdgcn_raw_buffer_load_b128(rn, vn + (i & 3) * 1024, sn + (i >> 2) * 16384, AUX_NT);
          SB();
        });
      }
      lsum = rows_sum(lsum);
      LAS float* mrg = (LAS float*)(L + L_MRG) + br * 16 * MRG_REC;
      LAS float* rec = mrg + (w * 4 + (lane >> 4)) * MRG_REC + 8 * (lane & 15);
      *(LAS f32x4*)rec = accLo;
      *(LAS f32x4*)(rec + 4) = accHi;
      if (lane < 16 && jq == 0) *(LAS f32x2*)(mrg + (w * 4 + (lane >> 2)) * MRG_REC + 128) = f32x2{mc, lsum};
      lds_post(L, F_REC + br, w);
    }
  } else {
    const int tid = w * 64 + lane;
    const v2u ep = __builtin_amdgcn_raw_buffer_load_b64(rw, 0, WS_EPOCH, AUX_SC1);
    v4u wr[2][4], xf[2];
#pragma unroll
    for (int br = 0; br < 2; ++br) {
      const __amdgpu_buffer_rsrc_t rq = __builtin_amdgcn_make_buffer_rsrc((void*)p.wq[br], 0, W_BYTES, 0x00020000);
      const __amdgpu_buffer_rsrc_t rx = __builtin_amdgcn_make_buffer_rsrc((void*)p.x[br], 0, NB * DM * 2, 0x00020000);
      const int so = (128 * qd + 32 * w) * (HQ * DH * 2) + (kvh * 512 + b * 64) * 2;
#pragma unroll
      for (int i = 0; i < 4; ++i)
        wr[br][i] = __builtin_amdgcn_raw_buffer_load_b128(rq, (lane >> 3) * (HQ * DH * 2) + (lane & 7) * 16, so + i * 8 * (HQ * DH * 2), AUX_NT);
      xf[br] = __builtin_amdgcn_raw_buffer_load_b128(rx, (lane & 7) * (DM * 2) + (lane >> 4) * 16, (128 * qd + 32 * w) * 2, 0);
      SB();
    }
    if (w == 0 && lane < 32) ((LAS u32*)(L + L_FLAG))[lane] = 0u;
    asm volatile("s_waitcnt lgkmcnt(0)" ::: "memory");
    __builtin_amdgcn_s_barrier();
    tagA = __builtin_amdgcn_readfirstlane(ep.x) + 1u;
    tagB = __builtin_amdgcn_readfirstlane(ep.y) + 0x9E3779B9u;
    if (lane == 0) *(LAS v2u*)(L + L_FLAG + 128) = v2u{tagA, tagB};
#pragma unroll
    for (int br = 0; br < 2; ++br) {
      LAS unsigned char* wl = L + L_W + br * 16384 + w * 4096;
#pragma unroll
      for (int i = 0; i < 4; ++i) *(LAS v4u*)(wl + i * 1024 + lane * 16) = wr[br][i];
      LAS unsigned char* rb = wl + (8 * (lane >> 4) + ((lane >> 2) & 3)) * 128 + (lane & 3) * 8;
      LAS f32x4* red = (LAS f32x4*)(L + L_RED + br * 8192);
#pragma unroll
      for (int ct = 0; ct < 4; ++ct) {
        const v2u lo = tr_read(rb + ct * 32), hi = tr_read(rb + ct * 32 + 512);
        const f32x4 d = mfma32(xf[br], v4u{lo.x, lo.y, hi.x, hi.y}, f32x4{0.f, 0.f, 0.f, 0.f});
        if (lane < 32) red[(w * 4 + ct) * 32 + lane] = d;
      }
    }
    lds_post(L, F_PROJ, w);
    lds_wait(L, F_PROJ, false);
    {
      const int pb = 4 * ((tid >> 3) & 1) + ((tid >> 4) & 3), pn = 16 * w + 2 * (tid & 7);
      const u32 tag8 = ((tid & 1) ? tagB : tagA) << 8;
#pragma unroll
      for (int br = 0; br < 2; ++br) {
        LAS float* rf = (LAS float*)(L + L_RED + br * 8192) + (w * 32 + 16 * ((tid >> 3) & 1) + 2 * (tid & 7)) * 4 + ((tid >> 4) & 3);
        float s0 = 0.f, s1 = 0.f;
#pragma unroll
        for (int ww = 0; ww < 4; ++ww) { s0 += rf[ww * 512]; s1 += rf[ww * 512 + 4]; }
        __builtin_amdgcn_raw_buffer_store_b64(packh(s0, s1, tag8), rw, WS_QG + (br * 8 + kvh) * 65536 + pb * 8192 + qd * 2048 + b * 256 + (pn >> 1) * 8, 0, AUX_SC1);
      }
    }
    SB();
    __builtin_amdgcn_s_sleep(Q_WAIT);
    SB();
    {
      const int qvo = WS_QG + (kvh * 8 + b) * 8192 + w * 512 + (lane >> 5) * 2048 + (lane & 31) * 16;
      const u32 ta = tagA << 8, tb = tagB << 8;
      v4u g[2][2];
      for (unsigned spins = 0; spins < SPIN_CAP; ++spins) {
        MEM_FENCE();
        u32 bad = 0u;
#pragma unroll
        for (int br = 0; br < 2; ++br)
#pragma unroll
          for (int i = 0; i < 2; ++i) g[br][i] = __builtin_amdgcn_raw_buffer_load_b128(rw, qvo, br * 8 * 65536 + i * 4096, AUX_SC1);
#pragma unroll
        for (int br = 0; br < 2; ++br)
#pragma unroll
          for (int i = 0; i < 2; ++i) bad |= (g[br][i].y ^ ta) | (g[br][i].w ^ tb);
        if (all_lanes((bad >> 8) == 0u)) break;
        __builtin_amdgcn_s_sleep(4);
      }
#pragma unroll
      for (int br = 0; br < 2; ++br) {
        const f32x4 s = unpack4(g[br][0]) + unpack4(g[br][1]);
        const float u0 = red32(s[0], s[2]), u1 = red32(s[1], s[3]);
        *(LAS u32*)(L + L_Q + br * 1024 + w * 256 + (lane & 31) * 8 + (lane >> 5) * 4) = pack_bf16(u0, u1);
      }
      lds_post(L, F_Q, w);
    }
  }

  const int br = 1 - grp;
  const u32 ta = tagA << 8, tb = tagB << 8;
  auto bad4 = [=](const v4u& g) AI -> u32 { return (g.y ^ tagA) | (g.w ^ tagB); };
  auto badp = [=](const v4u& g) AI -> u32 { return ((g.y ^ ta) | (g.w ^ tb)) >> 8; };
  int a_po = WS_PG + (br * NWG + wg) * PG_WG + w * PG_HEAD + lane * 8;
  int a_pm = lane == 0 ? WS_ML + ((br * 8 + kvh) * 4 + w) * 512 + (b * 4 + qd) * 16 : OOB;
  int a_ml = WS_ML + ((br * 8 + kvh) * 4 + (ks >> 1)) * 512 + (w * 8 + (lane >> 4)) * 16;
  int a_o = WS_PG + (br * NWG + (2 * w * 8 + kvh) * 4 + (lane >> 4)) * PG_WG + (ks >> 1) * PG_HEAD + (ks & 1) * 256 + (lane & 15) * 16;
  const bool odd = lane & 1;
  int a_og = lane < 32 ? WS_OG + br * NWG * 4096 + (((2 * (b >> 1) + (w >> 1)) * 8 + 4 * (lane >> 4) + (odd ? 2 : 0)) * 4 + 2 * (w & 1)) * 4096 +
                             (kvh * 8 + ks) * 64 + ((lane & 15) >> 1) * 8
                       : OOB;
  u32 t_po = (lane & 1) ? tb : ta, t_og = (lane & 2) ? tb : ta;
  asm volatile("" : "+v"(a_po), "+v"(a_pm), "+v"(a_ml), "+v"(a_o), "+v"(a_og), "+v"(t_po), "+v"(t_og));

  lds_wait(L, F_REC + br, grp != 0);
  {
    LAS float* mrg = (LAS float*)(L + L_MRG) + br * 16 * MRG_REC;
    f32x2 ml4[4], o4[4];
#pragma unroll
    for (int ww = 0; ww < 4; ++ww) {
      ml4[ww] = *(LAS f32x2*)(mrg + (ww * 4 + w) * MRG_REC + 128);
      o4[ww] = *(LAS f32x2*)(mrg + (ww * 4 + w) * MRG_REC + 2 * lane);
    }
    float M = fmaxf(fmaxf(ml4[0].x, ml4[1].x), fmaxf(ml4[2].x, ml4[3].x));
    float Ls = 0.f, o0 = 0.f, o1 = 0.f;
#pragma unroll
    for (int ww = 0; ww < 4; ++ww) {
      const float f = __builtin_amdgcn_exp2f(ml4[ww].x - M);
      Ls = fmaf(f, ml4[ww].y, Ls);
      o0 = fmaf(f, o4[ww].x, o0);
      o1 = fmaf(f, o4[ww].y, o1);
    }
    v2u ph = packh(o0, o1, t_po);
    v4u wo4[2][4];
    u32 tw0 = 0u, tw1 = 0u, tw2 = 0u;
    if (grp == 1) {
      asm volatile("" : "+v"(ph), "+v"(M), "+v"(Ls));
      lds_wait(L, F_LAST, true);
#pragma unroll
      for (int bq = 0; bq < 2; ++bq) {
        const __amdgpu_buffer_rsrc_t ro = __builtin_amdgcn_make_buffer_rsrc((void*)p.wo[bq], 0, W_BYTES, 0x00020000);
        const int so = (kvh * 512 + 64 * ks + 16 * w) * (DM * 2) + (b >> 1) * 256;
#pragma unroll
        for (int i = 0; i < 4; ++i)
          wo4[bq][i] = __builtin_amdgcn_raw_buffer_load_b128(ro, (lane >> 4) * (DM * 2) + (lane & 15) * 16, so + i * 4 * (DM * 2), AUX_NT);
      }
      if (TOUCH) {
        const __amdgpu_buffer_rsrc_t rout = __builtin_amdgcn_make_buffer_rsrc(p.out, 0, NB * 2 * DM * 2, 0x00020000);
        tw0 = __builtin_amdgcn_raw_buffer_load_b32(rw, (w == 0 && lane < 8) ? WS_PG + lane * 524288 : OOB, 0, AUX_NT);
        tw1 = __builtin_amdgcn_raw_buffer_load_b32(rout, (w == 0 && lane == 0) ? 0 : OOB, 0, AUX_NT);
      }
      if (CODEW) {
        unsigned long long pc;
        asm volatile("s_getpc_b64 %0" : "=s"(pc));
        const __amdgpu_buffer_rsrc_t rc = __builtin_amdgcn_make_buffer_rsrc((void*)(pc & ~127ull), 0, w == 1 ? CODEW * 128 : 0, 0x00020000);
        tw2 = __builtin_amdgcn_raw_buffer_load_b32(rc, lane * 128, 0, 0);
      }
      SB();
      lds_wait(L, F_REC + 1, HSLEEP);
    }
    __builtin_amdgcn_raw_buffer_store_b64(ph, rw, a_po, 0, AUX_SC1);
    __builtin_amdgcn_raw_buffer_store_b128(v4u{f2u(M), tagA, f2u(Ls), tagB}, rw, a_pm, 0, AUX_SC1);
    if (grp == 1) {
#pragma unroll
      for (int bq = 0; bq < 2; ++bq)
#pragma unroll
        for (int i = 0; i < 4; ++i) *(LAS v4u*)(L + L_W + (2 + bq) * 16384 + w * 4096 + i * 1024 + lane * 16) = wo4[bq][i];
      lds_post(L, F_WO, w);
      asm volatile("" :: "v"(tw0), "v"(tw1), "v"(tw2));
    }
    lds_wait(L, F_WO, false);
  }
  v4u bf[2][2];
  {
    LAS unsigned char* rb = L + L_W + (2 + br) * 16384 + (8 * (lane >> 4) + ((lane >> 2) & 3)) * 256 + w * 64 + (lane & 3) * 8;
#pragma unroll
    for (int s = 0; s < 2; ++s)
#pragma unroll
      for (int c = 0; c < 2; ++c) {
        const v2u lo = tr_read(rb + s * 8192 + c * 32), hi = tr_read(rb + s * 8192 + c * 32 + 1024);
        bf[s][c] = v4u{lo.x, lo.y, hi.x, hi.y};
      }
  }
  v4u ml[2], gr[2];
  {
#pragma unroll
    for (int j = 0; j < 2; ++j) { ml[j] = v4u{0u, ~tagA, 0u, 0u}; gr[j] = v4u{0u, ~ta, 0u, 0u}; }
    bool few = false;
    if (H1_WAIT) __builtin_amdgcn_s_sleep(H1_WAIT);
    for (unsigned spins = 0; spins < SPIN_CAP; ++spins) {
      MEM_FENCE();
      v4u tm[2], t[2];
      bool wm[2], want[2];
#pragma unroll
      for (int j = 0; j < 2; ++j) {
        wm[j] = bad4(ml[j]) != 0u;
        want[j] = badp(gr[j]) != 0u && (few || !wm[j]);
        tm[j] = __builtin_amdgcn_raw_buffer_load_b128(rw, wm[j] ? a_ml + j * 64 : OOB, 0, AUX_SC1);
        t[j] = __builtin_amdgcn_raw_buffer_load_b128(rw, want[j] ? a_o + j * 32 * PG_WG : OOB, 0, AUX_SC1);
      }
      SB();
      u32 bad = 0u;
      int miss = 0;
#pragma unroll
      for (int j = 0; j < 2; ++j) {
        ml[j] = wm[j] ? tm[j] : ml[j];
        gr[j] = want[j] ? t[j] : gr[j];
        const u32 bm = bad4(ml[j]);
        miss += __builtin_popcountll(__builtin_amdgcn_ballot_w64(bm != 0u));
        bad |= bm | badp(gr[j]);
      }
      if (all_lanes(bad == 0u)) break;
      few = miss <= 16 * FEW;
      if (!few) __builtin_amdgcn_s_sleep(POLL_SLEEP);
    }
  }
  {
    LAS unsigned char* ol = L + L_O + br * 1024;
    const int r = lane >> 4;
#pragma unroll
    for (int j = 0; j < 2; ++j) {
      const float m = u2f(ml[j].x);
      const float e = __builtin_amdgcn_exp2f(m - rows_max(m));
      const float fw = e * __builtin_amdgcn_rcpf(rows_sum(e * u2f(ml[j].z)));
      const f32x4 a = unpack4(gr[j]) * fw;
      const float o = red16(red32(a[0], a[1]), red32(a[2], a[3]));
      *(LAS unsigned short*)(ol + (2 * w + j) * 128 + ((lane & 15) * 4 + ((r & 1) * 2 + (r >> 1))) * 2) = (unsigned short)(pack_bf16(o, o) & 0xffffu);
    }
    lds_post(L, F_O + br, w);
    lds_wait(L, F_O + br, false);
    const v4u af0 = *(LAS v4u*)(ol + (lane & 7) * 128 + (lane >> 4) * 16), af1 = *(LAS v4u*)(ol + (lane & 7) * 128 + 64 + (lane >> 4) * 16);
#pragma unroll
    for (int c = 0; c < 2; ++c) {
      f32x4 d = mfma32(af0, bf[0][c], f32x4{0.f, 0.f, 0.f, 0.f});
      d = mfma32(af1, bf[1][c], d);
      const float e0 = dpp<0xB1>(d[0]), e1 = dpp<0xB1>(d[1]), e2 = dpp<0xB1>(d[2]), e3 = dpp<0xB1>(d[3]);
      __builtin_amdgcn_raw_buffer_store_b64(packh(odd ? e2 : d[0], odd ? d[2] : e0, t_og), rw, a_og + c * 4096, 0, AUX_SC1);
      __builtin_amdgcn_raw_buffer_store_b64(packh(odd ? e3 : d[1], odd ? d[3] : e1, t_og), rw, a_og + c * 4096 + 4 * 4096, 0, AUX_SC1);
    }
  }
  if (w == 0) {
    v4u a[4];
#pragma unroll
    for (int i = 0; i < 4; ++i) a[i] = v4u{0u, ~ta, 0u, 0u};
    const int vo = WS_OG + (br * NWG + wg) * 4096 + lane * 16;
    __builtin_amdgcn_s_sleep(H2_WAIT);
    for (unsigned spins = 0; spins < SPIN_CAP; ++spins) {
      MEM_FENCE();
      v4u t[4];
#pragma unroll
      for (int i = 0; i < 4; ++i) t[i] = __builtin_amdgcn_raw_buffer_load_b128(rw, badp(a[i]) != 0u ? vo + i * 1024 : OOB, 0, AUX_SC1);
      SB();
      u32 bad = 0u;
#pragma unroll
      for (int i = 0; i < 4; ++i) { a[i] = badp(a[i]) != 0u ? t[i] : a[i]; bad |= badp(a[i]); }
      if (all_lanes(bad == 0u)) break;
      __builtin_amdgcn_s_sleep(H2_SLEEP);
    }
    f32x4 s = (unpack4(a[0]) + unpack4(a[1])) + (unpack4(a[2]) + unpack4(a[3]));
#pragma unroll
    for (int j = 0; j < 4; ++j) { s[j] += dpp<0x124>(s[j]); s[j] += dpp<0x128>(s[j]); }
    const float o = red16(red32(s[0], s[1]), red32(s[2], s[3]));
    const int r = lane >> 4;
    const __amdgpu_buffer_rsrc_t rout = __builtin_amdgcn_make_buffer_rsrc(p.out, 0, NB * 2 * DM * 2, 0x00020000);
    __builtin_amdgcn_raw_buffer_store_b16((unsigned short)(pack_bf16(o, o) & 0xffffu), rout,
                                          (lane & 15) < 4 ? (kvh * 1024 + br * 512 + b * 64 + qd * 16 + (lane & 3) * 4 + (r & 1) * 2 + (r >> 1)) * 2 : OOB, 0, AUX_SC1);
    if (wg == 0 && grp == 1 && lane == 0) __builtin_amdgcn_raw_buffer_store_b64(v2u{tagA, tagB}, rw, 0, WS_EPOCH, AUX_SC1);
  }
  pst = (u32)__builtin_amdgcn_s_memrealtime() - pt0;
  { const u32 tgt = 9000u + 4u * (pst - 7000u); for (unsigned k = 0; k < (1u << 20); ++k) { if ((u32)__builtin_amdgcn_s_memrealtime() - pt0 >= tgt) break; __builtin_amdgcn_s_sleep(1); } }
}

extern "C" void kernel_launch(void* const* d_in, const int* in_sizes, int n_in, void* d_out, int out_size, void* d_ws, size_t ws_size,
                              hipStream_t stream) {
  Params p;
  p.x[0] = d_in[0]; p.k[0] = d_in[1]; p.v[0] = d_in[2]; p.wq[0] = d_in[3]; p.wo[0] = d_in[4];
  p.x[1] = d_in[5]; p.k[1] = d_in[6]; p.v[1] = d_in[7]; p.wq[1] = d_in[8]; p.wo[1] = d_in[9];
  p.out = d_out;
  p.ws = (unsigned char*)d_ws;
  k_fused<<<NWG, NTHR, 0, stream>>>(p);
}
```

```cpp
#include <hip/hip_runtime.h>
#include <hip/hip_bf16.h>
#include <stdint.h>
#include <utility>

typedef unsigned u32;
typedef u32 v4u __attribute__((ext_vector_type(4)));
typedef u32 v2u __attribute__((ext_vector_type(2)));
typedef float f32x4 __attribute__((ext_vector_type(4)));
typedef float f32x2 __attribute__((ext_vector_type(2)));
typedef __bf16 bf16x8 __attribute__((ext_vector_type(8)));
typedef __bf16 bf16x2 __attribute__((ext_vector_type(2)));
typedef short v4s __attribute__((ext_vector_type(4)));
#define LAS __attribute__((address_space(3)))

#define NFLY 5
#define NWIDE 6
#define NHEAD 3
#define NSLOW 12
#define TAU 217
#define TBASE 400
#define LEAK 4
#define Q_WAIT 32
#define FEW 4
#define POLL_SLEEP 4
#define H1_WAIT 6
#define H2_WAIT 16
#define H2_SLEEP 2

constexpr int NB = 8, DM = 512, HQ = 32, HKV = 8, SK = 8192, DH = 128;
constexpr int NWG = 256, NTHR = 512;
constexpr int RING = 16, TPI = RING / 4, NIT = 32 / TPI;
constexpr int KV_BYTES = NB * HKV * SK * DH * 2;
constexpr int W_BYTES = DM * HQ * DH * 2;
constexpr float CSC = 0.08838834764831845f * 1.4426950408889634f;
constexpr int AUX_NT = 2, AUX_SC1 = 16;
constexpr unsigned SPIN_CAP = 1u << 17, LSPIN_CAP = 1u << 22;
constexpr int OOB = 0x40000000;

constexpr int WS_EPOCH = 0;
constexpr int WS_QG = 4096;
constexpr int PG_HEAD = 512, PG_WG = 4 * PG_HEAD;
constexpr int WS_PG = WS_QG + 2 * 8 * 8 * 8192;
constexpr int WS_ML = WS_PG + 2 * NWG * PG_WG;
constexpr int WS_OG = WS_ML + 2 * 8 * 4 * 512;
constexpr int WS_END = WS_OG + 2 * NWG * 4096;

constexpr int MRG_REC = 132;
constexpr int L_W = 0;
constexpr int L_RED = 65536;
constexpr int L_MRG = L_RED + 16384;
constexpr int L_Q = L_MRG + 2 * 16 * MRG_REC * 4;
constexpr int L_SC = L_Q + 2048;
constexpr int L_O = L_SC + 32768;
constexpr int L_FLAG = L_O + 2048;
constexpr int L_END = L_FLAG + 128 + 16;
enum { F_Q = 0, F_LAST = 1, F_REC = 2  , F_PROJ = 4, F_WO = 5, F_O = 6   };

struct Params {
  const void* x[2];
  const void* k[2];
  const void* v[2];
  const void* wq[2];
  const void* wo[2];
  void* out;
  unsigned char* ws;
};

__device__ __forceinline__ f32x4 mfma32(v4u a, v4u b, f32x4 c) {
  return __builtin_amdgcn_mfma_f32_16x16x32_bf16(__builtin_bit_cast(bf16x8, a), __builtin_bit_cast(bf16x8, b), c, 0, 0, 0);
}
__device__ __forceinline__ f32x4 mfma16(v2u a, v2u b, f32x4 c) {
  return __builtin_amdgcn_mfma_f32_16x16x16bf16_1k(__builtin_bit_cast(v4s, a), __builtin_bit_cast(v4s, b), c, 0, 0, 0);
}
__device__ __forceinline__ u32 pack_bf16(float lo, float hi) {
  f32x2 f = {lo, hi};
  return __builtin_bit_cast(u32, __builtin_convertvector(f, bf16x2));
}
__device__ __forceinline__ v2u tr_read(LAS unsigned char* p) {
  return __builtin_bit_cast(v2u, __builtin_amdgcn_ds_read_tr16_b64_v4i16((LAS v4s*)p));
}
__device__ __forceinline__ u32 f2u(float f) { return __builtin_bit_cast(u32, f); }
__device__ __forceinline__ float u2f(u32 u) { return __builtin_bit_cast(float, u); }
template <int CTRL> __device__ __forceinline__ float dpp(float x) {
  return u2f((u32)__builtin_amdgcn_mov_dpp((int)f2u(x), CTRL, 0xf, 0xf, true));
}
__device__ __forceinline__ v2u packh(float v0, float v1, u32 tag8) {
  const u32 a = f2u(v0) + 0x800u, c = f2u(v1) + 0x800u;
  return v2u{(a >> 12) | ((c << 8) & 0xFFF00000u), (c >> 24) | tag8};
}
__device__ __forceinline__ f32x4 unpack4(v4u g) {
  return f32x4{u2f(g.x << 12), u2f(((g.x >> 20) << 12) | (g.y << 24)), u2f(g.z << 12), u2f(((g.z >> 20) << 12) | (g.w << 24))};
}
__device__ __forceinline__ float red32(float a, float b) {
  const auto s = __builtin_amdgcn_permlane32_swap(f2u(a), f2u(b), false, false);
  return u2f(s[0]) + u2f(s[1]);
}
__device__ __forceinline__ float red16(float a, float b) {
  const auto s = __builtin_amdgcn_permlane16_swap(f2u(a), f2u(b), false, false);
  return u2f(s[0]) + u2f(s[1]);
}
__device__ __forceinline__ float rows_max(float x) {
  const auto s = __builtin_amdgcn_permlane16_swap(f2u(x), f2u(x), false, false);
  x = fmaxf(u2f(s[0]), u2f(s[1]));
  const auto t = __builtin_amdgcn_permlane32_swap(f2u(x), f2u(x), false, false);
  return fmaxf(u2f(t[0]), u2f(t[1]));
}
__device__ __forceinline__ float rows_sum(float x) { x = red16(x, x); return red32(x, x); }
#define MEM_FENCE() asm volatile("" ::: "memory")
#define SB() __builtin_amdgcn_sched_barrier(0)
#define WAITVM(n) do { asm volatile("s_waitcnt vmcnt(%0)" :: "i"(n) : "memory"); SB(); } while (0)
#define WAITVM2(x, y, n, m) do { asm volatile("s_cmp_gt_u32 %0, %1\n\ts_cbranch_scc1 1f\n\ts_waitcnt vmcnt(%2)\n1:\n\ts_waitcnt vmcnt(%3)" :: "s"(x), "s"(y), "i"(n), "i"(m) : "memory", "scc"); SB(); } while (0)
#define AI __attribute__((always_inline))
__device__ __forceinline__ bool all_lanes(bool c) { return __builtin_amdgcn_ballot_w64(c) == ~0ull; }
template <int... I, class F> __device__ __forceinline__ void static_for_impl(std::integer_sequence<int, I...>, F&& f) {
  (f(std::integral_constant<int, I>{}), ...);
}
template <int N, class F> __device__ __forceinline__ void static_for(F&& f) { static_for_impl(std::make_integer_sequence<int, N>{}, f); }

__device__ __forceinline__ void lds_post(LAS unsigned char* L, int slot, int w) {
  MEM_FENCE();
  *(volatile LAS u32*)(L + L_FLAG + slot * 16 + w * 4) = 1u;
  MEM_FENCE();
}
__device__ __forceinline__ void lds_wait(LAS unsigned char* L, int slot, bool sleep) {
  for (unsigned spins = 0; spins < LSPIN_CAP; ++spins) {
    MEM_FENCE();
    const v4u v = *(volatile LAS v4u*)(L + L_FLAG + slot * 16);
    if (__builtin_amdgcn_readfirstlane(v.x & v.y & v.z & v.w) != 0u) break;
    if (sleep) __builtin_amdgcn_s_sleep(1);
  }
  MEM_FENCE();
}

__global__ void __launch_bounds__(NTHR, 2) k_fused(Params p) {
  asm volatile("" :: "s"(p.x[0]), "s"(p.x[1]), "s"(p.k[0]), "s"(p.k[1]), "s"(p.v[0]), "s"(p.v[1]), "s"(p.wq[0]), "s"(p.wq[1]), "s"(p.wo[0]), "s"(p.wo[1]),
               "s"(p.out), "s"(p.ws));
  __shared__ __attribute__((aligned(16))) unsigned char smem[L_END];
  LAS unsigned char* L = (LAS unsigned char*)smem;
  const int lane = threadIdx.x & 63, w8 = __builtin_amdgcn_readfirstlane(threadIdx.x >> 6), w = w8 & 3;
  const int grp = w8 >> 2;
  const int wg = blockIdx.x, qd = wg & 3, ub = wg >> 2, b = ub >> 3, kvh = ub & 7;
  const int ks = (b & 1) * 4 + qd;
  const __amdgpu_buffer_rsrc_t rw = __builtin_amdgcn_make_buffer_rsrc((void*)p.ws, 0, WS_END, 0x00020000);
  u32 tagA, tagB;

  if (grp == 0) {
    const u32 t0 = (u32)__builtin_amdgcn_s_memrealtime();
    const int voffK = ((lane >> 2) & 3) * 256 + (lane >> 4) * 64 + (lane & 3) * 16;
    const int voffV = lane * 16;
    const int sbase = wg * 524288 + w * 4096;
    v4u ring[RING], pre[RING];
    const __amdgpu_buffer_rsrc_t rk0 = __builtin_amdgcn_make_buffer_rsrc((void*)p.k[0], 0, KV_BYTES, 0x00020000);
    static_for<2 * RING>([&](auto I) AI {
      constexpr int i = decltype(I)::value;
      if constexpr (i == NHEAD) __builtin_amdgcn_s_barrier();
      if constexpr (i >= NHEAD && i < NSLOW) WAITVM(NHEAD - 1);
      if constexpr (i >= NSLOW) WAITVM(NFLY - 1);
      const v4u t = __builtin_amdgcn_raw_buffer_load_b128(rk0, voffK + (i & 3) * 1024, sbase + (i >> 2) * 16384, AUX_NT);
      if constexpr (i < RING) pre[i] = t; else ring[i - RING] = t;
      SB();
    });
    const int jq = lane & 3;
    const u32 m0 = jq == 0 ? 0x0000ffffu : (jq == 1 ? 0xffff0000u : 0u);
    const u32 m1 = jq == 2 ? 0x0000ffffu : (jq == 3 ? 0xffff0000u : 0u);
    LAS unsigned char* scl = L + L_SC + w * 8192 + (lane >> 2) * 16;
    u32 due = TBASE;
    lds_wait(L, F_Q, false);
    tagA = __builtin_amdgcn_readfirstlane(*(LAS u32*)(L + L_FLAG + 128));
    tagB = __builtin_amdgcn_readfirstlane(*(LAS u32*)(L + L_FLAG + 132));
    auto score = [&](v4u k, v4u q, float& mx) AI -> float {
      const f32x4 d = mfma32(k, q, f32x4{0.f, 0.f, 0.f, 0.f});
      float s = (dpp<0x00>(d[0]) + dpp<0x55>(d[1])) + (dpp<0xAA>(d[2]) + dpp<0xFF>(d[3]));
      mx = fmaxf(mx, s);
      asm volatile("" : "+v"(s), "+v"(mx));
      return s;
    };
    float mx0 = -INFINITY;
    {
      const v4u qf = *(LAS v4u*)(L + L_Q + voffK);
      const u32 now = (u32)__builtin_amdgcn_s_memrealtime() - t0;
      due += TAU; due += now > due ? (now - due) >> LEAK : 0u;
      f32x4 st, sp;
      static_for<RING>([&](auto I) AI {
        constexpr int i = decltype(I)::value;
        st[i & 3] = score(ring[i], qf, mx0);
        SB();
        WAITVM2(now, due, NFLY - 1, NWIDE - 1);
        ring[i] = __builtin_amdgcn_raw_buffer_load_b128(rk0, voffK + (i & 3) * 1024, sbase + (2 * TPI + (i >> 2)) * 16384, AUX_NT);
        SB();
        sp[i & 3] = score(pre[i], qf, mx0);
        if constexpr ((i & 3) == 3) {
          if (jq == 0) {
            *(LAS f32x4*)(scl + (i >> 2) * 256) = sp;
            *(LAS f32x4*)(scl + (TPI + (i >> 2)) * 256) = st;
          }
        }
        SB();
      });
    }
#pragma unroll 1
    for (int br = 0; br < 2; ++br) {
      const v4u qf = *(LAS v4u*)(L + L_Q + br * 1024 + voffK);
      const void* pk = p.k[br];
      const void* pv = p.v[br];
      float mx = br == 0 ? mx0 : -INFINITY;
#pragma unroll 1
      for (int it = br == 0 ? 2 : 0; it < NIT; ++it) {
        const bool last = it == NIT - 1;
        const u32 now = (u32)__builtin_amdgcn_s_memrealtime() - t0;
        due += TAU; due += now > due ? (now - due) >> LEAK : 0u;
        const __amdgpu_buffer_rsrc_t rn = __builtin_amdgcn_make_buffer_rsrc((void*)(last ? pv : pk), 0, KV_BYTES, 0x00020000);
        const int vn = last ? voffV : voffK;
        const int sn = sbase + (last ? 0 : (it + 1) * TPI * 16384);
        f32x4 st;
        static_for<RING>([&](auto I) AI {
          constexpr int i = decltype(I)::value;
          st[i & 3] = score(ring[i], qf, mx);
          if constexpr ((i & 3) == 3) { if (jq == 0) *(LAS f32x4*)(scl + (it * TPI + (i >> 2)) * 256) = st; }
          SB();
          WAITVM2(now, due, NFLY - 1, NWIDE - 1);
          ring[i] = __builtin_amdgcn_raw_buffer_load_b128(rn, vn + (i & 3) * 1024, sn + (i >> 2) * 16384, AUX_NT);
          SB();
        });
      }
      const float mc = rows_max(mx) * CSC;
      float lsum = 0.f;
      f32x4 accLo = {0.f, 0.f, 0.f, 0.f}, accHi = {0.f, 0.f, 0.f, 0.f};
#pragma unroll 1
      for (int it = 0; it < NIT; ++it) {
        const bool last = it == NIT - 1;
        const u32 now = (u32)__builtin_amdgcn_s_memrealtime() - t0;
        due += TAU; due += now > due ? (now - due) >> LEAK : 0u;
        const __amdgpu_buffer_rsrc_t rn =
            __builtin_amdgcn_make_buffer_rsrc((void*)(last ? p.k[1] : pv), 0, (last && br == 1) ? 0 : KV_BYTES, 0x00020000);
        const int vn = last ? voffK : voffV;
        const int sn = sbase + (last ? 0 : (it + 1) * TPI * 16384);
        if (last && br == 1) lds_post(L, F_LAST, w);
        f32x4 sv[TPI];
#pragma unroll
        for (int k = 0; k < TPI; ++k) sv[k] = *(LAS f32x4*)(scl + (it * TPI + k) * 256);
        static_for<RING>([&](auto I) AI {
          constexpr int i = decltype(I)::value;
          const float pe = __builtin_amdgcn_exp2f(fmaf(sv[i >> 2][i & 3], CSC, -mc));
          lsum += pe;
          asm volatile("" : "+v"(lsum));
          const u32 pkd = pack_bf16(pe, pe);
          const v2u a = {pkd & m0, pkd & m1};
          accLo = mfma16(a, v2u{ring[i].x, ring[i].y}, accLo);
          accHi = mfma16(a, v2u{ring[i].z, ring[i].w}, accHi);
          SB();
          WAITVM2(now, due, NFLY - 1, NWIDE - 1);
          ring[i] = __builtin_amdgcn_raw_buffer_load_b128(rn, vn + (i & 3) * 1024, sn + (i >> 2) * 16384, AUX_NT);
          SB();
        });
      }
      lsum = rows_sum(lsum);
      LAS float* mrg = (LAS float*)(L + L_MRG) + br * 16 * MRG_REC;
      LAS float* rec = mrg + (w * 4 + (lane >> 4)) * MRG_REC + 8 * (lane & 15);
      *(LAS f32x4*)rec = accLo;
      *(LAS f32x4*)(rec + 4) = accHi;
      if (lane < 16 && jq == 0) *(LAS f32x2*)(mrg + (w * 4 + (lane >> 2)) * MRG_REC + 128) = f32x2{mc, lsum};
      lds_post(L, F_REC + br, w);
    }
  } else {
    const int tid = w * 64 + lane;
    const v2u ep = __builtin_amdgcn_raw_buffer_load_b64(rw, 0, WS_EPOCH, AUX_SC1);
    v4u wr[2][4], xf[2];
#pragma unroll
    for (int br = 0; br < 2; ++br) {
      const __amdgpu_buffer_rsrc_t rq = __builtin_amdgcn_make_buffer_rsrc((void*)p.wq[br], 0, W_BYTES, 0x00020000);
      const __amdgpu_buffer_rsrc_t rx = __builtin_amdgcn_make_buffer_rsrc((void*)p.x[br], 0, NB * DM * 2, 0x00020000);
      const int so = (128 * qd + 32 * w) * (HQ * DH * 2) + (kvh * 512 + b * 64) * 2;
#pragma unroll
      for (int i = 0; i < 4; ++i)
        wr[br][i] = __builtin_amdgcn_raw_buffer_load_b128(rq, (lane >> 3) * (HQ * DH * 2) + (lane & 7) * 16, so + i * 8 * (HQ * DH * 2), AUX_NT);
      xf[br] = __builtin_amdgcn_raw_buffer_load_b128(rx, (lane & 7) * (DM * 2) + (lane >> 4) * 16, (128 * qd + 32 * w) * 2, 0);
      SB();
    }
    if (w == 0 && lane < 32) ((LAS u32*)(L + L_FLAG))[lane] = 0u;
    asm volatile("s_waitcnt lgkmcnt(0)" ::: "memory");
    __builtin_amdgcn_s_barrier();
    tagA = __builtin_amdgcn_readfirstlane(ep.x) + 1u;
    tagB = __builtin_amdgcn_readfirstlane(ep.y) + 0x9E3779B9u;
    if (lane == 0) *(LAS v2u*)(L + L_FLAG + 128) = v2u{tagA, tagB};
#pragma unroll
    for (int br = 0; br < 2; ++br) {
      LAS unsigned char* wl = L + L_W + br * 16384 + w * 4096;
#pragma unroll
      for (int i = 0; i < 4; ++i) *(LAS v4u*)(wl + i * 1024 + lane * 16) = wr[br][i];
      LAS unsigned char* rb = wl + (8 * (lane >> 4) + ((lane >> 2) & 3)) * 128 + (lane & 3) * 8;
      LAS f32x4* red = (LAS f32x4*)(L + L_RED + br * 8192);
#pragma unroll
      for (int ct = 0; ct < 4; ++ct) {
        const v2u lo = tr_read(rb + ct * 32), hi = tr_read(rb + ct * 32 + 512);
        const f32x4 d = mfma32(xf[br], v4u{lo.x, lo.y, hi.x, hi.y}, f32x4{0.f, 0.f, 0.f, 0.f});
        if (lane < 32) red[(w * 4 + ct) * 32 + lane] = d;
      }
    }
    lds_post(L, F_PROJ, w);
    lds_wait(L, F_PROJ, false);
    {
      const int pb = 4 * ((tid >> 3) & 1) + ((tid >> 4) & 3), pn = 16 * w + 2 * (tid & 7);
      const u32 tag8 = ((tid & 1) ? tagB : tagA) << 8;
#pragma unroll
      for (int br = 0; br < 2; ++br) {
        LAS float* rf = (LAS float*)(L + L_RED + br * 8192) + (w * 32 + 16 * ((tid >> 3) & 1) + 2 * (tid & 7)) * 4 + ((tid >> 4) & 3);
        float s0 = 0.f, s1 = 0.f;
#pragma unroll
        for (int ww = 0; ww < 4; ++ww) { s0 += rf[ww * 512]; s1 += rf[ww * 512 + 4]; }
        __builtin_amdgcn_raw_buffer_store_b64(packh(s0, s1, tag8), rw, WS_QG + (br * 8 + kvh) * 65536 + pb * 8192 + qd * 2048 + b * 256 + (pn >> 1) * 8, 0, AUX_SC1);
      }
    }
    SB();
    __builtin_amdgcn_s_sleep(Q_WAIT);
    SB();
    {
      const int qvo = WS_QG + (kvh * 8 + b) * 8192 + w * 512 + (lane >> 5) * 2048 + (lane & 31) * 16;
      const u32 ta = tagA << 8, tb = tagB << 8;
      v4u g[2][2];
      for (unsigned spins = 0; spins < SPIN_CAP; ++spins) {
        MEM_FENCE();
        u32 bad = 0u;
#pragma unroll
        for (int br = 0; br < 2; ++br)
#pragma unroll
          for (int i = 0; i < 2; ++i) g[br][i] = __builtin_amdgcn_raw_buffer_load_b128(rw, qvo, br * 8 * 65536 + i * 4096, AUX_SC1);
#pragma unroll
        for (int br = 0; br < 2; ++br)
#pragma unroll
          for (int i = 0; i < 2; ++i) bad |= (g[br][i].y ^ ta) | (g[br][i].w ^ tb);
        if (all_lanes((bad >> 8) == 0u)) break;
        __builtin_amdgcn_s_sleep(4);
      }
#pragma unroll
      for (int br = 0; br < 2; ++br) {
        const f32x4 s = unpack4(g[br][0]) + unpack4(g[br][1]);
        const float u0 = red32(s[0], s[2]), u1 = red32(s[1], s[3]);
        *(LAS u32*)(L + L_Q + br * 1024 + w * 256 + (lane & 31) * 8 + (lane >> 5) * 4) = pack_bf16(u0, u1);
      }
      lds_post(L, F_Q, w);
    }
  }

  const int br = 1 - grp;
  const u32 ta = tagA << 8, tb = tagB << 8;
  auto bad4 = [=](const v4u& g) AI -> u32 { return (g.y ^ tagA) | (g.w ^ tagB); };
  auto badp = [=](const v4u& g) AI -> u32 { return ((g.y ^ ta) | (g.w ^ tb)) >> 8; };
  int a_po = WS_PG + (br * NWG + wg) * PG_WG + w * PG_HEAD + lane * 8;
  int a_pm = lane == 0 ? WS_ML + ((br * 8 + kvh) * 4 + w) * 512 + (b * 4 + qd) * 16 : OOB;
  int a_ml = WS_ML + ((br * 8 + kvh) * 4 + (ks >> 1)) * 512 + (w * 8 + (lane >> 4)) * 16;
  int a_o = WS_PG + (br * NWG + (2 * w * 8 + kvh) * 4 + (lane >> 4)) * PG_WG + (ks >> 1) * PG_HEAD + (ks & 1) * 256 + (lane & 15) * 16;
  const bool odd = lane & 1;
  int a_og = lane < 32 ? WS_OG + br * NWG * 4096 + (((2 * (b >> 1) + (w >> 1)) * 8 + 4 * (lane >> 4) + (odd ? 2 : 0)) * 4 + 2 * (w & 1)) * 4096 +
                             (kvh * 8 + ks) * 64 + ((lane & 15) >> 1) * 8
                       : OOB;
  u32 t_po = (lane & 1) ? tb : ta, t_og = (lane & 2) ? tb : ta;
  asm volatile("" : "+v"(a_po), "+v"(a_pm), "+v"(a_ml), "+v"(a_o), "+v"(a_og), "+v"(t_po), "+v"(t_og));

  lds_wait(L, F_REC + br, grp != 0);
  {
    LAS float* mrg = (LAS float*)(L + L_MRG) + br * 16 * MRG_REC;
    f32x2 ml4[4], o4[4];
#pragma unroll
    for (int ww = 0; ww < 4; ++ww) {
      ml4[ww] = *(LAS f32x2*)(mrg + (ww * 4 + w) * MRG_REC + 128);
      o4[ww] = *(LAS f32x2*)(mrg + (ww * 4 + w) * MRG_REC + 2 * lane);
    }
    float M = fmaxf(fmaxf(ml4[0].x, ml4[1].x), fmaxf(ml4[2].x, ml4[3].x));
    float Ls = 0.f, o0 = 0.f, o1 = 0.f;
#pragma unroll
    for (int ww = 0; ww < 4; ++ww) {
      const float f = __builtin_amdgcn_exp2f(ml4[ww].x - M);
      Ls = fmaf(f, ml4[ww].y, Ls);
      o0 = fmaf(f, o4[ww].x, o0);
      o1 = fmaf(f, o4[ww].y, o1);
    }
    v2u ph = packh(o0, o1, t_po);
    v4u wo4[2][4];
    u32 tw0 = 0u, tw1 = 0u;
    if (grp == 1) {
      asm volatile("" : "+v"(ph), "+v"(M), "+v"(Ls));
      lds_wait(L, F_LAST, true);
#pragma unroll
      for (int bq = 0; bq < 2; ++bq) {
        const __amdgpu_buffer_rsrc_t ro = __builtin_amdgcn_make_buffer_rsrc((void*)p.wo[bq], 0, W_BYTES, 0x00020000);
        const int so = (kvh * 512 + 64 * ks + 16 * w) * (DM * 2) + (b >> 1) * 256;
#pragma unroll
        for (int i = 0; i < 4; ++i)
          wo4[bq][i] = __builtin_amdgcn_raw_buffer_load_b128(ro, (lane >> 4) * (DM * 2) + (lane & 15) * 16, so + i * 4 * (DM * 2), AUX_NT);
      }
      {
        const __amdgpu_buffer_rsrc_t rout = __builtin_amdgcn_make_buffer_rsrc(p.out, 0, NB * 2 * DM * 2, 0x00020000);
        tw0 = __builtin_amdgcn_raw_buffer_load_b32(rw, (w == 0 && lane < 8) ? WS_PG + lane * 524288 : OOB, 0, AUX_SC1);
        tw1 = __builtin_amdgcn_raw_buffer_load_b32(rout, (w == 0 && lane == 0) ? 0 : OOB, 0, AUX_SC1);
      }
      SB();
      lds_wait(L, F_REC + 1, true);
    }
    __builtin_amdgcn_raw_buffer_store_b64(ph, rw, a_po, 0, AUX_SC1);
    __builtin_amdgcn_raw_buffer_store_b128(v4u{f2u(M), tagA, f2u(Ls), tagB}, rw, a_pm, 0, AUX_SC1);
    if (grp == 1) {
#pragma unroll
      for (int bq = 0; bq < 2; ++bq)
#pragma unroll
        for (int i = 0; i < 4; ++i) *(LAS v4u*)(L + L_W + (2 + bq) * 16384 + w * 4096 + i * 1024 + lane * 16) = wo4[bq][i];
      lds_post(L, F_WO, w);
      asm volatile("" :: "v"(tw0), "v"(tw1));
    }
    lds_wait(L, F_WO, false);
  }
  v4u bf[2][2];
  {
    LAS unsigned char* rb = L + L_W + (2 + br) * 16384 + (8 * (lane >> 4) + ((lane >> 2) & 3)) * 256 + w * 64 + (lane & 3) * 8;
#pragma unroll
    for (int s = 0; s < 2; ++s)
#pragma unroll
      for (int c = 0; c < 2; ++c) {
        const v2u lo = tr_read(rb + s * 8192 + c * 32), hi = tr_read(rb + s * 8192 + c * 32 + 1024);
        bf[s][c] = v4u{lo.x, lo.y, hi.x, hi.y};
      }
  }
  v4u ml[2], gr[2];
  {
#pragma unroll
    for (int j = 0; j < 2; ++j) { ml[j] = v4u{0u, ~tagA, 0u, 0u}; gr[j] = v4u{0u, ~ta, 0u, 0u}; }
    bool few = false;
    if (H1_WAIT) __builtin_amdgcn_s_sleep(H1_WAIT);
    for (unsigned spins = 0; spins < SPIN_CAP; ++spins) {
      MEM_FENCE();
      v4u tm[2], t[2];
      bool wm[2], want[2];
#pragma unroll
      for (int j = 0; j < 2; ++j) {
        wm[j] = bad4(ml[j]) != 0u;
        want[j] = badp(gr[j]) != 0u && (few || !wm[j]);
        tm[j] = __builtin_amdgcn_raw_buffer_load_b128(rw, wm[j] ? a_ml + j * 64 : OOB, 0, AUX_SC1);
        t[j] = __builtin_amdgcn_raw_buffer_load_b128(rw, want[j] ? a_o + j * 32 * PG_WG : OOB, 0, AUX_SC1);
      }
      SB();
      u32 bad = 0u;
      int miss = 0;
#pragma unroll
      for (int j = 0; j < 2; ++j) {
        ml[j] = wm[j] ? tm[j] : ml[j];
        gr[j] = want[j] ? t[j] : gr[j];
        const u32 bm = bad4(ml[j]);
        miss += __builtin_popcountll(__builtin_amdgcn_ballot_w64(bm != 0u));
        bad |= bm | badp(gr[j]);
      }
      if (all_lanes(bad == 0u)) break;
      few = miss <= 16 * FEW;
      if (!few) __builtin_amdgcn_s_sleep(POLL_SLEEP);
    }
  }
  {
    LAS unsigned char* ol = L + L_O + br * 1024;
    const int r = lane >> 4;
#pragma unroll
    for (int j = 0; j < 2; ++j) {
      const float m = u2f(ml[j].x);
      const float e = __builtin_amdgcn_exp2f(m - rows_max(m));
      const float fw = e * __builtin_amdgcn_rcpf(rows_sum(e * u2f(ml[j].z)));
      const f32x4 a = unpack4(gr[j]) * fw;
      const float o = red16(red32(a[0], a[1]), red32(a[2], a[3]));
      *(LAS unsigned short*)(ol + (2 * w + j) * 128 + ((lane & 15) * 4 + ((r & 1) * 2 + (r >> 1))) * 2) = (unsigned short)(pack_bf16(o, o) & 0xffffu);
    }
    lds_post(L, F_O + br, w);
    lds_wait(L, F_O + br, false);
    const v4u af0 = *(LAS v4u*)(ol + (lane & 7) * 128 + (lane >> 4) * 16), af1 = *(LAS v4u*)(ol + (lane & 7) * 128 + 64 + (lane >> 4) * 16);
#pragma unroll
    for (int c = 0; c < 2; ++c) {
      f32x4 d = mfma32(af0, bf[0][c], f32x4{0.f, 0.f, 0.f, 0.f});
      d = mfma32(af1, bf[1][c], d);
      const float e0 = dpp<0xB1>(d[0]), e1 = dpp<0xB1>(d[1]), e2 = dpp<0xB1>(d[2]), e3 = dpp<0xB1>(d[3]);
      __builtin_amdgcn_raw_buffer_store_b64(packh(odd ? e2 : d[0], odd ? d[2] : e0, t_og), rw, a_og + c * 4096, 0, AUX_SC1);
      __builtin_amdgcn_raw_buffer_store_b64(packh(odd ? e3 : d[1], odd ? d[3] : e1, t_og), rw, a_og + c * 4096 + 4 * 4096, 0, AUX_SC1);
    }
  }
  if (w == 0) {
    v4u a[4];
#pragma unroll
    for (int i = 0; i < 4; ++i) a[i] = v4u{0u, ~ta, 0u, 0u};
    const int vo = WS_OG + (br * NWG + wg) * 4096 + lane * 16;
    __builtin_amdgcn_s_sleep(H2_WAIT);
    for (unsigned spins = 0; spins < SPIN_CAP; ++spins) {
      MEM_FENCE();
      v4u t[4];
#pragma unroll
      for (int i = 0; i < 4; ++i) t[i] = __builtin_amdgcn_raw_buffer_load_b128(rw, badp(a[i]) != 0u ? vo + i * 1024 : OOB, 0, AUX_SC1);
      SB();
      u32 bad = 0u;
#pragma unroll
      for (int i = 0; i < 4; ++i) { a[i] = badp(a[i]) != 0u ? t[i] : a[i]; bad |= badp(a[i]); }
      if (all_lanes(bad == 0u)) break;
      __builtin_amdgcn_s_sleep(H2_SLEEP);
    }
    f32x4 s = (unpack4(a[0]) + unpack4(a[1])) + (unpack4(a[2]) + unpack4(a[3]));
#pragma unroll
    for (int j = 0; j < 4; ++j) { s[j] += dpp<0x124>(s[j]); s[j] += dpp<0x128>(s[j]); }
    const float o = red16(red32(s[0], s[1]), red32(s[2], s[3]));
    const int r = lane >> 4;
    const __amdgpu_buffer_rsrc_t rout = __builtin_amdgcn_make_buffer_rsrc(p.out, 0, NB * 2 * DM * 2, 0x00020000);
    __builtin_amdgcn_raw_buffer_store_b16((unsigned short)(pack_bf16(o, o) & 0xffffu), rout,
                                          (lane & 15) < 4 ? (kvh * 1024 + br * 512 + b * 64 + qd * 16 + (lane & 3) * 4 + (r & 1) * 2 + (r >> 1)) * 2 : OOB, 0, AUX_SC1);
    if (wg == 0 && grp == 1 && lane == 0) __builtin_amdgcn_raw_buffer_store_b64(v2u{tagA, tagB}, rw, 0, WS_EPOCH, AUX_SC1);
  }
}

extern "C" void kernel_launch(void* const* d_in, const int* in_sizes, int n_in, void* d_out, int out_size, void* d_ws, size_t ws_size,
                              hipStream_t stream) {
  Params p;
  p.x[0] = d_in[0]; p.k[0] = d_in[1]; p.v[0] = d_in[2]; p.wq[0] = d_in[3]; p.wo[0] = d_in[4];
  p.x[1] = d_in[5]; p.k[1] = d_in[6]; p.v[1] = d_in[7]; p.wq[1] = d_in[8]; p.wo[1] = d_in[9];
  p.out = d_out;
  p.ws = (unsigned char*)d_ws;
  k_fused<<<NWG, NTHR, 0, stream>>>(p);
}
```

```cpp
#include <hip/hip_runtime.h>
#include <hip/hip_bf16.h>
#include <stdint.h>
#include <utility>

typedef unsigned u32;
typedef u32 v4u __attribute__((ext_vector_type(4)));
typedef u32 v2u __attribute__((ext_vector_type(2)));
typedef float f32x4 __attribute__((ext_vector_type(4)));
typedef float f32x2 __attribute__((ext_vector_type(2)));
typedef __bf16 bf16x8 __attribute__((ext_vector_type(8)));
typedef __bf16 bf16x2 __attribute__((ext_vector_type(2)));
typedef short v4s __attribute__((ext_vector_type(4)));
#define LAS __attribute__((address_space(3)))

#define NFLY 5
#define NWIDE 6
#define NFAR 8
#define FARM 100
#define NHEAD 3
#define NSLOW 12
#define TAU 217
#define TBASE 400
#define LEAK 4
#define Q_WAIT 32
#define FEW 4
#define POLL_SLEEP 4
#define H1_WAIT 6
#define H2_WAIT 16
#define H2_SLEEP 2

constexpr int NB = 8, DM = 512, HQ = 32, HKV = 8, SK = 8192, DH = 128;
constexpr int NWG = 256, NTHR = 512;
constexpr int RING = 16, TPI = RING / 4, NIT = 32 / TPI;
constexpr int KV_BYTES = NB * HKV * SK * DH * 2;
constexpr int W_BYTES = DM * HQ * DH * 2;
constexpr float CSC = 0.08838834764831845f * 1.4426950408889634f;
constexpr int AUX_NT = 2, AUX_SC1 = 16;
constexpr unsigned SPIN_CAP = 1u << 17, LSPIN_CAP = 1u << 22;
constexpr int OOB = 0x40000000;

constexpr int WS_EPOCH = 0;
constexpr int WS_QG = 4096;
constexpr int PG_HEAD = 512, PG_WG = 4 * PG_HEAD;
constexpr int WS_PG = WS_QG + 2 * 8 * 8 * 8192;
constexpr int WS_ML = WS_PG + 2 * NWG * PG_WG;
constexpr int WS_OG = WS_ML + 2 * 8 * 4 * 512;
constexpr int WS_END = WS_OG + 2 * NWG * 4096;

constexpr int MRG_REC = 132;
constexpr int L_W = 0;
constexpr int L_RED = 65536;
constexpr int L_MRG = L_RED + 16384;
constexpr int L_Q = L_MRG + 2 * 16 * MRG_REC * 4;
constexpr int L_SC = L_Q + 2048;
constexpr int L_O = L_SC + 32768;
constexpr int L_FLAG = L_O + 2048;
constexpr int L_END = L_FLAG + 128 + 16;
enum { F_Q = 0, F_LAST = 1, F_REC = 2  , F_PROJ = 4, F_WO = 5, F_O = 6   };

struct Params {
  const void* x[2];
  const void* k[2];
  const void* v[2];
  const void* wq[2];
  const void* wo[2];
  void* out;
  unsigned char* ws;
};

__device__ __forceinline__ f32x4 mfma32(v4u a, v4u b, f32x4 c) {
  return __builtin_amdgcn_mfma_f32_16x16x32_bf16(__builtin_bit_cast(bf16x8, a), __builtin_bit_cast(bf16x8, b), c, 0, 0, 0);
}
__device__ __forceinline__ f32x4 mfma16(v2u a, v2u b, f32x4 c) {
  return __builtin_amdgcn_mfma_f32_16x16x16bf16_1k(__builtin_bit_cast(v4s, a), __builtin_bit_cast(v4s, b), c, 0, 0, 0);
}
__device__ __forceinline__ u32 pack_bf16(float lo, float hi) {
  f32x2 f = {lo, hi};
  return __builtin_bit_cast(u32, __builtin_convertvector(f, bf16x2));
}
__device__ __forceinline__ v2u tr_read(LAS unsigned char* p) {
  return __builtin_bit_cast(v2u, __builtin_amdgcn_ds_read_tr16_b64_v4i16((LAS v4s*)p));
}
__device__ __forceinline__ u32 f2u(float f) { return __builtin_bit_cast(u32, f); }
__device__ __forceinline__ float u2f(u32 u) { return __builtin_bit_cast(float, u); }
template <int CTRL> __device__ __forceinline__ float dpp(float x) {
  return u2f((u32)__builtin_amdgcn_mov_dpp((int)f2u(x), CTRL, 0xf, 0xf, true));
}
__device__ __forceinline__ v2u packh(float v0, float v1, u32 tag8) {
  const u32 a = f2u(v0) + 0x800u, c = f2u(v1) + 0x800u;
  return v2u{(a >> 12) | ((c << 8) & 0xFFF00000u), (c >> 24) | tag8};
}
__device__ __forceinline__ f32x4 unpack4(v4u g) {
  return f32x4{u2f(g.x << 12), u2f(((g.x >> 20) << 12) | (g.y << 24)), u2f(g.z << 12), u2f(((g.z >> 20) << 12) | (g.w << 24))};
}
__device__ __forceinline__ float red32(float a, float b) {
  const auto s = __builtin_amdgcn_permlane32_swap(f2u(a), f2u(b), false, false);
  return u2f(s[0]) + u2f(s[1]);
}
__device__ __forceinline__ float red16(float a, float b) {
  const auto s = __builtin_amdgcn_permlane16_swap(f2u(a), f2u(b), false, false);
  return u2f(s[0]) + u2f(s[1]);
}
__device__ __forceinline__ float rows_max(float x) {
  const auto s = __builtin_amdgcn_permlane16_swap(f2u(x), f2u(x), false, false);
  x = fmaxf(u2f(s[0]), u2f(s[1]));
  const auto t = __builtin_amdgcn_permlane32_swap(f2u(x), f2u(x), false, false);
  return fmaxf(u2f(t[0]), u2f(t[1]));
}
__device__ __forceinline__ float rows_sum(float x) { x = red16(x, x); return red32(x, x); }
#define MEM_FENCE() asm volatile("" ::: "memory")
#define SB() __builtin_amdgcn_sched_barrier(0)
#define WAITVM(n) do { asm volatile("s_waitcnt vmcnt(%0)" :: "i"(n) : "memory"); SB(); } while (0)
#define WAITVM3(x, y, z, n, m, k) do { asm volatile("s_cmp_gt_u32 %0, %2\n\ts_cbranch_scc1 2f\n\ts_cmp_gt_u32 %0, %1\n\ts_cbranch_scc1 1f\n\ts_waitcnt vmcnt(%3)\n1:\n\ts_waitcnt vmcnt(%4)\n2:\n\ts_waitcnt vmcnt(%5)" :: "s"(x), "s"(y), "s"(z), "i"(n), "i"(m), "i"(k) : "memory", "scc"); SB(); } while (0)
#define AI __attribute__((always_inline))
__device__ __forceinline__ bool all_lanes(bool c) { return __builtin_amdgcn_ballot_w64(c) == ~0ull; }
template <int... I, class F> __device__ __forceinline__ void static_for_impl(std::integer_sequence<int, I...>, F&& f) {
  (f(std::integral_constant<int, I>{}), ...);
}
template <int N, class F> __device__ __forceinline__ void static_for(F&& f) { static_for_impl(std::make_integer_sequence<int, N>{}, f); }

__device__ __forceinline__ void lds_post(LAS unsigned char* L, int slot, int w) {
  MEM_FENCE();
  *(volatile LAS u32*)(L + L_FLAG + slot * 16 + w * 4) = 1u;
  MEM_FENCE();
}
__device__ __forceinline__ void lds_wait(LAS unsigned char* L, int slot, bool sleep) {
  for (unsigned spins = 0; spins < LSPIN_CAP; ++spins) {
    MEM_FENCE();
    const v4u v = *(volatile LAS v4u*)(L + L_FLAG + slot * 16);
    if (__builtin_amdgcn_readfirstlane(v.x & v.y & v.z & v.w) != 0u) break;
    if (sleep) __builtin_amdgcn_s_sleep(1);
  }
  MEM_FENCE();
}

__global__ void __launch_bounds__(NTHR, 2) k_fused(Params p) {
  asm volatile("" :: "s"(p.x[0]), "s"(p.x[1]), "s"(p.k[0]), "s"(p.k[1]), "s"(p.v[0]), "s"(p.v[1]), "s"(p.wq[0]), "s"(p.wq[1]), "s"(p.wo[0]), "s"(p.wo[1]),
               "s"(p.out), "s"(p.ws));
  __shared__ __attribute__((aligned(16))) unsigned char smem[L_END];
  LAS unsigned char* L = (LAS unsigned char*)smem;
  const int lane = threadIdx.x & 63, w8 = __builtin_amdgcn_readfirstlane(threadIdx.x >> 6), w = w8 & 3;
  const int grp = w8 >> 2;
  const int wg = blockIdx.x, qd = wg & 3, ub = wg >> 2, b = ub >> 3, kvh = ub & 7;
  const int ks = (b & 1) * 4 + qd;
  const __amdgpu_buffer_rsrc_t rw = __builtin_amdgcn_make_buffer_rsrc((void*)p.ws, 0, WS_END, 0x00020000);
  u32 tagA, tagB;

  if (grp == 0) {
    const u32 t0 = (u32)__builtin_amdgcn_s_memrealtime();
    const int voffK = ((lane >> 2) & 3) * 256 + (lane >> 4) * 64 + (lane & 3) * 16;
    const int voffV = lane * 16;
    const int sbase = wg * 524288 + w * 4096;
    v4u ring[RING], pre[RING];
    const __amdgpu_buffer_rsrc_t rk0 = __builtin_amdgcn_make_buffer_rsrc((void*)p.k[0], 0, KV_BYTES, 0x00020000);
    static_for<2 * RING>([&](auto I) AI {
      constexpr int i = decltype(I)::value;
      if constexpr (i == NHEAD) __builtin_amdgcn_s_barrier();
      if constexpr (i >= NHEAD && i < NSLOW) WAITVM(NHEAD - 1);
      if constexpr (i >= NSLOW) WAITVM(NFLY - 1);
      const v4u t = __builtin_amdgcn_raw_buffer_load_b128(rk0, voffK + (i & 3) * 1024, sbase + (i >> 2) * 16384, AUX_NT);
      if constexpr (i < RING) pre[i] = t; else ring[i - RING] = t;
      SB();
    });
    const int jq = lane & 3;
    const u32 m0 = jq == 0 ? 0x0000ffffu : (jq == 1 ? 0xffff0000u : 0u);
    const u32 m1 = jq == 2 ? 0x0000ffffu : (jq == 3 ? 0xffff0000u : 0u);
    LAS unsigned char* scl = L + L_SC + w * 8192 + (lane >> 2) * 16;
    u32 due = TBASE;
    lds_wait(L, F_Q, false);
    tagA = __builtin_amdgcn_readfirstlane(*(LAS u32*)(L + L_FLAG + 128));
    tagB = __builtin_amdgcn_readfirstlane(*(LAS u32*)(L + L_FLAG + 132));
    auto score = [&](v4u k, v4u q, float& mx) AI -> float {
      const f32x4 d = mfma32(k, q, f32x4{0.f, 0.f, 0.f, 0.f});
      float s = (dpp<0x00>(d[0]) + dpp<0x55>(d[1])) + (dpp<0xAA>(d[2]) + dpp<0xFF>(d[3]));
      mx = fmaxf(mx, s);
      asm volatile("" : "+v"(s), "+v"(mx));
      return s;
    };
    float mx0 = -INFINITY;
    {
      const v4u qf = *(LAS v4u*)(L + L_Q + voffK);
      const u32 now = (u32)__builtin_amdgcn_s_memrealtime() - t0;
      due += TAU;
      due += now > due ? (now - due) >> LEAK : 0u;
      f32x4 st, sp;
      static_for<RING>([&](auto I) AI {
        constexpr int i = decltype(I)::value;
        st[i & 3] = score(ring[i], qf, mx0);
        SB();
        WAITVM3(now, due, due + FARM, NFLY - 1, NWIDE - 1, NFAR - 1);
        ring[i] = __builtin_amdgcn_raw_buffer_load_b128(rk0, voffK + (i & 3) * 1024, sbase + (2 * TPI + (i >> 2)) * 16384, AUX_NT);
        SB();
        sp[i & 3] = score(pre[i], qf, mx0);
        if constexpr ((i & 3) == 3) {
          if (jq == 0) {
            *(LAS f32x4*)(scl + (i >> 2) * 256) = sp;
            *(LAS f32x4*)(scl + (TPI + (i >> 2)) * 256) = st;
          }
        }
        SB();
      });
    }
#pragma unroll 1
    for (int br = 0; br < 2; ++br) {
      const v4u qf = *(LAS v4u*)(L + L_Q + br * 1024 + voffK);
      const void* pk = p.k[br];
      const void* pv = p.v[br];
      float mx = br == 0 ? mx0 : -INFINITY;
#pragma unroll 1
      for (int it = br == 0 ? 2 : 0; it < NIT; ++it) {
        const bool last = it == NIT - 1;
        const u32 now = (u32)__builtin_amdgcn_s_memrealtime() - t0;
        due += TAU;
        due += now > due ? (now - due) >> LEAK : 0u;
        const __amdgpu_buffer_rsrc_t rn = __builtin_amdgcn_make_buffer_rsrc((void*)(last ? pv : pk), 0, KV_BYTES, 0x00020000);
        const int vn = last ? voffV : voffK;
        const int sn = sbase + (last ? 0 : (it + 1) * TPI * 16384);
        f32x4 st;
        static_for<RING>([&](auto I) AI {
          constexpr int i = decltype(I)::value;
          st[i & 3] = score(ring[i], qf, mx);
          if constexpr ((i & 3) == 3) { if (jq == 0) *(LAS f32x4*)(scl + (it * TPI + (i >> 2)) * 256) = st; }
          SB();
          WAITVM3(now, due, due + FARM, NFLY - 1, NWIDE - 1, NFAR - 1);
          ring[i] = __builtin_amdgcn_raw_buffer_load_b128(rn, vn + (i & 3) * 1024, sn + (i >> 2) * 16384, AUX_NT);
          SB();
        });
      }
      const float mc = rows_max(mx) * CSC;
      float lsum = 0.f;
      f32x4 accLo = {0.f, 0.f, 0.f, 0.f}, accHi = {0.f, 0.f, 0.f, 0.f};
#pragma unroll 1
      for (int it = 0; it < NIT; ++it) {
        const bool last = it == NIT - 1;
        const u32 now = (u32)__builtin_amdgcn_s_memrealtime() - t0;
        due += TAU;
        due += now > due ? (now - due) >> LEAK : 0u;
        const __amdgpu_buffer_rsrc_t rn =
            __builtin_amdgcn_make_buffer_rsrc((void*)(last ? p.k[1] : pv), 0, (last && br == 1) ? 0 : KV_BYTES, 0x00020000);
        const int vn = last ? voffK : voffV;
        const int sn = sbase + (last ? 0 : (it + 1) * TPI * 16384);
        if (last && br == 1) lds_post(L, F_LAST, w);
        f32x4 sv[TPI];
#pragma unroll
        for (int k = 0; k < TPI; ++k) sv[k] = *(LAS f32x4*)(scl + (it * TPI + k) * 256);
        static_for<RING>([&](auto I) AI {
          constexpr int i = decltype(I)::value;
          const float pe = __builtin_amdgcn_exp2f(fmaf(sv[i >> 2][i & 3], CSC, -mc));
          lsum += pe;
          asm volatile("" : "+v"(lsum));
          const u32 pkd = pack_bf16(pe, pe);
          const v2u a = {pkd & m0, pkd & m1};
          accLo = mfma16(a, v2u{ring[i].x, ring[i].y}, accLo);
          accHi = mfma16(a, v2u{ring[i].z, ring[i].w}, accHi);
          SB();
          WAITVM3(now, due, due + FARM, NFLY - 1, NWIDE - 1, NFAR - 1);
          ring[i] = __builtin_amdgcn_raw_buffer_load_b128(rn, vn + (i & 3) * 1024, sn + (i >> 2) * 16384, AUX_NT);
          SB();
        });
      }
      lsum = rows_sum(lsum);
      LAS float* mrg = (LAS float*)(L + L_MRG) + br * 16 * MRG_REC;
      LAS float* rec = mrg + (w * 4 + (lane >> 4)) * MRG_REC + 8 * (lane & 15);
      *(LAS f32x4*)rec = accLo;
      *(LAS f32x4*)(rec + 4) = accHi;
      if (lane < 16 && jq == 0) *(LAS f32x2*)(mrg + (w * 4 + (lane >> 2)) * MRG_REC + 128) = f32x2{mc, lsum};
      lds_post(L, F_REC + br, w);
    }
  } else {
    const int tid = w * 64 + lane;
    const v2u ep = __builtin_amdgcn_raw_buffer_load_b64(rw, 0, WS_EPOCH, AUX_SC1);
    v4u wr[2][4], xf[2];
#pragma unroll
    for (int br = 0; br < 2; ++br) {
      const __amdgpu_buffer_rsrc_t rq = __builtin_amdgcn_make_buffer_rsrc((void*)p.wq[br], 0, W_BYTES, 0x00020000);
      const __amdgpu_buffer_rsrc_t rx = __builtin_amdgcn_make_buffer_rsrc((void*)p.x[br], 0, NB * DM * 2, 0x00020000);
      const int so = (128 * qd + 32 * w) * (HQ * DH * 2) + (kvh * 512 + b * 64) * 2;
#pragma unroll
      for (int i = 0; i < 4; ++i)
        wr[br][i] = __builtin_amdgcn_raw_buffer_load_b128(rq, (lane >> 3) * (HQ * DH * 2) + (lane & 7) * 16, so + i * 8 * (HQ * DH * 2), AUX_NT);
      xf[br] = __builtin_amdgcn_raw_buffer_load_b128(rx, (lane & 7) * (DM * 2) + (lane >> 4) * 16, (128 * qd + 32 * w) * 2, 0);
      SB();
    }
    if (w == 0 && lane < 32) ((LAS u32*)(L + L_FLAG))[lane] = 0u;
    asm volatile("s_waitcnt lgkmcnt(0)" ::: "memory");
    __builtin_amdgcn_s_barrier();
    tagA = __builtin_amdgcn_readfirstlane(ep.x) + 1u;
    tagB = __builtin_amdgcn_readfirstlane(ep.y) + 0x9E3779B9u;
    if (lane == 0) *(LAS v2u*)(L + L_FLAG + 128) = v2u{tagA, tagB};
#pragma unroll
    for (int br = 0; br < 2; ++br) {
      LAS unsigned char* wl = L + L_W + br * 16384 + w * 4096;
#pragma unroll
      for (int i = 0; i < 4; ++i) *(LAS v4u*)(wl + i * 1024 + lane * 16) = wr[br][i];
      LAS unsigned char* rb = wl + (8 * (lane >> 4) + ((lane >> 2) & 3)) * 128 + (lane & 3) * 8;
      LAS f32x4* red = (LAS f32x4*)(L + L_RED + br * 8192);
#pragma unroll
      for (int ct = 0; ct < 4; ++ct) {
        const v2u lo = tr_read(rb + ct * 32), hi = tr_read(rb + ct * 32 + 512);
        const f32x4 d = mfma32(xf[br], v4u{lo.x, lo.y, hi.x, hi.y}, f32x4{0.f, 0.f, 0.f, 0.f});
        if (lane < 32) red[(w * 4 + ct) * 32 + lane] = d;
      }
    }
    lds_post(L, F_PROJ, w);
    lds_wait(L, F_PROJ, false);
    {
      const int pb = 4 * ((tid >> 3) & 1) + ((tid >> 4) & 3), pn = 16 * w + 2 * (tid & 7);
      const u32 tag8 = ((tid & 1) ? tagB : tagA) << 8;
#pragma unroll
      for (int br = 0; br < 2; ++br) {
        LAS float* rf = (LAS float*)(L + L_RED + br * 8192) + (w * 32 + 16 * ((tid >> 3) & 1) + 2 * (tid & 7)) * 4 + ((tid >> 4) & 3);
        float s0 = 0.f, s1 = 0.f;
#pragma unroll
        for (int ww = 0; ww < 4; ++ww) { s0 += rf[ww * 512]; s1 += rf[ww * 512 + 4]; }
        __builtin_amdgcn_raw_buffer_store_b64(packh(s0, s1, tag8), rw, WS_QG + (br * 8 + kvh) * 65536 + pb * 8192 + qd * 2048 + b * 256 + (pn >> 1) * 8, 0, AUX_SC1);
      }
    }
    SB();
    __builtin_amdgcn_s_sleep(Q_WAIT);
    SB();
    {
      const int qvo = WS_QG + (kvh * 8 + b) * 8192 + w * 512 + (lane >> 5) * 2048 + (lane & 31) * 16;
      const u32 ta = tagA << 8, tb = tagB << 8;
      v4u g[2][2];
      for (unsigned spins = 0; spins < SPIN_CAP; ++spins) {
        MEM_FENCE();
        u32 bad = 0u;
#pragma unroll
        for (int br = 0; br < 2; ++br)
#pragma unroll
          for (int i = 0; i < 2; ++i) g[br][i] = __builtin_amdgcn_raw_buffer_load_b128(rw, qvo, br * 8 * 65536 + i * 4096, AUX_SC1);
#pragma unroll
        for (int br = 0; br < 2; ++br)
#pragma unroll
          for (int i = 0; i < 2; ++i) bad |= (g[br][i].y ^ ta) | (g[br][i].w ^ tb);
        if (all_lanes((bad >> 8) == 0u)) break;
        __builtin_amdgcn_s_sleep(4);
      }
#pragma unroll
      for (int br = 0; br < 2; ++br) {
        const f32x4 s = unpack4(g[br][0]) + unpack4(g[br][1]);
        const float u0 = red32(s[0], s[2]), u1 = red32(s[1], s[3]);
        *(LAS u32*)(L + L_Q + br * 1024 + w * 256 + (lane & 31) * 8 + (lane >> 5) * 4) = pack_bf16(u0, u1);
      }
      lds_post(L, F_Q, w);
    }
  }

  const int br = 1 - grp;
  const u32 ta = tagA << 8, tb = tagB << 8;
  auto bad4 = [=](const v4u& g) AI -> u32 { return (g.y ^ tagA) | (g.w ^ tagB); };
  auto badp = [=](const v4u& g) AI -> u32 { return ((g.y ^ ta) | (g.w ^ tb)) >> 8; };
  int a_po = WS_PG + (br * NWG + wg) * PG_WG + w * PG_HEAD + lane * 8;
  int a_pm = lane == 0 ? WS_ML + ((br * 8 + kvh) * 4 + w) * 512 + (b * 4 + qd) * 16 : OOB;
  int a_ml = WS_ML + ((br * 8 + kvh) * 4 + (ks >> 1)) * 512 + (w * 8 + (lane >> 4)) * 16;
  int a_o = WS_PG + (br * NWG + (2 * w * 8 + kvh) * 4 + (lane >> 4)) * PG_WG + (ks >> 1) * PG_HEAD + (ks & 1) * 256 + (lane & 15) * 16;
  const bool odd = lane & 1;
  int a_og = lane < 32 ? WS_OG + br * NWG * 4096 + (((2 * (b >> 1) + (w >> 1)) * 8 + 4 * (lane >> 4) + (odd ? 2 : 0)) * 4 + 2 * (w & 1)) * 4096 +
                             (kvh * 8 + ks) * 64 + ((lane & 15) >> 1) * 8
                       : OOB;
  u32 t_po = (lane & 1) ? tb : ta, t_og = (lane & 2) ? tb : ta;
  asm volatile("" : "+v"(a_po), "+v"(a_pm), "+v"(a_ml), "+v"(a_o), "+v"(a_og), "+v"(t_po), "+v"(t_og));

  lds_wait(L, F_REC + br, grp != 0);
  {
    LAS float* mrg = (LAS float*)(L + L_MRG) + br * 16 * MRG_REC;
    f32x2 ml4[4], o4[4];
#pragma unroll
    for (int ww = 0; ww < 4; ++ww) {
      ml4[ww] = *(LAS f32x2*)(mrg + (ww * 4 + w) * MRG_REC + 128);
      o4[ww] = *(LAS f32x2*)(mrg + (ww * 4 + w) * MRG_REC + 2 * lane);
    }
    float M = fmaxf(fmaxf(ml4[0].x, ml4[1].x), fmaxf(ml4[2].x, ml4[3].x));
    float Ls = 0.f, o0 = 0.f, o1 = 0.f;
#pragma unroll
    for (int ww = 0; ww < 4; ++ww) {
      const float f = __builtin_amdgcn_exp2f(ml4[ww].x - M);
      Ls = fmaf(f, ml4[ww].y, Ls);
      o0 = fmaf(f, o4[ww].x, o0);
      o1 = fmaf(f, o4[ww].y, o1);
    }
    v2u ph = packh(o0, o1, t_po);
    v4u wo4[2][4];
    u32 tw0 = 0u, tw1 = 0u;
    if (grp == 1) {
      asm volatile("" : "+v"(ph), "+v"(M), "+v"(Ls));
      lds_wait(L, F_LAST, true);
#pragma unroll
      for (int bq = 0; bq < 2; ++bq) {
        const __amdgpu_buffer_rsrc_t ro = __builtin_amdgcn_make_buffer_rsrc((void*)p.wo[bq], 0, W_BYTES, 0x00020000);
        const int so = (kvh * 512 + 64 * ks + 16 * w) * (DM * 2) + (b >> 1) * 256;
#pragma unroll
        for (int i = 0; i < 4; ++i)
          wo4[bq][i] = __builtin_amdgcn_raw_buffer_load_b128(ro, (lane >> 4) * (DM * 2) + (lane & 15) * 16, so + i * 4 * (DM * 2), AUX_NT);
      }
      {
        const __amdgpu_buffer_rsrc_t rout = __builtin_amdgcn_make_buffer_rsrc(p.out, 0, NB * 2 * DM * 2, 0x00020000);
        tw0 = __builtin_amdgcn_raw_buffer_load_b32(rw, (w == 0 && lane < 8) ? WS_PG + lane * 524288 : OOB, 0, AUX_SC1);
        tw1 = __builtin_amdgcn_raw_buffer_load_b32(rout, (w == 0 && lane == 0) ? 0 : OOB, 0, AUX_SC1);
      }
      SB();
      lds_wait(L, F_REC + 1, true);
    }
    __builtin_amdgcn_raw_buffer_store_b64(ph, rw, a_po, 0, AUX_SC1);
    __builtin_amdgcn_raw_buffer_store_b128(v4u{f2u(M), tagA, f2u(Ls), tagB}, rw, a_pm, 0, AUX_SC1);
    if (grp == 1) {
#pragma unroll
      for (int bq = 0; bq < 2; ++bq)
#pragma unroll
        for (int i = 0; i < 4; ++i) *(LAS v4u*)(L + L_W + (2 + bq) * 16384 + w * 4096 + i * 1024 + lane * 16) = wo4[bq][i];
      lds_post(L, F_WO, w);
      asm volatile("" :: "v"(tw0), "v"(tw1));
    }
    lds_wait(L, F_WO, false);
  }
  v4u bf[2][2];
  {
    LAS unsigned char* rb = L + L_W + (2 + br) * 16384 + (8 * (lane >> 4) + ((lane >> 2) & 3)) * 256 + w * 64 + (lane & 3) * 8;
#pragma unroll
    for (int s = 0; s < 2; ++s)
#pragma unroll
      for (int c = 0; c < 2; ++c) {
        const v2u lo = tr_read(rb + s * 8192 + c * 32), hi = tr_read(rb + s * 8192 + c * 32 + 1024);
        bf[s][c] = v4u{lo.x, lo.y, hi.x, hi.y};
      }
  }
  v4u ml[2], gr[2];
  {
#pragma unroll
    for (int j = 0; j < 2; ++j) { ml[j] = v4u{0u, ~tagA, 0u, 0u}; gr[j] = v4u{0u, ~ta, 0u, 0u}; }
    bool few = false;
    if (H1_WAIT) __builtin_amdgcn_s_sleep(H1_WAIT);
    for (unsigned spins = 0; spins < SPIN_CAP; ++spins) {
      MEM_FENCE();
      v4u tm[2], t[2];
      bool wm[2], want[2];
#pragma unroll
      for (int j = 0; j < 2; ++j) {
        wm[j] = bad4(ml[j]) != 0u;
        want[j] = badp(gr[j]) != 0u && (few || !wm[j]);
        tm[j] = __builtin_amdgcn_raw_buffer_load_b128(rw, wm[j] ? a_ml + j * 64 : OOB, 0, AUX_SC1);
        t[j] = __builtin_amdgcn_raw_buffer_load_b128(rw, want[j] ? a_o + j * 32 * PG_WG : OOB, 0, AUX_SC1);
      }
      SB();
      u32 bad = 0u;
      int miss = 0;
#pragma unroll
      for (int j = 0; j < 2; ++j) {
        ml[j] = wm[j] ? tm[j] : ml[j];
        gr[j] = want[j] ? t[j] : gr[j];
        const u32 bm = bad4(ml[j]);
        miss += __builtin_popcountll(__builtin_amdgcn_ballot_w64(bm != 0u));
        bad |= bm | badp(gr[j]);
      }
      if (all_lanes(bad == 0u)) break;
      few = miss <= 16 * FEW;
      if (!few) __builtin_amdgcn_s_sleep(POLL_SLEEP);
    }
  }
  {
    LAS unsigned char* ol = L + L_O + br * 1024;
    const int r = lane >> 4;
#pragma unroll
    for (int j = 0; j < 2; ++j) {
      const float m = u2f(ml[j].x);
      const float e = __builtin_amdgcn_exp2f(m - rows_max(m));
      const float fw = e * __builtin_amdgcn_rcpf(rows_sum(e * u2f(ml[j].z)));
      const f32x4 a = unpack4(gr[j]) * fw;
      const float o = red16(red32(a[0], a[1]), red32(a[2], a[3]));
      *(LAS unsigned short*)(ol + (2 * w + j) * 128 + ((lane & 15) * 4 + ((r & 1) * 2 + (r >> 1))) * 2) = (unsigned short)(pack_bf16(o, o) & 0xffffu);
    }
    lds_post(L, F_O + br, w);
    lds_wait(L, F_O + br, false);
    const v4u af0 = *(LAS v4u*)(ol + (lane & 7) * 128 + (lane >> 4) * 16), af1 = *(LAS v4u*)(ol + (lane & 7) * 128 + 64 + (lane >> 4) * 16);
#pragma unroll
    for (int c = 0; c < 2; ++c) {
      f32x4 d = mfma32(af0, bf[0][c], f32x4{0.f, 0.f, 0.f, 0.f});
      d = mfma32(af1, bf[1][c], d);
      const float e0 = dpp<0xB1>(d[0]), e1 = dpp<0xB1>(d[1]), e2 = dpp<0xB1>(d[2]), e3 = dpp<0xB1>(d[3]);
      __builtin_amdgcn_raw_buffer_store_b64(packh(odd ? e2 : d[0], odd ? d[2] : e0, t_og), rw, a_og + c * 4096, 0, AUX_SC1);
      __builtin_amdgcn_raw_buffer_store_b64(packh(odd ? e3 : d[1], odd ? d[3] : e1, t_og), rw, a_og + c * 4096 + 4 * 4096, 0, AUX_SC1);
    }
  }
  if (w == 0) {
    v4u a[4];
#pragma unroll
    for (int i = 0; i < 4; ++i) a[i] = v4u{0u, ~ta, 0u, 0u};
    const int vo = WS_OG + (br * NWG + wg) * 4096 + lane * 16;
    __builtin_amdgcn_s_sleep(H2_WAIT);
    for (unsigned spins = 0; spins < SPIN_CAP; ++spins) {
      MEM_FENCE();
      v4u t[4];
#pragma unroll
      for (int i = 0; i < 4; ++i) t[i] = __builtin_amdgcn_raw_buffer_load_b128(rw, badp(a[i]) != 0u ? vo + i * 1024 : OOB, 0, AUX_SC1);
      SB();
      u32 bad = 0u;
#pragma unroll
      for (int i = 0; i < 4; ++i) { a[i] = badp(a[i]) != 0u ? t[i] : a[i]; bad |= badp(a[i]); }
      if (all_lanes(bad == 0u)) break;
      __builtin_amdgcn_s_sleep(H2_SLEEP);
    }
    f32x4 s = (unpack4(a[0]) + unpack4(a[1])) + (unpack4(a[2]) + unpack4(a[3]));
#pragma unroll
    for (int j = 0; j < 4; ++j) { s[j] += dpp<0x124>(s[j]); s[j] += dpp<0x128>(s[j]); }
    const float o = red16(red32(s[0], s[1]), red32(s[2], s[3]));
    const int r = lane >> 4;
    const __amdgpu_buffer_rsrc_t rout = __builtin_amdgcn_make_buffer_rsrc(p.out, 0, NB * 2 * DM * 2, 0x00020000);
    __builtin_amdgcn_raw_buffer_store_b16((unsigned short)(pack_bf16(o, o) & 0xffffu), rout,
                                          (lane & 15) < 4 ? (kvh * 1024 + br * 512 + b * 64 + qd * 16 + (lane & 3) * 4 + (r & 1) * 2 + (r >> 1)) * 2 : OOB, 0, AUX_SC1);
    if (wg == 0 && grp == 1 && lane == 0) __builtin_amdgcn_raw_buffer_store_b64(v2u{tagA, tagB}, rw, 0, WS_EPOCH, AUX_SC1);
  }
}

extern "C" void kernel_launch(void* const* d_in, const int* in_sizes, int n_in, void* d_out, int out_size, void* d_ws, size_t ws_size,
                              hipStream_t stream) {
  Params p;
  p.x[0] = d_in[0]; p.k[0] = d_in[1]; p.v[0] = d_in[2]; p.wq[0] = d_in[3]; p.wo[0] = d_in[4];
  p.x[1] = d_in[5]; p.k[1] = d_in[6]; p.v[1] = d_in[7]; p.wq[1] = d_in[8]; p.wo[1] = d_in[9];
  p.out = d_out;
  p.ws = (unsigned char*)d_ws;
  k_fused<<<NWG, NTHR, 0, stream>>>(p);
}
```
